```python
import torch

def _softmax(x, axis=-1):
    return torch.softmax(x, dim=axis)
def _gelu(x):
    return torch.nn.functional.gelu(x, approximate="tanh")
def _silu(x):
    return torch.nn.functional.silu(x)
def _sigmoid(x):
    return torch.sigmoid(x)
def _layernorm(x, axis=-1, eps=1e-5):
    m = x.mean(dim=axis, keepdim=True)
    v = x.var(dim=axis, keepdim=True, unbiased=False)
    return (x - m) / torch.sqrt(v + eps)
def _scatter_add(tgt, idx, src, axis=0):
    return tgt.clone().index_add_(axis, idx, src)
def _segment_sum(src, seg, n):
    out = torch.zeros((n,) + src.shape[1:], dtype=src.dtype)
    return out.index_add_(0, seg, src)
def _pad(x, pw):
    flat = tuple(v for pair in reversed(pw) for v in pair)
    return torch.nn.functional.pad(x, flat)

kernel_name = 'concat_heads__compressed_kv_cla__linear_kernel_hedgehog__prompt'

def setup_inputs():

    _w = (lambda a: (lambda t: t.to(torch.bfloat16) if t.is_floating_point() else t)(torch.as_tensor(a)))
    def _A_setup():
        B, H, Sq, Sk, D, M = 1, 32, 2048, 2048, 128, 512
        Dc = 4 * D
        return {
            "x": _w((lambda *s: torch.randn(*s, dtype=torch.bfloat16) * 0.8)(B, Sq, M)),
            "c_kv": _w((lambda *s: torch.randn(*s, dtype=torch.bfloat16) * 0.4)(B, Sk, Dc)),
            "w_q": _w((lambda *s: torch.randn(*s, dtype=torch.bfloat16) * 6.4)(M, H, D) * (M ** -0.5)),
            "w_uk": _w((lambda *s: torch.randn(*s, dtype=torch.bfloat16) * 6.4)(Dc, H, D) * (Dc ** -0.5)),
            "w_uv": _w((lambda *s: torch.randn(*s, dtype=torch.bfloat16) * 0.4)(Dc, H, D) * (Dc ** -0.5)),
            "w_o": _w((lambda *s: torch.randn(*s, dtype=torch.bfloat16) * 0.4)(H, D, M) * ((H * D) ** -0.5)),
        }
    _A_in = _A_setup()

    def _B_setup():
        B, S, D, H = 1, 2048, 512, 32
        Dh = D // H
        return {
            "x": _w((lambda *s: torch.randn(*s, dtype=torch.bfloat16) * 0.4)(B, S, D)),
            "wqkv": _w((lambda *s: torch.randn(*s, dtype=torch.bfloat16) * 0.4)(D, 3 * D)),
            "w_phi": _w((lambda *s: torch.randn(*s, dtype=torch.bfloat16) * 0.4)(H, Dh, Dh) * (Dh ** -0.5)),
            "wo": _w((lambda *s: torch.randn(*s, dtype=torch.bfloat16) * 0.05)(D, D)),
        }
    _B_in = _B_setup()

    return {
        "A_x": _A_in["x"],
        "A_c_kv": _A_in["c_kv"],
        "A_w_q": _A_in["w_q"],
        "A_w_uk": _A_in["w_uk"],
        "A_w_uv": _A_in["w_uv"],
        "A_w_o": _A_in["w_o"],
        "B_x": _B_in["x"],
        "B_wqkv": _B_in["wqkv"],
        "B_w_phi": _B_in["w_phi"],
        "B_wo": _B_in["wo"],
    }

def reference(A_x, A_c_kv, A_w_q, A_w_uk, A_w_uv, A_w_o, B_x, B_wqkv, B_w_phi, B_wo):
    def _A_ref(x, c_kv, w_q, w_uk, w_uv, w_o):
        B, Sq, M = x.shape[0], x.shape[1], x.shape[2]
        Sk = c_kv.shape[1]
        H, D = w_q.shape[1], w_q.shape[2]
        q = torch.einsum("bqm,mhd->bhqd", x, w_q)
        k = torch.einsum("bkc,chd->bhkd", c_kv, w_uk)
        v = torch.einsum("bkc,chd->bhkd", c_kv, w_uv)
        scores = torch.einsum("bhqd,bhkd->bhqk", q, k) * (D ** -0.5)
        off = Sk - Sq
        qi = torch.arange(Sq)[:, None]
        kj = torch.arange(Sk)[None, :]
        cmask = kj <= (qi + off)
        scores = torch.where(cmask[None, None], scores, -1e9)
        p = _softmax(scores, axis=-1)
        ctx = torch.einsum("bhqk,bhkd->bhqd", p, v)
        return torch.einsum("bhqd,hdm->bqm", ctx, w_o)

    def _B_ref(x, wqkv, w_phi, wo):
        B, S, D = x.shape
        H = 32; Dh = D // H
        qkv = (lambda x, *d: x.permute(*d))((x @ wqkv).reshape(B, S, 3, H, Dh), 2, 0, 3, 1, 4)
        q, k, v = qkv[0], qkv[1], qkv[2]
        pq = torch.einsum("bhsd,hdf->bhsf", q, w_phi)
        pk = torch.einsum("bhsd,hdf->bhsf", k, w_phi)
        phi_q = torch.concatenate([_softmax(pq, axis=-1), _softmax(-pq, axis=-1)], axis=-1)
        phi_k = torch.concatenate([_softmax(pk, axis=-1), _softmax(-pk, axis=-1)], axis=-1)
        kv = torch.einsum("bhsf,bhsd->bhfd", phi_k, v)
        z  = torch.einsum("bhsf->bhf", phi_k)
        num = torch.einsum("bhqf,bhfd->bhqd", phi_q, kv)
        den = torch.einsum("bhqf,bhf->bhq", phi_q, z)[..., None] + 1e-6
        o = num / den
        return (lambda x, *d: x.permute(*d))(o, 0, 2, 1, 3).reshape(B, S, D) @ wo

    a = _A_ref(A_x, A_c_kv, A_w_q, A_w_uk, A_w_uv, A_w_o)
    b = _B_ref(B_x, B_wqkv, B_w_phi, B_wo)
    return torch.concatenate([a, b], axis=-1)
```

```cpp
#include <hip/hip_runtime.h>
#include <hip/hip_bf16.h>
#include <stdint.h>
#define MIMIC 0
typedef unsigned short u16;
__device__ __forceinline__ float bf2f(u16 b){ return __uint_as_float(((unsigned)b)<<16); }
__device__ __forceinline__ u16 f2bf(float f){ unsigned u=__float_as_uint(f); u += 0x7fffu + ((u>>16)&1u); return (u16)(u>>16); }
__device__ __forceinline__ float rbf(float f){ return bf2f(f2bf(f)); }

__global__ void gemm_naive(const u16* A, int lda, const u16* B, int ldb, u16* C, int ldc, int M, int N, int K){
  __shared__ float As[16][17]; __shared__ float Bs[16][17];
  int tx=threadIdx.x, ty=threadIdx.y; int row=blockIdx.y*16+ty, col=blockIdx.x*16+tx;
  float acc=0;
  for(int k0=0;k0<K;k0+=16){
    As[ty][tx]=bf2f(A[(size_t)row*lda+k0+tx]);
    Bs[ty][tx]=bf2f(B[(size_t)(k0+ty)*ldb+col]);
    __syncthreads();
    for(int k=0;k<16;k++) acc+=As[ty][k]*Bs[k][tx];
    __syncthreads();
  }
  C[(size_t)row*ldc+col]=f2bf(acc);
}
__global__ void attn_naive(const u16* Q,const u16* Kk,const u16* V,u16* ctx){
  int q=blockIdx.x, h=blockIdx.y; int t=threadIdx.x;
  __shared__ float qs[128]; __shared__ float s[2048]; __shared__ float red[256];
  if(t<128) qs[t]=bf2f(Q[(size_t)q*4096+h*128+t]);
  __syncthreads();
  float mx=-1e30f;
  for(int j=t;j<=q;j+=256){
    float a=0; const u16* kp=Kk+(size_t)j*4096+h*128;
    for(int d=0;d<128;d++) a+=qs[d]*bf2f(kp[d]);
    a=a*0.08838834764831845f;
    s[j]=a; mx=fmaxf(mx,a);
  }
  red[t]=mx; __syncthreads();
  for(int o=128;o>0;o>>=1){ if(t<o) red[t]=fmaxf(red[t],red[t+o]); __syncthreads(); }
  mx=red[0]; __syncthreads();
  float sm=0;
  for(int j=t;j<=q;j+=256){ float e=__expf(s[j]-mx); s[j]=e; sm+=e; }
  red[t]=sm; __syncthreads();
  for(int o=128;o>0;o>>=1){ if(t<o) red[t]+=red[t+o]; __syncthreads(); }
  sm=red[0]; __syncthreads();
  float inv=1.f/sm;
  if(t<128){
    float a=0;
    for(int j=0;j<=q;j++){
      float p=rbf(s[j]);
      a+=p*bf2f(V[(size_t)j*4096+h*128+t]);
    }
    a*=inv;
    ctx[(size_t)q*4096+h*128+t]=f2bf(a);
  }
}
__global__ void phi_naive(const u16* qkv,const u16* wphi,float* phiq,float* phik){
  int s=blockIdx.x; int t=threadIdx.x;
  int h=t&31; int which=t>>5;
  const u16* x=qkv+(size_t)s*1536+which*512+h*16;
  float p[16]; float mx=-1e30f,mn=1e30f;
  for(int f=0;f<16;f++){ float a=0; for(int d=0;d<16;d++) a+=bf2f(x[d])*bf2f(wphi[h*256+d*16+f]);
    p[f]=a; mx=fmaxf(mx,a); mn=fminf(mn,a);}
  float s1=0,s2=0; float e1[16],e2[16];
  for(int f=0;f<16;f++){ e1[f]=__expf(p[f]-mx); s1+=e1[f]; e2[f]=__expf(mn-p[f]); s2+=e2[f]; }
  float* o=(which?phik:phiq)+((size_t)s*32+h)*32;
  for(int f=0;f<16;f++){
    o[f]=(e1[f]/s1); o[16+f]=(e2[f]/s2);
  }
}
__global__ void kv_naive(const u16* qkv,const float* phik,float* kv,float* z){
  int h=blockIdx.x; int t=threadIdx.x;
  int f=t/17,d=t%17; float a=0;
  for(int s=0;s<2048;s++){ float p=phik[((size_t)s*32+h)*32+f]; float v=(d<16)?bf2f(qkv[(size_t)s*1536+1024+h*16+d]):1.f; a+=p*v; }
  if(d<16) kv[(h*32+f)*16+d]=a; else z[h*32+f]=a;
}
__global__ void o_naive(const float* phiq,const float* kv,const float* z,u16* o){
  int s=blockIdx.x; int t=threadIdx.x;
  int h=t>>4,d=t&15; float num=0,den=0;
  for(int f=0;f<32;f++){ float p=phiq[((size_t)s*32+h)*32+f]; num+=p*kv[(h*32+f)*16+d]; den+=p*z[h*32+f]; }
  den+=1e-6f;
  o[(size_t)s*512+t]=f2bf(num/den);
}
extern "C" void kernel_launch(void* const* d_in, const int* in_sizes, int n_in, void* d_out, int out_size, void* d_ws, size_t ws_size, hipStream_t stream) {
  const u16* Ax=(const u16*)d_in[0]; const u16* ckv=(const u16*)d_in[1]; const u16* wq=(const u16*)d_in[2]; const u16* wuk=(const u16*)d_in[3];
  const u16* wuv=(const u16*)d_in[4]; const u16* wo=(const u16*)d_in[5]; const u16* Bx=(const u16*)d_in[6]; const u16* wqkv=(const u16*)d_in[7];
  const u16* wphi=(const u16*)d_in[8]; const u16* Bwo=(const u16*)d_in[9];
  u16* out=(u16*)d_out; char* ws=(char*)d_ws;
  u16* Q=(u16*)ws; u16* K=Q+2048*4096; u16* V=K+2048*4096; u16* ctx=V+2048*4096; u16* qkv=ctx+2048*4096; u16* o=qkv+2048*1536;
  float* phiq=(float*)(o+2048*512); float* phik=phiq+2048*1024; float* kv=phik+2048*1024; float* z=kv+32*32*16;
  dim3 b(16,16);
  gemm_naive<<<dim3(4096/16,2048/16),b,0,stream>>>(Ax,512,wq,4096,Q,4096,2048,4096,512);
  gemm_naive<<<dim3(4096/16,2048/16),b,0,stream>>>(ckv,512,wuk,4096,K,4096,2048,4096,512);
  gemm_naive<<<dim3(4096/16,2048/16),b,0,stream>>>(ckv,512,wuv,4096,V,4096,2048,4096,512);
  attn_naive<<<dim3(2048,32),256,0,stream>>>(Q,K,V,ctx);
  gemm_naive<<<dim3(512/16,2048/16),b,0,stream>>>(ctx,4096,wo,512,out,1024,2048,512,4096);
  gemm_naive<<<dim3(1536/16,2048/16),b,0,stream>>>(Bx,512,wqkv,1536,qkv,1536,2048,1536,512);
  phi_naive<<<2048,64,0,stream>>>(qkv,wphi,phiq,phik);
  kv_naive<<<32,544,0,stream>>>(qkv,phik,kv,z);
  o_naive<<<2048,512,0,stream>>>(phiq,kv,z,o);
  gemm_naive<<<dim3(512/16,2048/16),b,0,stream>>>(o,512,Bwo,512,out+512,1024,2048,512,512);
}
```

```cpp
#include <hip/hip_runtime.h>
#include <stdint.h>
typedef unsigned u32x4 __attribute__((ext_vector_type(4)));
typedef unsigned short u16;

struct FP {
  const char *x, *c, *bx, *wq, *wk, *wv, *wb, *wphi, *woa, *wob;
  char *ws, *out;
};
constexpr int F_LDS = 163840;
__global__ __launch_bounds__(256, 1) void fused_kernel(FP P) {
  extern __shared__ __attribute__((aligned(16))) char lds[];
  const int tid = threadIdx.x, lane = tid & 63, r = lane & 15, g = lane >> 4;
  const int w = __builtin_amdgcn_readfirstlane(tid >> 6);
  const int bid = blockIdx.x, xcd = bid & 7, idx = bid >> 3, h = xcd * 4 + (idx >> 3), j = idx & 7;
  const unsigned voffA = (unsigned)((8 * w + (lane >> 3)) * 1024 + (((lane & 7) ^ ((4 * (w & 1) + (lane >> 4)) & 7)) * 16));
  const int fw = (lane >> 4) | (((w >> 1) & 1) << 2);
  const unsigned voffW = (unsigned)((4 * w + (lane >> 4)) * 8192 + ((((lane & 15) >> 1) ^ fw) * 32) + (lane & 1) * 16);
  const int f2 = ((lane >> 4) & 1) | ((w & 1) << 1), lg = ((lane & 7) >> 1) ^ f2;
  const unsigned voffB = (unsigned)((8 * w + (lane >> 3)) * 3072 + (lg < 3 ? lg : 0) * 1024 + (lane & 1) * 16);
  const unsigned abase = (unsigned)(r * 128 + ((g ^ (r >> 1)) * 16));
  const unsigned wbase = (unsigned)((8 * g + (r >> 2)) * 256 + (lane & 3) * 8 + (((r >> 2) | ((g & 1) << 2)) << 5));
  const unsigned wbaseB = (unsigned)((8 * g + (r >> 2)) * 128 + (lane & 3) * 8 + ((((r >> 3) & 1) | ((g & 1) << 1)) << 5));
  const unsigned lane16 = (unsigned)lane * 16;
  const char* tp[2];
#pragma unroll
  for (int t = 0; t < 2; ++t) {
    const int c = (bid * 4 + w) * 2 + t;
    tp[t] = c < 256 ? P.bx + (size_t)c * 8192 : c < 448 ? P.wb + (size_t)(c - 256) * 8192 : c < 704 ? P.x + (size_t)(c - 448) * 8192 : c < 1216 ? P.wq + (size_t)(c - 704) * 8192
          : c < 1728 ? P.woa + (size_t)(c - 1216) * 8192 : c < 1792 ? P.wob + (size_t)(c - 1728) * 8192 : P.x;
  }
  const unsigned ldsb = (unsigned)(size_t)lds;
  char* stp = P.ws;
  asm volatile(
R"(.macro M a:vararg
v_mfma_f32_16x16x32_bf16 \a
.endm
.macro T a:vararg
ds_read_b64_tr_b16 \a
.endm
.macro R a:vararg
ds_read_b128 \a
)"
R"(.endm
.macro B a:vararg
buffer_load_dwordx4 \a
.endm
.macro AR a:vararg
v_accvgpr_read_b32 \a
.endm
.macro CV a:vararg
)"
R"(v_cvt_pk_bf16_f32 \a
.endm
.macro W a:vararg
s_waitcnt \a
.endm
.macro EX a:vararg
v_exp_f32 \a
.endm
)"
R"(.macro AD a:vararg
v_add_f32 \a
.endm
.macro MX a:vararg
v_max3_f32 \a
.endm
.macro SA a:vararg
s_add_u32 \a
)"
R"(.endm
.macro SM a:vararg
s_mov_b32 \a
.endm
.macro GS a:vararg
global_store_dwordx4 \a
.endm
.macro GL a:vararg
)"
R"(global_load_dwordx4 \a
.endm
.macro AW a:vararg
v_accvgpr_write_b32 \a
.endm
.macro MU a:vararg
v_mul_f32 \a
.endm
)"
R"(s_getpc_b64 s[90:91]
CP_%=:
s_and_b32 s92,%19,3
s_lshl_b32 s92,s92,3
SA s92,s92,%20
s_lshl_b32 s92,s92,2
SA s92,s92,%17
s_and_b32 s92,s92,31
)"
R"(s_lshl_b32 s92,s92,13
v_lshlrev_b32 v0,3,%6
v_add_u32 v0,s92,v0
SM s92,END_%=-CP_%=-4
v_min_u32 v0,s92,v0
v_and_b32 v0,-4,v0
global_load_dword v2,v0,s[90:91]
v_mov_b32 v3,%6
)"
R"(s_lshl_b32 s40,%17,10
SA s40,s40,%18
s_lshl_b32 s42,%20,18
s_mov_b64 s[48:49],%9
SA s48,s48,s42
s_addc_u32 s49,s49,0
s_and_b32 s49,s49,0xffff
SM s50,0x40000
)"
R"(SM s51,0x20000
s_mov_b64 s[52:53],%10
SA s52,s52,s42
s_addc_u32 s53,s53,0
s_and_b32 s53,s53,0xffff
SM s54,0x40000
SM s55,0x20000
s_lshl_b32 s42,%19,8
)"
R"(s_mov_b64 s[60:61],%12
SA s60,s60,s42
s_addc_u32 s61,s61,0
s_and_b32 s61,s61,0xffff
s_sub_u32 s62,0x400000,s42
SM s63,0x20000
s_mov_b64 s[64:65],%13
SA s64,s64,s42
)"
R"(s_addc_u32 s65,s65,0
s_and_b32 s65,s65,0xffff
s_sub_u32 s66,0x400000,s42
SM s67,0x20000
SA m0,s40,98304
SM s41,0
B %1,s[60:63],s41 offen lds
SA m0,s40,114688
)"
R"(SM s41,0
B %1,s[64:67],s41 offen lds
SA m0,s40,102400
SM s41,131072
B %1,s[60:63],s41 offen lds
SA m0,s40,118784
SM s41,131072
B %1,s[64:67],s41 offen lds
)"
R"(SA m0,s40,106496
SM s41,262144
B %1,s[60:63],s41 offen lds
SA m0,s40,122880
SM s41,262144
B %1,s[64:67],s41 offen lds
SA m0,s40,110592
SM s41,393216
)"
R"(B %1,s[60:63],s41 offen lds
SA m0,s40,126976
SM s41,393216
B %1,s[64:67],s41 offen lds
SA m0,s40,0
SM s41,0
B %0,s[48:51],s41 offen lds
SA m0,s40,4096
)"
R"(SM s41,32768
B %0,s[48:51],s41 offen lds
SA m0,s40,8192
SM s41,65536
B %0,s[48:51],s41 offen lds
SA m0,s40,12288
SM s41,98304
B %0,s[48:51],s41 offen lds
)"
R"(SA m0,s40,16384
SM s41,131072
B %0,s[48:51],s41 offen lds
SA m0,s40,20480
SM s41,163840
B %0,s[48:51],s41 offen lds
SA m0,s40,24576
SM s41,196608
)"
R"(B %0,s[48:51],s41 offen lds
SA m0,s40,28672
SM s41,229376
B %0,s[48:51],s41 offen lds
v_lshlrev_b32 v0,3,v3
global_load_dword v2,v0,%25
global_load_dword v2,v0,%26
s_mov_b64 s[68:69],%14
)"
R"(SA s68,s68,s42
s_addc_u32 s69,s69,0
s_and_b32 s69,s69,0xffff
s_sub_u32 s70,0x400000,s42
SM s71,0x20000
s_mov_b64 s[56:57],%11
s_and_b32 s57,s57,0xffff
SM s58,0x200000
)"
R"(SM s59,0x20000
s_lshl_b32 s42,%19,5
s_mov_b64 s[72:73],%15
SA s72,s72,s42
s_addc_u32 s73,s73,0
s_and_b32 s73,s73,0xffff
s_sub_u32 s74,0x180000,s42
SM s75,0x20000
)"
R"(s_lshl_b32 s76,%20,17
s_sub_u32 s77,15,%20
s_lshl_b32 s77,s77,17
s_mov_b64 s[90:91],%16
SA s90,s90,0x5000000
s_addc_u32 s91,s91,0
s_load_dword s88,s[90:91],0x0
W lgkmcnt(0)
)"
R"(SA s88,s88,1
s_lshl_b32 s95,%19,3
SA s95,s95,%20
s_lshl_b32 s42,%19,7
s_lshl_b32 s92,%20,4
SA s42,s42,s92
s_lshr_b32 s92,%17,1
s_lshl_b32 s92,s92,3
)"
R"(SA s92,s92,s42
s_lshl_b32 s92,s92,12
s_and_b32 s93,%17,1
s_lshl_b32 s93,s93,24
SA s92,s92,s93
s_mov_b64 s[44:45],%16
SA s44,s44,s92
s_addc_u32 s45,s45,0
)"
R"(SA s44,s44,0x1000000
s_addc_u32 s45,s45,0
s_lshl_b32 s92,%17,2
SA s92,s92,s42
s_lshl_b32 s92,s92,10
s_mov_b64 s[78:79],%16
SA s78,s78,s92
s_addc_u32 s79,s79,0
)"
R"(SA s78,s78,0x4000000
s_addc_u32 s79,s79,0
s_lshl_b32 s92,s95,12
s_lshl_b32 s93,%17,10
SA s92,s92,s93
s_mov_b64 s[80:81],%16
SA s80,s80,s92
s_addc_u32 s81,s81,0
)"
R"(SA s80,s80,0x4400000
s_addc_u32 s81,s81,0
v_add_u32 v27,%18,%3
v_add_u32 v28,%18,%4
v_add_u32 v28,98304,v28
v_add_u32 v29,%18,%5
v_add_u32 v29,98304,v29
v_mov_b32 v30,0x3fb8aa3b
)"
R"(v_mov_b32 v31,0x3e0293ee
v_xor_b32 v24,0,v29
v_xor_b32 v25,32,v29
v_xor_b32 v26,64,v29
s_lshl_b32 s42,%17,12
SA s42,s42,%18
SA s42,s42,98304
v_lshrrev_b32 v14,4,v3
)"
R"(v_and_b32 v10,15,v14
v_lshlrev_b32 v10,5,v10
v_lshrrev_b32 v15,4,v14
v_and_b32 v9,15,v14
v_lshl_add_u32 v9,v15,6,v9
v_lshlrev_b32 v9,1,v9
s_lshl_b32 s92,%19,9
v_add_u32 v9,s92,v9
)"
R"(v_lshl_add_u32 v10,v15,3,v10
v_add_u32 v10,s42,v10
v_bfe_u32 v11,v14,2,2
v_lshl_add_u32 v11,v15,2,v11
v_lshlrev_b32 v11,5,v11
v_and_b32 v14,3,v14
v_lshl_add_u32 v11,v14,3,v11
v_add_u32 v11,s42,v11
)"
R"(SA s42,s42,16384
v_add_u32 v12,s42,v3
SA s42,s40,114688
v_add_u32 v13,s42,v3
s_lshl_b32 s42,%17,13
SA s42,s42,0x10000
v_add_u32 v8,s42,v27
s_lshl_b32 s42,%17,12
)"
R"(v_add_u32 v0,s42,v3
SA s46,s42,%18
v_bfe_u32 v1,v3,4,3
v_lshlrev_b32 v1,6,v1
s_lshl_b32 s42,%19,9
SA s42,s42,4096
v_add_u32 v1,s42,v1
s_lshr_b32 s42,%17,1
)"
R"(s_lshl_b32 s42,s42,14
v_add_u32 v4,s42,v27
v_xor_b32 v5,64,v4
v_add_u32 v6,0x10000,v4
v_add_u32 v7,0x10000,v5
SA m0,s40,131072
SM s41,524288
B %1,s[60:63],s41 offen lds
)"
R"(SA m0,s40,147456
SM s41,524288
B %1,s[64:67],s41 offen lds
SA m0,s40,135168
SM s41,655360
B %1,s[60:63],s41 offen lds
SA m0,s40,151552
SM s41,655360
)"
R"(B %1,s[64:67],s41 offen lds
SA m0,s40,139264
SM s41,786432
B %1,s[60:63],s41 offen lds
SA m0,s40,155648
SM s41,786432
B %1,s[64:67],s41 offen lds
SA m0,s40,143360
)"
R"(SM s41,917504
B %1,s[60:63],s41 offen lds
SA m0,s40,159744
SM s41,917504
B %1,s[64:67],s41 offen lds
SA m0,s40,32768
SM s41,128
B %0,s[48:51],s41 offen lds
)"
R"(SA m0,s40,36864
SM s41,32896
B %0,s[48:51],s41 offen lds
SA m0,s40,40960
SM s41,65664
B %0,s[48:51],s41 offen lds
SA m0,s40,45056
SM s41,98432
)"
R"(B %0,s[48:51],s41 offen lds
SA m0,s40,49152
SM s41,131200
B %0,s[48:51],s41 offen lds
SA m0,s40,53248
SM s41,163968
B %0,s[48:51],s41 offen lds
SA m0,s40,57344
)"
R"(SM s41,196736
B %0,s[48:51],s41 offen lds
SA m0,s40,61440
SM s41,229504
B %0,s[48:51],s41 offen lds
s_getpc_b64 s[90:91]
JT_%=:
SM s92,KV0_%=-JT_%=
)"
R"(s_and_b32 s93,%17,1
s_cmp_eq_u32 s93,1
s_cselect_b32 s92,KV1_%=-JT_%=,s92
SA s90,s90,s92
s_addc_u32 s91,s91,0
s_setpc_b64 s[90:91]
KV0_%=:
v_xor_b32 v16,0,v28
)"
R"(v_xor_b32 v17,32,v28
v_xor_b32 v18,64,v28
v_xor_b32 v19,96,v28
v_xor_b32 v20,128,v28
v_xor_b32 v21,160,v28
v_xor_b32 v22,192,v28
v_xor_b32 v23,224,v28
W vmcnt(18) lgkmcnt(0)
)"
R"(s_barrier
T v[64:65],v16 offset:0
T v[66:67],v16 offset:1024
T v[68:69],v17 offset:0
T v[70:71],v17 offset:1024
T v[72:73],v18 offset:0
T v[74:75],v18 offset:1024
T v[76:77],v19 offset:0
)"
R"(T v[78:79],v19 offset:1024
T v[80:81],v20 offset:0
T v[82:83],v20 offset:1024
T v[84:85],v21 offset:0
T v[86:87],v21 offset:1024
T v[88:89],v22 offset:0
T v[90:91],v22 offset:1024
T v[92:93],v23 offset:0
)"
R"(T v[94:95],v23 offset:1024
R v[32:35],v4 offset:0
R v[36:39],v4 offset:2048
R v[40:43],v4 offset:4096
R v[44:47],v4 offset:6144
R v[48:51],v4 offset:8192
R v[52:55],v4 offset:10240
R v[56:59],v4 offset:12288
)"
R"(R v[60:63],v4 offset:14336
global_load_ushort v208,v9,%7 offset:0
global_load_ushort v209,v9,%7 offset:32
global_load_ushort v210,v9,%7 offset:64
global_load_ushort v211,v9,%7 offset:96
W lgkmcnt(7)
M a[0:3],v[64:67],v[32:35],0
SA m0,s40,65536
)"
R"(M a[4:7],v[68:71],v[32:35],0
T v[96:97],v16 offset:8192
M a[8:11],v[72:75],v[32:35],0
SM s41,256
M a[12:15],v[76:79],v[32:35],0
M a[16:19],v[80:83],v[32:35],0
T v[98:99],v16 offset:9216
B %0,s[48:51],s41 offen lds
)"
R"(M a[20:23],v[84:87],v[32:35],0
M a[24:27],v[88:91],v[32:35],0
M a[28:31],v[92:95],v[32:35],0
R v[32:35],v5 offset:0
T v[100:101],v17 offset:8192
W lgkmcnt(10)
M a[32:35],v[64:67],v[36:39],0
SA m0,s40,69632
)"
R"(M a[36:39],v[68:71],v[36:39],0
SM s41,33024
M a[40:43],v[72:75],v[36:39],0
T v[102:103],v17 offset:9216
M a[44:47],v[76:79],v[36:39],0
M a[48:51],v[80:83],v[36:39],0
B %0,s[48:51],s41 offen lds
M a[52:55],v[84:87],v[36:39],0
)"
R"(T v[104:105],v18 offset:8192
M a[56:59],v[88:91],v[36:39],0
SA m0,s40,73728
M a[60:63],v[92:95],v[36:39],0
R v[36:39],v5 offset:2048
W lgkmcnt(12)
M a[64:67],v[64:67],v[40:43],0
T v[106:107],v18 offset:9216
)"
R"(SM s41,65792
M a[68:71],v[68:71],v[40:43],0
M a[72:75],v[72:75],v[40:43],0
M a[76:79],v[76:79],v[40:43],0
T v[108:109],v19 offset:8192
B %0,s[48:51],s41 offen lds
M a[80:83],v[80:83],v[40:43],0
M a[84:87],v[84:87],v[40:43],0
)"
R"(SA m0,s40,77824
M a[88:91],v[88:91],v[40:43],0
T v[110:111],v19 offset:9216
M a[92:95],v[92:95],v[40:43],0
R v[40:43],v5 offset:4096
W lgkmcnt(15)
M a[96:99],v[64:67],v[44:47],0
SM s41,98560
)"
R"(M a[100:103],v[68:71],v[44:47],0
T v[112:113],v20 offset:8192
M a[104:107],v[72:75],v[44:47],0
B %0,s[48:51],s41 offen lds
M a[108:111],v[76:79],v[44:47],0
M a[112:115],v[80:83],v[44:47],0
T v[114:115],v20 offset:9216
SA m0,s40,81920
)"
R"(M a[116:119],v[84:87],v[44:47],0
M a[120:123],v[88:91],v[44:47],0
M a[124:127],v[92:95],v[44:47],0
R v[44:47],v5 offset:6144
T v[116:117],v21 offset:8192
W lgkmcnt(15)
M a[128:131],v[64:67],v[48:51],0
SM s41,131328
)"
R"(M a[132:135],v[68:71],v[48:51],0
B %0,s[48:51],s41 offen lds
M a[136:139],v[72:75],v[48:51],0
T v[118:119],v21 offset:9216
M a[140:143],v[76:79],v[48:51],0
M a[144:147],v[80:83],v[48:51],0
SA m0,s40,86016
M a[148:151],v[84:87],v[48:51],0
)"
R"(T v[120:121],v22 offset:8192
M a[152:155],v[88:91],v[48:51],0
SM s41,164096
M a[156:159],v[92:95],v[48:51],0
R v[48:51],v5 offset:8192
M a[160:163],v[64:67],v[52:55],0
T v[122:123],v22 offset:9216
B %0,s[48:51],s41 offen lds
)"
R"(M a[164:167],v[68:71],v[52:55],0
M a[168:171],v[72:75],v[52:55],0
M a[172:175],v[76:79],v[52:55],0
T v[124:125],v23 offset:8192
SA m0,s40,90112
M a[176:179],v[80:83],v[52:55],0
M a[180:183],v[84:87],v[52:55],0
SM s41,196864
)"
R"(M a[184:187],v[88:91],v[52:55],0
T v[126:127],v23 offset:9216
M a[188:191],v[92:95],v[52:55],0
R v[52:55],v5 offset:10240
M a[192:195],v[64:67],v[56:59],0
B %0,s[48:51],s41 offen lds
M a[196:199],v[68:71],v[56:59],0
M a[200:203],v[72:75],v[56:59],0
)"
R"(SA m0,s40,94208
M a[204:207],v[76:79],v[56:59],0
M a[208:211],v[80:83],v[56:59],0
SM s41,229632
M a[212:215],v[84:87],v[56:59],0
M a[216:219],v[88:91],v[56:59],0
M a[220:223],v[92:95],v[56:59],0
R v[56:59],v5 offset:12288
)"
R"(B %0,s[48:51],s41 offen lds
M a[224:227],v[64:67],v[60:63],0
M a[228:231],v[68:71],v[60:63],0
M a[232:235],v[72:75],v[60:63],0
M a[236:239],v[76:79],v[60:63],0
M a[240:243],v[80:83],v[60:63],0
M a[244:247],v[84:87],v[60:63],0
M a[248:251],v[88:91],v[60:63],0
)"
R"(M a[252:255],v[92:95],v[60:63],0
R v[60:63],v5 offset:14336
W vmcnt(12) lgkmcnt(0)
s_barrier
M a[0:3],v[96:99],v[32:35],a[0:3]
SA m0,s40,98304
M a[4:7],v[100:103],v[32:35],a[4:7]
T v[64:65],v16 offset:32768
)"
R"(M a[8:11],v[104:107],v[32:35],a[8:11]
SM s41,1048576
M a[12:15],v[108:111],v[32:35],a[12:15]
M a[16:19],v[112:115],v[32:35],a[16:19]
T v[66:67],v16 offset:33792
M a[20:23],v[116:119],v[32:35],a[20:23]
B %1,s[60:63],s41 offen lds
M a[24:27],v[120:123],v[32:35],a[24:27]
)"
R"(M a[28:31],v[124:127],v[32:35],a[28:31]
R v[32:35],v4 offset:32768
T v[68:69],v17 offset:32768
M a[32:35],v[96:99],v[36:39],a[32:35]
SA m0,s40,114688
M a[36:39],v[100:103],v[36:39],a[36:39]
M a[40:43],v[104:107],v[36:39],a[40:43]
T v[70:71],v17 offset:33792
)"
R"(SM s41,1048576
M a[44:47],v[108:111],v[36:39],a[44:47]
M a[48:51],v[112:115],v[36:39],a[48:51]
M a[52:55],v[116:119],v[36:39],a[52:55]
T v[72:73],v18 offset:32768
B %1,s[64:67],s41 offen lds
M a[56:59],v[120:123],v[36:39],a[56:59]
M a[60:63],v[124:127],v[36:39],a[60:63]
)"
R"(R v[36:39],v4 offset:34816
M a[64:67],v[96:99],v[40:43],a[64:67]
T v[74:75],v18 offset:33792
SA m0,s40,102400
M a[68:71],v[100:103],v[40:43],a[68:71]
M a[72:75],v[104:107],v[40:43],a[72:75]
SM s41,1179648
M a[76:79],v[108:111],v[40:43],a[76:79]
)"
R"(T v[76:77],v19 offset:32768
M a[80:83],v[112:115],v[40:43],a[80:83]
M a[84:87],v[116:119],v[40:43],a[84:87]
B %1,s[60:63],s41 offen lds
M a[88:91],v[120:123],v[40:43],a[88:91]
T v[78:79],v19 offset:33792
M a[92:95],v[124:127],v[40:43],a[92:95]
R v[40:43],v4 offset:36864
)"
R"(M a[96:99],v[96:99],v[44:47],a[96:99]
SA m0,s40,118784
M a[100:103],v[100:103],v[44:47],a[100:103]
T v[80:81],v20 offset:32768
M a[104:107],v[104:107],v[44:47],a[104:107]
SM s41,1179648
M a[108:111],v[108:111],v[44:47],a[108:111]
M a[112:115],v[112:115],v[44:47],a[112:115]
)"
R"(T v[82:83],v20 offset:33792
M a[116:119],v[116:119],v[44:47],a[116:119]
B %1,s[64:67],s41 offen lds
M a[120:123],v[120:123],v[44:47],a[120:123]
M a[124:127],v[124:127],v[44:47],a[124:127]
R v[44:47],v4 offset:38912
T v[84:85],v21 offset:32768
M a[128:131],v[96:99],v[48:51],a[128:131]
)"
R"(SA m0,s40,106496
M a[132:135],v[100:103],v[48:51],a[132:135]
M a[136:139],v[104:107],v[48:51],a[136:139]
T v[86:87],v21 offset:33792
SM s41,1310720
M a[140:143],v[108:111],v[48:51],a[140:143]
M a[144:147],v[112:115],v[48:51],a[144:147]
M a[148:151],v[116:119],v[48:51],a[148:151]
)"
R"(T v[88:89],v22 offset:32768
B %1,s[60:63],s41 offen lds
M a[152:155],v[120:123],v[48:51],a[152:155]
M a[156:159],v[124:127],v[48:51],a[156:159]
R v[48:51],v4 offset:40960
M a[160:163],v[96:99],v[52:55],a[160:163]
T v[90:91],v22 offset:33792
SA m0,s40,122880
)"
R"(M a[164:167],v[100:103],v[52:55],a[164:167]
M a[168:171],v[104:107],v[52:55],a[168:171]
SM s41,1310720
M a[172:175],v[108:111],v[52:55],a[172:175]
T v[92:93],v23 offset:32768
M a[176:179],v[112:115],v[52:55],a[176:179]
M a[180:183],v[116:119],v[52:55],a[180:183]
B %1,s[64:67],s41 offen lds
)"
R"(M a[184:187],v[120:123],v[52:55],a[184:187]
T v[94:95],v23 offset:33792
M a[188:191],v[124:127],v[52:55],a[188:191]
R v[52:55],v4 offset:43008
M a[192:195],v[96:99],v[56:59],a[192:195]
SA m0,s40,110592
M a[196:199],v[100:103],v[56:59],a[196:199]
M a[200:203],v[104:107],v[56:59],a[200:203]
)"
R"(SM s41,1441792
M a[204:207],v[108:111],v[56:59],a[204:207]
M a[208:211],v[112:115],v[56:59],a[208:211]
M a[212:215],v[116:119],v[56:59],a[212:215]
B %1,s[60:63],s41 offen lds
M a[216:219],v[120:123],v[56:59],a[216:219]
M a[220:223],v[124:127],v[56:59],a[220:223]
R v[56:59],v4 offset:45056
)"
R"(M a[224:227],v[96:99],v[60:63],a[224:227]
SA m0,s40,126976
M a[228:231],v[100:103],v[60:63],a[228:231]
M a[232:235],v[104:107],v[60:63],a[232:235]
SM s41,1441792
M a[236:239],v[108:111],v[60:63],a[236:239]
M a[240:243],v[112:115],v[60:63],a[240:243]
M a[244:247],v[116:119],v[60:63],a[244:247]
)"
R"(B %1,s[64:67],s41 offen lds
M a[248:251],v[120:123],v[60:63],a[248:251]
M a[252:255],v[124:127],v[60:63],a[252:255]
R v[60:63],v4 offset:47104
W lgkmcnt(14)
M a[0:3],v[64:67],v[32:35],a[0:3]
SA m0,s40,0
M a[4:7],v[68:71],v[32:35],a[4:7]
)"
R"(T v[96:97],v16 offset:40960
M a[8:11],v[72:75],v[32:35],a[8:11]
SM s41,384
M a[12:15],v[76:79],v[32:35],a[12:15]
W lgkmcnt(4)
M a[16:19],v[80:83],v[32:35],a[16:19]
T v[98:99],v16 offset:41984
B %0,s[48:51],s41 offen lds
)"
R"(M a[20:23],v[84:87],v[32:35],a[20:23]
M a[24:27],v[88:91],v[32:35],a[24:27]
M a[28:31],v[92:95],v[32:35],a[28:31]
R v[32:35],v5 offset:32768
T v[100:101],v17 offset:40960
M a[32:35],v[64:67],v[36:39],a[32:35]
SA m0,s40,4096
M a[36:39],v[68:71],v[36:39],a[36:39]
)"
R"(SM s41,33152
M a[40:43],v[72:75],v[36:39],a[40:43]
T v[102:103],v17 offset:41984
M a[44:47],v[76:79],v[36:39],a[44:47]
M a[48:51],v[80:83],v[36:39],a[48:51]
B %0,s[48:51],s41 offen lds
M a[52:55],v[84:87],v[36:39],a[52:55]
T v[104:105],v18 offset:40960
)"
R"(M a[56:59],v[88:91],v[36:39],a[56:59]
SA m0,s40,8192
M a[60:63],v[92:95],v[36:39],a[60:63]
R v[36:39],v5 offset:34816
M a[64:67],v[64:67],v[40:43],a[64:67]
T v[106:107],v18 offset:41984
SM s41,65920
M a[68:71],v[68:71],v[40:43],a[68:71]
)"
R"(M a[72:75],v[72:75],v[40:43],a[72:75]
M a[76:79],v[76:79],v[40:43],a[76:79]
T v[108:109],v19 offset:40960
B %0,s[48:51],s41 offen lds
M a[80:83],v[80:83],v[40:43],a[80:83]
M a[84:87],v[84:87],v[40:43],a[84:87]
SA m0,s40,12288
M a[88:91],v[88:91],v[40:43],a[88:91]
)"
R"(T v[110:111],v19 offset:41984
M a[92:95],v[92:95],v[40:43],a[92:95]
R v[40:43],v5 offset:36864
M a[96:99],v[64:67],v[44:47],a[96:99]
SM s41,98688
M a[100:103],v[68:71],v[44:47],a[100:103]
T v[112:113],v20 offset:40960
M a[104:107],v[72:75],v[44:47],a[104:107]
)"
R"(B %0,s[48:51],s41 offen lds
M a[108:111],v[76:79],v[44:47],a[108:111]
M a[112:115],v[80:83],v[44:47],a[112:115]
T v[114:115],v20 offset:41984
SA m0,s40,16384
M a[116:119],v[84:87],v[44:47],a[116:119]
M a[120:123],v[88:91],v[44:47],a[120:123]
M a[124:127],v[92:95],v[44:47],a[124:127]
)"
R"(R v[44:47],v5 offset:38912
T v[116:117],v21 offset:40960
M a[128:131],v[64:67],v[48:51],a[128:131]
SM s41,131456
M a[132:135],v[68:71],v[48:51],a[132:135]
B %0,s[48:51],s41 offen lds
M a[136:139],v[72:75],v[48:51],a[136:139]
T v[118:119],v21 offset:41984
)"
R"(M a[140:143],v[76:79],v[48:51],a[140:143]
M a[144:147],v[80:83],v[48:51],a[144:147]
SA m0,s40,20480
M a[148:151],v[84:87],v[48:51],a[148:151]
T v[120:121],v22 offset:40960
M a[152:155],v[88:91],v[48:51],a[152:155]
SM s41,164224
M a[156:159],v[92:95],v[48:51],a[156:159]
)"
R"(R v[48:51],v5 offset:40960
W lgkmcnt(15)
M a[160:163],v[64:67],v[52:55],a[160:163]
T v[122:123],v22 offset:41984
B %0,s[48:51],s41 offen lds
M a[164:167],v[68:71],v[52:55],a[164:167]
M a[168:171],v[72:75],v[52:55],a[168:171]
M a[172:175],v[76:79],v[52:55],a[172:175]
)"
R"(T v[124:125],v23 offset:40960
SA m0,s40,24576
M a[176:179],v[80:83],v[52:55],a[176:179]
M a[180:183],v[84:87],v[52:55],a[180:183]
SM s41,196992
M a[184:187],v[88:91],v[52:55],a[184:187]
T v[126:127],v23 offset:41984
M a[188:191],v[92:95],v[52:55],a[188:191]
)"
R"(R v[52:55],v5 offset:43008
M a[192:195],v[64:67],v[56:59],a[192:195]
B %0,s[48:51],s41 offen lds
M a[196:199],v[68:71],v[56:59],a[196:199]
M a[200:203],v[72:75],v[56:59],a[200:203]
SA m0,s40,28672
M a[204:207],v[76:79],v[56:59],a[204:207]
M a[208:211],v[80:83],v[56:59],a[208:211]
)"
R"(SM s41,229760
M a[212:215],v[84:87],v[56:59],a[212:215]
M a[216:219],v[88:91],v[56:59],a[216:219]
M a[220:223],v[92:95],v[56:59],a[220:223]
R v[56:59],v5 offset:45056
B %0,s[48:51],s41 offen lds
M a[224:227],v[64:67],v[60:63],a[224:227]
M a[228:231],v[68:71],v[60:63],a[228:231]
)"
R"(M a[232:235],v[72:75],v[60:63],a[232:235]
M a[236:239],v[76:79],v[60:63],a[236:239]
M a[240:243],v[80:83],v[60:63],a[240:243]
M a[244:247],v[84:87],v[60:63],a[244:247]
M a[248:251],v[88:91],v[60:63],a[248:251]
M a[252:255],v[92:95],v[60:63],a[252:255]
R v[60:63],v5 offset:47104
W vmcnt(8) lgkmcnt(0)
)"
R"(s_barrier
M a[0:3],v[96:99],v[32:35],a[0:3]
SA m0,s40,131072
M a[4:7],v[100:103],v[32:35],a[4:7]
T v[64:65],v16 offset:0
M a[8:11],v[104:107],v[32:35],a[8:11]
SM s41,1572864
M a[12:15],v[108:111],v[32:35],a[12:15]
)"
R"(M a[16:19],v[112:115],v[32:35],a[16:19]
T v[66:67],v16 offset:1024
M a[20:23],v[116:119],v[32:35],a[20:23]
B %1,s[60:63],s41 offen lds
M a[24:27],v[120:123],v[32:35],a[24:27]
M a[28:31],v[124:127],v[32:35],a[28:31]
R v[32:35],v6 offset:0
T v[68:69],v17 offset:0
)"
R"(M a[32:35],v[96:99],v[36:39],a[32:35]
SA m0,s40,147456
M a[36:39],v[100:103],v[36:39],a[36:39]
M a[40:43],v[104:107],v[36:39],a[40:43]
T v[70:71],v17 offset:1024
SM s41,1572864
M a[44:47],v[108:111],v[36:39],a[44:47]
M a[48:51],v[112:115],v[36:39],a[48:51]
)"
R"(M a[52:55],v[116:119],v[36:39],a[52:55]
T v[72:73],v18 offset:0
B %1,s[64:67],s41 offen lds
M a[56:59],v[120:123],v[36:39],a[56:59]
M a[60:63],v[124:127],v[36:39],a[60:63]
R v[36:39],v6 offset:2048
M a[64:67],v[96:99],v[40:43],a[64:67]
T v[74:75],v18 offset:1024
)"
R"(SA m0,s40,135168
M a[68:71],v[100:103],v[40:43],a[68:71]
M a[72:75],v[104:107],v[40:43],a[72:75]
SM s41,1703936
M a[76:79],v[108:111],v[40:43],a[76:79]
T v[76:77],v19 offset:0
M a[80:83],v[112:115],v[40:43],a[80:83]
M a[84:87],v[116:119],v[40:43],a[84:87]
)"
R"(B %1,s[60:63],s41 offen lds
M a[88:91],v[120:123],v[40:43],a[88:91]
T v[78:79],v19 offset:1024
M a[92:95],v[124:127],v[40:43],a[92:95]
R v[40:43],v6 offset:4096
M a[96:99],v[96:99],v[44:47],a[96:99]
SA m0,s40,151552
M a[100:103],v[100:103],v[44:47],a[100:103]
)"
R"(T v[80:81],v20 offset:0
M a[104:107],v[104:107],v[44:47],a[104:107]
SM s41,1703936
M a[108:111],v[108:111],v[44:47],a[108:111]
M a[112:115],v[112:115],v[44:47],a[112:115]
T v[82:83],v20 offset:1024
M a[116:119],v[116:119],v[44:47],a[116:119]
B %1,s[64:67],s41 offen lds
)"
R"(M a[120:123],v[120:123],v[44:47],a[120:123]
M a[124:127],v[124:127],v[44:47],a[124:127]
R v[44:47],v6 offset:6144
T v[84:85],v21 offset:0
M a[128:131],v[96:99],v[48:51],a[128:131]
SA m0,s40,139264
M a[132:135],v[100:103],v[48:51],a[132:135]
M a[136:139],v[104:107],v[48:51],a[136:139]
)"
R"(T v[86:87],v21 offset:1024
SM s41,1835008
M a[140:143],v[108:111],v[48:51],a[140:143]
M a[144:147],v[112:115],v[48:51],a[144:147]
M a[148:151],v[116:119],v[48:51],a[148:151]
T v[88:89],v22 offset:0
B %1,s[60:63],s41 offen lds
M a[152:155],v[120:123],v[48:51],a[152:155]
)"
R"(M a[156:159],v[124:127],v[48:51],a[156:159]
R v[48:51],v6 offset:8192
M a[160:163],v[96:99],v[52:55],a[160:163]
T v[90:91],v22 offset:1024
SA m0,s40,155648
M a[164:167],v[100:103],v[52:55],a[164:167]
M a[168:171],v[104:107],v[52:55],a[168:171]
SM s41,1835008
)"
R"(M a[172:175],v[108:111],v[52:55],a[172:175]
T v[92:93],v23 offset:0
M a[176:179],v[112:115],v[52:55],a[176:179]
M a[180:183],v[116:119],v[52:55],a[180:183]
B %1,s[64:67],s41 offen lds
M a[184:187],v[120:123],v[52:55],a[184:187]
T v[94:95],v23 offset:1024
M a[188:191],v[124:127],v[52:55],a[188:191]
)"
R"(R v[52:55],v6 offset:10240
M a[192:195],v[96:99],v[56:59],a[192:195]
SA m0,s40,143360
M a[196:199],v[100:103],v[56:59],a[196:199]
M a[200:203],v[104:107],v[56:59],a[200:203]
SM s41,1966080
M a[204:207],v[108:111],v[56:59],a[204:207]
M a[208:211],v[112:115],v[56:59],a[208:211]
)"
R"(M a[212:215],v[116:119],v[56:59],a[212:215]
B %1,s[60:63],s41 offen lds
M a[216:219],v[120:123],v[56:59],a[216:219]
M a[220:223],v[124:127],v[56:59],a[220:223]
R v[56:59],v6 offset:12288
M a[224:227],v[96:99],v[60:63],a[224:227]
SA m0,s40,159744
M a[228:231],v[100:103],v[60:63],a[228:231]
)"
R"(M a[232:235],v[104:107],v[60:63],a[232:235]
SM s41,1966080
M a[236:239],v[108:111],v[60:63],a[236:239]
M a[240:243],v[112:115],v[60:63],a[240:243]
M a[244:247],v[116:119],v[60:63],a[244:247]
B %1,s[64:67],s41 offen lds
M a[248:251],v[120:123],v[60:63],a[248:251]
M a[252:255],v[124:127],v[60:63],a[252:255]
)"
R"(R v[60:63],v6 offset:14336
W lgkmcnt(14)
M a[0:3],v[64:67],v[32:35],a[0:3]
SA m0,s40,32768
M a[4:7],v[68:71],v[32:35],a[4:7]
T v[96:97],v16 offset:8192
M a[8:11],v[72:75],v[32:35],a[8:11]
SM s41,512
)"
R"(M a[12:15],v[76:79],v[32:35],a[12:15]
W lgkmcnt(4)
M a[16:19],v[80:83],v[32:35],a[16:19]
T v[98:99],v16 offset:9216
B %0,s[48:51],s41 offen lds
M a[20:23],v[84:87],v[32:35],a[20:23]
M a[24:27],v[88:91],v[32:35],a[24:27]
M a[28:31],v[92:95],v[32:35],a[28:31]
)"
R"(R v[32:35],v7 offset:0
T v[100:101],v17 offset:8192
M a[32:35],v[64:67],v[36:39],a[32:35]
SA m0,s40,36864
M a[36:39],v[68:71],v[36:39],a[36:39]
SM s41,33280
M a[40:43],v[72:75],v[36:39],a[40:43]
T v[102:103],v17 offset:9216
)"
R"(M a[44:47],v[76:79],v[36:39],a[44:47]
M a[48:51],v[80:83],v[36:39],a[48:51]
B %0,s[48:51],s41 offen lds
M a[52:55],v[84:87],v[36:39],a[52:55]
T v[104:105],v18 offset:8192
M a[56:59],v[88:91],v[36:39],a[56:59]
SA m0,s40,40960
M a[60:63],v[92:95],v[36:39],a[60:63]
)"
R"(R v[36:39],v7 offset:2048
M a[64:67],v[64:67],v[40:43],a[64:67]
T v[106:107],v18 offset:9216
SM s41,66048
M a[68:71],v[68:71],v[40:43],a[68:71]
M a[72:75],v[72:75],v[40:43],a[72:75]
M a[76:79],v[76:79],v[40:43],a[76:79]
T v[108:109],v19 offset:8192
)"
R"(B %0,s[48:51],s41 offen lds
M a[80:83],v[80:83],v[40:43],a[80:83]
M a[84:87],v[84:87],v[40:43],a[84:87]
SA m0,s40,45056
M a[88:91],v[88:91],v[40:43],a[88:91]
T v[110:111],v19 offset:9216
M a[92:95],v[92:95],v[40:43],a[92:95]
R v[40:43],v7 offset:4096
)"
R"(M a[96:99],v[64:67],v[44:47],a[96:99]
SM s41,98816
M a[100:103],v[68:71],v[44:47],a[100:103]
T v[112:113],v20 offset:8192
M a[104:107],v[72:75],v[44:47],a[104:107]
B %0,s[48:51],s41 offen lds
M a[108:111],v[76:79],v[44:47],a[108:111]
M a[112:115],v[80:83],v[44:47],a[112:115]
)"
R"(T v[114:115],v20 offset:9216
SA m0,s40,49152
M a[116:119],v[84:87],v[44:47],a[116:119]
M a[120:123],v[88:91],v[44:47],a[120:123]
M a[124:127],v[92:95],v[44:47],a[124:127]
R v[44:47],v7 offset:6144
T v[116:117],v21 offset:8192
M a[128:131],v[64:67],v[48:51],a[128:131]
)"
R"(SM s41,131584
M a[132:135],v[68:71],v[48:51],a[132:135]
B %0,s[48:51],s41 offen lds
M a[136:139],v[72:75],v[48:51],a[136:139]
T v[118:119],v21 offset:9216
M a[140:143],v[76:79],v[48:51],a[140:143]
M a[144:147],v[80:83],v[48:51],a[144:147]
SA m0,s40,53248
)"
R"(M a[148:151],v[84:87],v[48:51],a[148:151]
T v[120:121],v22 offset:8192
M a[152:155],v[88:91],v[48:51],a[152:155]
SM s41,164352
M a[156:159],v[92:95],v[48:51],a[156:159]
R v[48:51],v7 offset:8192
W lgkmcnt(15)
M a[160:163],v[64:67],v[52:55],a[160:163]
)"
R"(T v[122:123],v22 offset:9216
B %0,s[48:51],s41 offen lds
M a[164:167],v[68:71],v[52:55],a[164:167]
M a[168:171],v[72:75],v[52:55],a[168:171]
M a[172:175],v[76:79],v[52:55],a[172:175]
T v[124:125],v23 offset:8192
SA m0,s40,57344
M a[176:179],v[80:83],v[52:55],a[176:179]
)"
R"(M a[180:183],v[84:87],v[52:55],a[180:183]
SM s41,197120
M a[184:187],v[88:91],v[52:55],a[184:187]
T v[126:127],v23 offset:9216
M a[188:191],v[92:95],v[52:55],a[188:191]
R v[52:55],v7 offset:10240
M a[192:195],v[64:67],v[56:59],a[192:195]
B %0,s[48:51],s41 offen lds
)"
R"(M a[196:199],v[68:71],v[56:59],a[196:199]
M a[200:203],v[72:75],v[56:59],a[200:203]
SA m0,s40,61440
M a[204:207],v[76:79],v[56:59],a[204:207]
M a[208:211],v[80:83],v[56:59],a[208:211]
SM s41,229888
M a[212:215],v[84:87],v[56:59],a[212:215]
M a[216:219],v[88:91],v[56:59],a[216:219]
)"
R"(M a[220:223],v[92:95],v[56:59],a[220:223]
R v[56:59],v7 offset:12288
B %0,s[48:51],s41 offen lds
M a[224:227],v[64:67],v[60:63],a[224:227]
M a[228:231],v[68:71],v[60:63],a[228:231]
M a[232:235],v[72:75],v[60:63],a[232:235]
M a[236:239],v[76:79],v[60:63],a[236:239]
M a[240:243],v[80:83],v[60:63],a[240:243]
)"
R"(M a[244:247],v[84:87],v[60:63],a[244:247]
M a[248:251],v[88:91],v[60:63],a[248:251]
M a[252:255],v[92:95],v[60:63],a[252:255]
R v[60:63],v7 offset:14336
W vmcnt(8) lgkmcnt(0)
s_barrier
M a[0:3],v[96:99],v[32:35],a[0:3]
SA m0,s40,98304
)"
R"(M a[4:7],v[100:103],v[32:35],a[4:7]
T v[64:65],v16 offset:32768
M a[8:11],v[104:107],v[32:35],a[8:11]
SM s41,2097152
M a[12:15],v[108:111],v[32:35],a[12:15]
M a[16:19],v[112:115],v[32:35],a[16:19]
T v[66:67],v16 offset:33792
M a[20:23],v[116:119],v[32:35],a[20:23]
)"
R"(B %1,s[60:63],s41 offen lds
M a[24:27],v[120:123],v[32:35],a[24:27]
M a[28:31],v[124:127],v[32:35],a[28:31]
R v[32:35],v4 offset:0
T v[68:69],v17 offset:32768
M a[32:35],v[96:99],v[36:39],a[32:35]
SA m0,s40,114688
M a[36:39],v[100:103],v[36:39],a[36:39]
)"
R"(M a[40:43],v[104:107],v[36:39],a[40:43]
T v[70:71],v17 offset:33792
SM s41,2097152
M a[44:47],v[108:111],v[36:39],a[44:47]
M a[48:51],v[112:115],v[36:39],a[48:51]
M a[52:55],v[116:119],v[36:39],a[52:55]
T v[72:73],v18 offset:32768
B %1,s[64:67],s41 offen lds
)"
R"(M a[56:59],v[120:123],v[36:39],a[56:59]
M a[60:63],v[124:127],v[36:39],a[60:63]
R v[36:39],v4 offset:2048
M a[64:67],v[96:99],v[40:43],a[64:67]
T v[74:75],v18 offset:33792
SA m0,s40,102400
M a[68:71],v[100:103],v[40:43],a[68:71]
M a[72:75],v[104:107],v[40:43],a[72:75]
)"
R"(SM s41,2228224
M a[76:79],v[108:111],v[40:43],a[76:79]
T v[76:77],v19 offset:32768
M a[80:83],v[112:115],v[40:43],a[80:83]
M a[84:87],v[116:119],v[40:43],a[84:87]
B %1,s[60:63],s41 offen lds
M a[88:91],v[120:123],v[40:43],a[88:91]
T v[78:79],v19 offset:33792
)"
R"(M a[92:95],v[124:127],v[40:43],a[92:95]
R v[40:43],v4 offset:4096
M a[96:99],v[96:99],v[44:47],a[96:99]
SA m0,s40,118784
M a[100:103],v[100:103],v[44:47],a[100:103]
T v[80:81],v20 offset:32768
M a[104:107],v[104:107],v[44:47],a[104:107]
SM s41,2228224
)"
R"(M a[108:111],v[108:111],v[44:47],a[108:111]
M a[112:115],v[112:115],v[44:47],a[112:115]
T v[82:83],v20 offset:33792
M a[116:119],v[116:119],v[44:47],a[116:119]
B %1,s[64:67],s41 offen lds
M a[120:123],v[120:123],v[44:47],a[120:123]
M a[124:127],v[124:127],v[44:47],a[124:127]
R v[44:47],v4 offset:6144
)"
R"(T v[84:85],v21 offset:32768
M a[128:131],v[96:99],v[48:51],a[128:131]
SA m0,s40,106496
M a[132:135],v[100:103],v[48:51],a[132:135]
M a[136:139],v[104:107],v[48:51],a[136:139]
T v[86:87],v21 offset:33792
SM s41,2359296
M a[140:143],v[108:111],v[48:51],a[140:143]
)"
R"(M a[144:147],v[112:115],v[48:51],a[144:147]
M a[148:151],v[116:119],v[48:51],a[148:151]
T v[88:89],v22 offset:32768
B %1,s[60:63],s41 offen lds
M a[152:155],v[120:123],v[48:51],a[152:155]
M a[156:159],v[124:127],v[48:51],a[156:159]
R v[48:51],v4 offset:8192
M a[160:163],v[96:99],v[52:55],a[160:163]
)"
R"(T v[90:91],v22 offset:33792
SA m0,s40,122880
M a[164:167],v[100:103],v[52:55],a[164:167]
M a[168:171],v[104:107],v[52:55],a[168:171]
SM s41,2359296
M a[172:175],v[108:111],v[52:55],a[172:175]
T v[92:93],v23 offset:32768
M a[176:179],v[112:115],v[52:55],a[176:179]
)"
R"(M a[180:183],v[116:119],v[52:55],a[180:183]
B %1,s[64:67],s41 offen lds
M a[184:187],v[120:123],v[52:55],a[184:187]
T v[94:95],v23 offset:33792
M a[188:191],v[124:127],v[52:55],a[188:191]
R v[52:55],v4 offset:10240
M a[192:195],v[96:99],v[56:59],a[192:195]
SA m0,s40,110592
)"
R"(M a[196:199],v[100:103],v[56:59],a[196:199]
M a[200:203],v[104:107],v[56:59],a[200:203]
SM s41,2490368
M a[204:207],v[108:111],v[56:59],a[204:207]
M a[208:211],v[112:115],v[56:59],a[208:211]
M a[212:215],v[116:119],v[56:59],a[212:215]
B %1,s[60:63],s41 offen lds
M a[216:219],v[120:123],v[56:59],a[216:219]
)"
R"(M a[220:223],v[124:127],v[56:59],a[220:223]
R v[56:59],v4 offset:12288
M a[224:227],v[96:99],v[60:63],a[224:227]
SA m0,s40,126976
M a[228:231],v[100:103],v[60:63],a[228:231]
M a[232:235],v[104:107],v[60:63],a[232:235]
SM s41,2490368
M a[236:239],v[108:111],v[60:63],a[236:239]
)"
R"(M a[240:243],v[112:115],v[60:63],a[240:243]
M a[244:247],v[116:119],v[60:63],a[244:247]
B %1,s[64:67],s41 offen lds
M a[248:251],v[120:123],v[60:63],a[248:251]
M a[252:255],v[124:127],v[60:63],a[252:255]
R v[60:63],v4 offset:14336
W lgkmcnt(14)
M a[0:3],v[64:67],v[32:35],a[0:3]
)"
R"(SA m0,s40,65536
M a[4:7],v[68:71],v[32:35],a[4:7]
T v[96:97],v16 offset:40960
M a[8:11],v[72:75],v[32:35],a[8:11]
SM s41,640
M a[12:15],v[76:79],v[32:35],a[12:15]
W lgkmcnt(4)
M a[16:19],v[80:83],v[32:35],a[16:19]
)"
R"(T v[98:99],v16 offset:41984
B %0,s[48:51],s41 offen lds
M a[20:23],v[84:87],v[32:35],a[20:23]
M a[24:27],v[88:91],v[32:35],a[24:27]
M a[28:31],v[92:95],v[32:35],a[28:31]
R v[32:35],v5 offset:0
T v[100:101],v17 offset:40960
M a[32:35],v[64:67],v[36:39],a[32:35]
)"
R"(SA m0,s40,69632
M a[36:39],v[68:71],v[36:39],a[36:39]
SM s41,33408
M a[40:43],v[72:75],v[36:39],a[40:43]
T v[102:103],v17 offset:41984
M a[44:47],v[76:79],v[36:39],a[44:47]
M a[48:51],v[80:83],v[36:39],a[48:51]
B %0,s[48:51],s41 offen lds
)"
R"(M a[52:55],v[84:87],v[36:39],a[52:55]
T v[104:105],v18 offset:40960
M a[56:59],v[88:91],v[36:39],a[56:59]
SA m0,s40,73728
M a[60:63],v[92:95],v[36:39],a[60:63]
R v[36:39],v5 offset:2048
M a[64:67],v[64:67],v[40:43],a[64:67]
T v[106:107],v18 offset:41984
)"
R"(SM s41,66176
M a[68:71],v[68:71],v[40:43],a[68:71]
M a[72:75],v[72:75],v[40:43],a[72:75]
M a[76:79],v[76:79],v[40:43],a[76:79]
T v[108:109],v19 offset:40960
B %0,s[48:51],s41 offen lds
M a[80:83],v[80:83],v[40:43],a[80:83]
M a[84:87],v[84:87],v[40:43],a[84:87]
)"
R"(SA m0,s40,77824
M a[88:91],v[88:91],v[40:43],a[88:91]
T v[110:111],v19 offset:41984
M a[92:95],v[92:95],v[40:43],a[92:95]
R v[40:43],v5 offset:4096
M a[96:99],v[64:67],v[44:47],a[96:99]
SM s41,98944
M a[100:103],v[68:71],v[44:47],a[100:103]
)"
R"(T v[112:113],v20 offset:40960
M a[104:107],v[72:75],v[44:47],a[104:107]
B %0,s[48:51],s41 offen lds
M a[108:111],v[76:79],v[44:47],a[108:111]
M a[112:115],v[80:83],v[44:47],a[112:115]
T v[114:115],v20 offset:41984
SA m0,s40,81920
M a[116:119],v[84:87],v[44:47],a[116:119]
)"
R"(M a[120:123],v[88:91],v[44:47],a[120:123]
M a[124:127],v[92:95],v[44:47],a[124:127]
R v[44:47],v5 offset:6144
T v[116:117],v21 offset:40960
M a[128:131],v[64:67],v[48:51],a[128:131]
SM s41,131712
M a[132:135],v[68:71],v[48:51],a[132:135]
B %0,s[48:51],s41 offen lds
)"
R"(M a[136:139],v[72:75],v[48:51],a[136:139]
T v[118:119],v21 offset:41984
M a[140:143],v[76:79],v[48:51],a[140:143]
M a[144:147],v[80:83],v[48:51],a[144:147]
SA m0,s40,86016
M a[148:151],v[84:87],v[48:51],a[148:151]
T v[120:121],v22 offset:40960
M a[152:155],v[88:91],v[48:51],a[152:155]
)"
R"(SM s41,164480
M a[156:159],v[92:95],v[48:51],a[156:159]
R v[48:51],v5 offset:8192
W lgkmcnt(15)
M a[160:163],v[64:67],v[52:55],a[160:163]
T v[122:123],v22 offset:41984
B %0,s[48:51],s41 offen lds
M a[164:167],v[68:71],v[52:55],a[164:167]
)"
R"(M a[168:171],v[72:75],v[52:55],a[168:171]
M a[172:175],v[76:79],v[52:55],a[172:175]
T v[124:125],v23 offset:40960
SA m0,s40,90112
M a[176:179],v[80:83],v[52:55],a[176:179]
M a[180:183],v[84:87],v[52:55],a[180:183]
SM s41,197248
M a[184:187],v[88:91],v[52:55],a[184:187]
)"
R"(T v[126:127],v23 offset:41984
M a[188:191],v[92:95],v[52:55],a[188:191]
R v[52:55],v5 offset:10240
M a[192:195],v[64:67],v[56:59],a[192:195]
B %0,s[48:51],s41 offen lds
M a[196:199],v[68:71],v[56:59],a[196:199]
M a[200:203],v[72:75],v[56:59],a[200:203]
SA m0,s40,94208
)"
R"(M a[204:207],v[76:79],v[56:59],a[204:207]
M a[208:211],v[80:83],v[56:59],a[208:211]
SM s41,230016
M a[212:215],v[84:87],v[56:59],a[212:215]
M a[216:219],v[88:91],v[56:59],a[216:219]
M a[220:223],v[92:95],v[56:59],a[220:223]
R v[56:59],v5 offset:12288
B %0,s[48:51],s41 offen lds
)"
R"(M a[224:227],v[64:67],v[60:63],a[224:227]
M a[228:231],v[68:71],v[60:63],a[228:231]
M a[232:235],v[72:75],v[60:63],a[232:235]
M a[236:239],v[76:79],v[60:63],a[236:239]
M a[240:243],v[80:83],v[60:63],a[240:243]
M a[244:247],v[84:87],v[60:63],a[244:247]
M a[248:251],v[88:91],v[60:63],a[248:251]
M a[252:255],v[92:95],v[60:63],a[252:255]
)"
R"(R v[60:63],v5 offset:14336
W vmcnt(8) lgkmcnt(0)
s_barrier
M a[0:3],v[96:99],v[32:35],a[0:3]
SA m0,s40,131072
M a[4:7],v[100:103],v[32:35],a[4:7]
T v[64:65],v16 offset:0
M a[8:11],v[104:107],v[32:35],a[8:11]
)"
R"(SM s41,2621440
M a[12:15],v[108:111],v[32:35],a[12:15]
M a[16:19],v[112:115],v[32:35],a[16:19]
T v[66:67],v16 offset:1024
M a[20:23],v[116:119],v[32:35],a[20:23]
B %1,s[60:63],s41 offen lds
M a[24:27],v[120:123],v[32:35],a[24:27]
M a[28:31],v[124:127],v[32:35],a[28:31]
)"
R"(R v[32:35],v4 offset:32768
T v[68:69],v17 offset:0
M a[32:35],v[96:99],v[36:39],a[32:35]
SA m0,s40,147456
M a[36:39],v[100:103],v[36:39],a[36:39]
M a[40:43],v[104:107],v[36:39],a[40:43]
T v[70:71],v17 offset:1024
SM s41,2621440
)"
R"(M a[44:47],v[108:111],v[36:39],a[44:47]
M a[48:51],v[112:115],v[36:39],a[48:51]
M a[52:55],v[116:119],v[36:39],a[52:55]
T v[72:73],v18 offset:0
B %1,s[64:67],s41 offen lds
M a[56:59],v[120:123],v[36:39],a[56:59]
M a[60:63],v[124:127],v[36:39],a[60:63]
R v[36:39],v4 offset:34816
)"
R"(M a[64:67],v[96:99],v[40:43],a[64:67]
T v[74:75],v18 offset:1024
SA m0,s40,135168
M a[68:71],v[100:103],v[40:43],a[68:71]
M a[72:75],v[104:107],v[40:43],a[72:75]
SM s41,2752512
M a[76:79],v[108:111],v[40:43],a[76:79]
T v[76:77],v19 offset:0
)"
R"(M a[80:83],v[112:115],v[40:43],a[80:83]
M a[84:87],v[116:119],v[40:43],a[84:87]
B %1,s[60:63],s41 offen lds
M a[88:91],v[120:123],v[40:43],a[88:91]
T v[78:79],v19 offset:1024
M a[92:95],v[124:127],v[40:43],a[92:95]
R v[40:43],v4 offset:36864
M a[96:99],v[96:99],v[44:47],a[96:99]
)"
R"(SA m0,s40,151552
M a[100:103],v[100:103],v[44:47],a[100:103]
T v[80:81],v20 offset:0
M a[104:107],v[104:107],v[44:47],a[104:107]
SM s41,2752512
M a[108:111],v[108:111],v[44:47],a[108:111]
M a[112:115],v[112:115],v[44:47],a[112:115]
T v[82:83],v20 offset:1024
)"
R"(M a[116:119],v[116:119],v[44:47],a[116:119]
B %1,s[64:67],s41 offen lds
M a[120:123],v[120:123],v[44:47],a[120:123]
M a[124:127],v[124:127],v[44:47],a[124:127]
R v[44:47],v4 offset:38912
T v[84:85],v21 offset:0
M a[128:131],v[96:99],v[48:51],a[128:131]
SA m0,s40,139264
)"
R"(M a[132:135],v[100:103],v[48:51],a[132:135]
M a[136:139],v[104:107],v[48:51],a[136:139]
T v[86:87],v21 offset:1024
SM s41,2883584
M a[140:143],v[108:111],v[48:51],a[140:143]
M a[144:147],v[112:115],v[48:51],a[144:147]
M a[148:151],v[116:119],v[48:51],a[148:151]
T v[88:89],v22 offset:0
)"
R"(B %1,s[60:63],s41 offen lds
M a[152:155],v[120:123],v[48:51],a[152:155]
M a[156:159],v[124:127],v[48:51],a[156:159]
R v[48:51],v4 offset:40960
M a[160:163],v[96:99],v[52:55],a[160:163]
T v[90:91],v22 offset:1024
SA m0,s40,155648
M a[164:167],v[100:103],v[52:55],a[164:167]
)"
R"(M a[168:171],v[104:107],v[52:55],a[168:171]
SM s41,2883584
M a[172:175],v[108:111],v[52:55],a[172:175]
T v[92:93],v23 offset:0
M a[176:179],v[112:115],v[52:55],a[176:179]
M a[180:183],v[116:119],v[52:55],a[180:183]
B %1,s[64:67],s41 offen lds
M a[184:187],v[120:123],v[52:55],a[184:187]
)"
R"(T v[94:95],v23 offset:1024
M a[188:191],v[124:127],v[52:55],a[188:191]
R v[52:55],v4 offset:43008
M a[192:195],v[96:99],v[56:59],a[192:195]
SA m0,s40,143360
M a[196:199],v[100:103],v[56:59],a[196:199]
M a[200:203],v[104:107],v[56:59],a[200:203]
SM s41,3014656
)"
R"(M a[204:207],v[108:111],v[56:59],a[204:207]
M a[208:211],v[112:115],v[56:59],a[208:211]
M a[212:215],v[116:119],v[56:59],a[212:215]
B %1,s[60:63],s41 offen lds
M a[216:219],v[120:123],v[56:59],a[216:219]
M a[220:223],v[124:127],v[56:59],a[220:223]
R v[56:59],v4 offset:45056
M a[224:227],v[96:99],v[60:63],a[224:227]
)"
R"(SA m0,s40,159744
M a[228:231],v[100:103],v[60:63],a[228:231]
M a[232:235],v[104:107],v[60:63],a[232:235]
SM s41,3014656
M a[236:239],v[108:111],v[60:63],a[236:239]
M a[240:243],v[112:115],v[60:63],a[240:243]
M a[244:247],v[116:119],v[60:63],a[244:247]
B %1,s[64:67],s41 offen lds
)"
R"(M a[248:251],v[120:123],v[60:63],a[248:251]
M a[252:255],v[124:127],v[60:63],a[252:255]
R v[60:63],v4 offset:47104
W lgkmcnt(14)
M a[0:3],v[64:67],v[32:35],a[0:3]
SA m0,s40,0
M a[4:7],v[68:71],v[32:35],a[4:7]
T v[96:97],v16 offset:8192
)"
R"(M a[8:11],v[72:75],v[32:35],a[8:11]
SM s41,768
M a[12:15],v[76:79],v[32:35],a[12:15]
W lgkmcnt(4)
M a[16:19],v[80:83],v[32:35],a[16:19]
T v[98:99],v16 offset:9216
B %0,s[48:51],s41 offen lds
M a[20:23],v[84:87],v[32:35],a[20:23]
)"
R"(M a[24:27],v[88:91],v[32:35],a[24:27]
M a[28:31],v[92:95],v[32:35],a[28:31]
R v[32:35],v5 offset:32768
T v[100:101],v17 offset:8192
M a[32:35],v[64:67],v[36:39],a[32:35]
SA m0,s40,4096
M a[36:39],v[68:71],v[36:39],a[36:39]
SM s41,33536
)"
R"(M a[40:43],v[72:75],v[36:39],a[40:43]
T v[102:103],v17 offset:9216
M a[44:47],v[76:79],v[36:39],a[44:47]
M a[48:51],v[80:83],v[36:39],a[48:51]
B %0,s[48:51],s41 offen lds
M a[52:55],v[84:87],v[36:39],a[52:55]
T v[104:105],v18 offset:8192
M a[56:59],v[88:91],v[36:39],a[56:59]
)"
R"(SA m0,s40,8192
M a[60:63],v[92:95],v[36:39],a[60:63]
R v[36:39],v5 offset:34816
M a[64:67],v[64:67],v[40:43],a[64:67]
T v[106:107],v18 offset:9216
SM s41,66304
M a[68:71],v[68:71],v[40:43],a[68:71]
M a[72:75],v[72:75],v[40:43],a[72:75]
)"
R"(M a[76:79],v[76:79],v[40:43],a[76:79]
T v[108:109],v19 offset:8192
B %0,s[48:51],s41 offen lds
M a[80:83],v[80:83],v[40:43],a[80:83]
M a[84:87],v[84:87],v[40:43],a[84:87]
SA m0,s40,12288
M a[88:91],v[88:91],v[40:43],a[88:91]
T v[110:111],v19 offset:9216
)"
R"(M a[92:95],v[92:95],v[40:43],a[92:95]
R v[40:43],v5 offset:36864
M a[96:99],v[64:67],v[44:47],a[96:99]
SM s41,99072
M a[100:103],v[68:71],v[44:47],a[100:103]
T v[112:113],v20 offset:8192
M a[104:107],v[72:75],v[44:47],a[104:107]
B %0,s[48:51],s41 offen lds
)"
R"(M a[108:111],v[76:79],v[44:47],a[108:111]
M a[112:115],v[80:83],v[44:47],a[112:115]
T v[114:115],v20 offset:9216
SA m0,s40,16384
M a[116:119],v[84:87],v[44:47],a[116:119]
M a[120:123],v[88:91],v[44:47],a[120:123]
M a[124:127],v[92:95],v[44:47],a[124:127]
R v[44:47],v5 offset:38912
)"
R"(T v[116:117],v21 offset:8192
M a[128:131],v[64:67],v[48:51],a[128:131]
SM s41,131840
M a[132:135],v[68:71],v[48:51],a[132:135]
B %0,s[48:51],s41 offen lds
M a[136:139],v[72:75],v[48:51],a[136:139]
T v[118:119],v21 offset:9216
M a[140:143],v[76:79],v[48:51],a[140:143]
)"
R"(M a[144:147],v[80:83],v[48:51],a[144:147]
SA m0,s40,20480
M a[148:151],v[84:87],v[48:51],a[148:151]
T v[120:121],v22 offset:8192
M a[152:155],v[88:91],v[48:51],a[152:155]
SM s41,164608
M a[156:159],v[92:95],v[48:51],a[156:159]
R v[48:51],v5 offset:40960
)"
R"(W lgkmcnt(15)
M a[160:163],v[64:67],v[52:55],a[160:163]
T v[122:123],v22 offset:9216
B %0,s[48:51],s41 offen lds
M a[164:167],v[68:71],v[52:55],a[164:167]
M a[168:171],v[72:75],v[52:55],a[168:171]
M a[172:175],v[76:79],v[52:55],a[172:175]
T v[124:125],v23 offset:8192
)"
R"(SA m0,s40,24576
M a[176:179],v[80:83],v[52:55],a[176:179]
M a[180:183],v[84:87],v[52:55],a[180:183]
SM s41,197376
M a[184:187],v[88:91],v[52:55],a[184:187]
T v[126:127],v23 offset:9216
M a[188:191],v[92:95],v[52:55],a[188:191]
R v[52:55],v5 offset:43008
)"
R"(M a[192:195],v[64:67],v[56:59],a[192:195]
B %0,s[48:51],s41 offen lds
M a[196:199],v[68:71],v[56:59],a[196:199]
M a[200:203],v[72:75],v[56:59],a[200:203]
SA m0,s40,28672
M a[204:207],v[76:79],v[56:59],a[204:207]
M a[208:211],v[80:83],v[56:59],a[208:211]
SM s41,230144
)"
R"(M a[212:215],v[84:87],v[56:59],a[212:215]
M a[216:219],v[88:91],v[56:59],a[216:219]
M a[220:223],v[92:95],v[56:59],a[220:223]
R v[56:59],v5 offset:45056
B %0,s[48:51],s41 offen lds
M a[224:227],v[64:67],v[60:63],a[224:227]
M a[228:231],v[68:71],v[60:63],a[228:231]
M a[232:235],v[72:75],v[60:63],a[232:235]
)"
R"(M a[236:239],v[76:79],v[60:63],a[236:239]
M a[240:243],v[80:83],v[60:63],a[240:243]
M a[244:247],v[84:87],v[60:63],a[244:247]
M a[248:251],v[88:91],v[60:63],a[248:251]
M a[252:255],v[92:95],v[60:63],a[252:255]
R v[60:63],v5 offset:47104
W vmcnt(8) lgkmcnt(0)
s_barrier
)"
R"(M a[0:3],v[96:99],v[32:35],a[0:3]
SA m0,s40,98304
M a[4:7],v[100:103],v[32:35],a[4:7]
T v[64:65],v16 offset:32768
M a[8:11],v[104:107],v[32:35],a[8:11]
SM s41,3145728
M a[12:15],v[108:111],v[32:35],a[12:15]
M a[16:19],v[112:115],v[32:35],a[16:19]
)"
R"(T v[66:67],v16 offset:33792
M a[20:23],v[116:119],v[32:35],a[20:23]
B %1,s[60:63],s41 offen lds
M a[24:27],v[120:123],v[32:35],a[24:27]
M a[28:31],v[124:127],v[32:35],a[28:31]
R v[32:35],v6 offset:0
T v[68:69],v17 offset:32768
M a[32:35],v[96:99],v[36:39],a[32:35]
)"
R"(SA m0,s40,114688
M a[36:39],v[100:103],v[36:39],a[36:39]
M a[40:43],v[104:107],v[36:39],a[40:43]
T v[70:71],v17 offset:33792
SM s41,3145728
M a[44:47],v[108:111],v[36:39],a[44:47]
M a[48:51],v[112:115],v[36:39],a[48:51]
M a[52:55],v[116:119],v[36:39],a[52:55]
)"
R"(T v[72:73],v18 offset:32768
B %1,s[64:67],s41 offen lds
M a[56:59],v[120:123],v[36:39],a[56:59]
M a[60:63],v[124:127],v[36:39],a[60:63]
R v[36:39],v6 offset:2048
M a[64:67],v[96:99],v[40:43],a[64:67]
T v[74:75],v18 offset:33792
SA m0,s40,102400
)"
R"(M a[68:71],v[100:103],v[40:43],a[68:71]
M a[72:75],v[104:107],v[40:43],a[72:75]
SM s41,3276800
M a[76:79],v[108:111],v[40:43],a[76:79]
T v[76:77],v19 offset:32768
M a[80:83],v[112:115],v[40:43],a[80:83]
M a[84:87],v[116:119],v[40:43],a[84:87]
B %1,s[60:63],s41 offen lds
)"
R"(M a[88:91],v[120:123],v[40:43],a[88:91]
T v[78:79],v19 offset:33792
M a[92:95],v[124:127],v[40:43],a[92:95]
R v[40:43],v6 offset:4096
M a[96:99],v[96:99],v[44:47],a[96:99]
SA m0,s40,118784
M a[100:103],v[100:103],v[44:47],a[100:103]
T v[80:81],v20 offset:32768
)"
R"(M a[104:107],v[104:107],v[44:47],a[104:107]
SM s41,3276800
M a[108:111],v[108:111],v[44:47],a[108:111]
M a[112:115],v[112:115],v[44:47],a[112:115]
T v[82:83],v20 offset:33792
M a[116:119],v[116:119],v[44:47],a[116:119]
B %1,s[64:67],s41 offen lds
M a[120:123],v[120:123],v[44:47],a[120:123]
)"
R"(M a[124:127],v[124:127],v[44:47],a[124:127]
R v[44:47],v6 offset:6144
T v[84:85],v21 offset:32768
M a[128:131],v[96:99],v[48:51],a[128:131]
SA m0,s40,106496
M a[132:135],v[100:103],v[48:51],a[132:135]
M a[136:139],v[104:107],v[48:51],a[136:139]
T v[86:87],v21 offset:33792
)"
R"(SM s41,3407872
M a[140:143],v[108:111],v[48:51],a[140:143]
M a[144:147],v[112:115],v[48:51],a[144:147]
M a[148:151],v[116:119],v[48:51],a[148:151]
T v[88:89],v22 offset:32768
B %1,s[60:63],s41 offen lds
M a[152:155],v[120:123],v[48:51],a[152:155]
M a[156:159],v[124:127],v[48:51],a[156:159]
)"
R"(R v[48:51],v6 offset:8192
M a[160:163],v[96:99],v[52:55],a[160:163]
T v[90:91],v22 offset:33792
SA m0,s40,122880
M a[164:167],v[100:103],v[52:55],a[164:167]
M a[168:171],v[104:107],v[52:55],a[168:171]
SM s41,3407872
M a[172:175],v[108:111],v[52:55],a[172:175]
)"
R"(T v[92:93],v23 offset:32768
M a[176:179],v[112:115],v[52:55],a[176:179]
M a[180:183],v[116:119],v[52:55],a[180:183]
B %1,s[64:67],s41 offen lds
M a[184:187],v[120:123],v[52:55],a[184:187]
T v[94:95],v23 offset:33792
M a[188:191],v[124:127],v[52:55],a[188:191]
R v[52:55],v6 offset:10240
)"
R"(M a[192:195],v[96:99],v[56:59],a[192:195]
SA m0,s40,110592
M a[196:199],v[100:103],v[56:59],a[196:199]
M a[200:203],v[104:107],v[56:59],a[200:203]
SM s41,3538944
M a[204:207],v[108:111],v[56:59],a[204:207]
M a[208:211],v[112:115],v[56:59],a[208:211]
M a[212:215],v[116:119],v[56:59],a[212:215]
)"
R"(B %1,s[60:63],s41 offen lds
M a[216:219],v[120:123],v[56:59],a[216:219]
M a[220:223],v[124:127],v[56:59],a[220:223]
R v[56:59],v6 offset:12288
M a[224:227],v[96:99],v[60:63],a[224:227]
SA m0,s40,126976
M a[228:231],v[100:103],v[60:63],a[228:231]
M a[232:235],v[104:107],v[60:63],a[232:235]
)"
R"(SM s41,3538944
M a[236:239],v[108:111],v[60:63],a[236:239]
M a[240:243],v[112:115],v[60:63],a[240:243]
M a[244:247],v[116:119],v[60:63],a[244:247]
B %1,s[64:67],s41 offen lds
M a[248:251],v[120:123],v[60:63],a[248:251]
M a[252:255],v[124:127],v[60:63],a[252:255]
R v[60:63],v6 offset:14336
)"
R"(W lgkmcnt(14)
M a[0:3],v[64:67],v[32:35],a[0:3]
SA m0,s40,32768
M a[4:7],v[68:71],v[32:35],a[4:7]
T v[96:97],v16 offset:40960
M a[8:11],v[72:75],v[32:35],a[8:11]
SM s41,896
M a[12:15],v[76:79],v[32:35],a[12:15]
)"
R"(W lgkmcnt(4)
M a[16:19],v[80:83],v[32:35],a[16:19]
T v[98:99],v16 offset:41984
B %0,s[48:51],s41 offen lds
M a[20:23],v[84:87],v[32:35],a[20:23]
M a[24:27],v[88:91],v[32:35],a[24:27]
M a[28:31],v[92:95],v[32:35],a[28:31]
R v[32:35],v7 offset:0
)"
R"(T v[100:101],v17 offset:40960
M a[32:35],v[64:67],v[36:39],a[32:35]
SA m0,s40,36864
M a[36:39],v[68:71],v[36:39],a[36:39]
SM s41,33664
M a[40:43],v[72:75],v[36:39],a[40:43]
T v[102:103],v17 offset:41984
M a[44:47],v[76:79],v[36:39],a[44:47]
)"
R"(M a[48:51],v[80:83],v[36:39],a[48:51]
B %0,s[48:51],s41 offen lds
M a[52:55],v[84:87],v[36:39],a[52:55]
T v[104:105],v18 offset:40960
M a[56:59],v[88:91],v[36:39],a[56:59]
SA m0,s40,40960
M a[60:63],v[92:95],v[36:39],a[60:63]
R v[36:39],v7 offset:2048
)"
R"(M a[64:67],v[64:67],v[40:43],a[64:67]
T v[106:107],v18 offset:41984
SM s41,66432
M a[68:71],v[68:71],v[40:43],a[68:71]
M a[72:75],v[72:75],v[40:43],a[72:75]
M a[76:79],v[76:79],v[40:43],a[76:79]
T v[108:109],v19 offset:40960
B %0,s[48:51],s41 offen lds
)"
R"(M a[80:83],v[80:83],v[40:43],a[80:83]
M a[84:87],v[84:87],v[40:43],a[84:87]
SA m0,s40,45056
M a[88:91],v[88:91],v[40:43],a[88:91]
T v[110:111],v19 offset:41984
M a[92:95],v[92:95],v[40:43],a[92:95]
R v[40:43],v7 offset:4096
M a[96:99],v[64:67],v[44:47],a[96:99]
)"
R"(SM s41,99200
M a[100:103],v[68:71],v[44:47],a[100:103]
T v[112:113],v20 offset:40960
M a[104:107],v[72:75],v[44:47],a[104:107]
B %0,s[48:51],s41 offen lds
M a[108:111],v[76:79],v[44:47],a[108:111]
M a[112:115],v[80:83],v[44:47],a[112:115]
T v[114:115],v20 offset:41984
)"
R"(SA m0,s40,49152
M a[116:119],v[84:87],v[44:47],a[116:119]
M a[120:123],v[88:91],v[44:47],a[120:123]
M a[124:127],v[92:95],v[44:47],a[124:127]
R v[44:47],v7 offset:6144
T v[116:117],v21 offset:40960
M a[128:131],v[64:67],v[48:51],a[128:131]
SM s41,131968
)"
R"(M a[132:135],v[68:71],v[48:51],a[132:135]
B %0,s[48:51],s41 offen lds
M a[136:139],v[72:75],v[48:51],a[136:139]
T v[118:119],v21 offset:41984
M a[140:143],v[76:79],v[48:51],a[140:143]
M a[144:147],v[80:83],v[48:51],a[144:147]
SA m0,s40,53248
M a[148:151],v[84:87],v[48:51],a[148:151]
)"
R"(T v[120:121],v22 offset:40960
M a[152:155],v[88:91],v[48:51],a[152:155]
SM s41,164736
M a[156:159],v[92:95],v[48:51],a[156:159]
R v[48:51],v7 offset:8192
W lgkmcnt(15)
M a[160:163],v[64:67],v[52:55],a[160:163]
T v[122:123],v22 offset:41984
)"
R"(B %0,s[48:51],s41 offen lds
M a[164:167],v[68:71],v[52:55],a[164:167]
M a[168:171],v[72:75],v[52:55],a[168:171]
M a[172:175],v[76:79],v[52:55],a[172:175]
T v[124:125],v23 offset:40960
SA m0,s40,57344
M a[176:179],v[80:83],v[52:55],a[176:179]
M a[180:183],v[84:87],v[52:55],a[180:183]
)"
R"(SM s41,197504
M a[184:187],v[88:91],v[52:55],a[184:187]
T v[126:127],v23 offset:41984
M a[188:191],v[92:95],v[52:55],a[188:191]
R v[52:55],v7 offset:10240
M a[192:195],v[64:67],v[56:59],a[192:195]
B %0,s[48:51],s41 offen lds
M a[196:199],v[68:71],v[56:59],a[196:199]
)"
R"(M a[200:203],v[72:75],v[56:59],a[200:203]
SA m0,s40,61440
M a[204:207],v[76:79],v[56:59],a[204:207]
M a[208:211],v[80:83],v[56:59],a[208:211]
SM s41,230272
M a[212:215],v[84:87],v[56:59],a[212:215]
M a[216:219],v[88:91],v[56:59],a[216:219]
M a[220:223],v[92:95],v[56:59],a[220:223]
)"
R"(R v[56:59],v7 offset:12288
B %0,s[48:51],s41 offen lds
M a[224:227],v[64:67],v[60:63],a[224:227]
M a[228:231],v[68:71],v[60:63],a[228:231]
M a[232:235],v[72:75],v[60:63],a[232:235]
M a[236:239],v[76:79],v[60:63],a[236:239]
M a[240:243],v[80:83],v[60:63],a[240:243]
M a[244:247],v[84:87],v[60:63],a[244:247]
)"
R"(M a[248:251],v[88:91],v[60:63],a[248:251]
M a[252:255],v[92:95],v[60:63],a[252:255]
R v[60:63],v7 offset:14336
W vmcnt(8) lgkmcnt(0)
s_barrier
M a[0:3],v[96:99],v[32:35],a[0:3]
SA m0,s40,131072
M a[4:7],v[100:103],v[32:35],a[4:7]
)"
R"(T v[64:65],v16 offset:0
M a[8:11],v[104:107],v[32:35],a[8:11]
SM s41,3670016
M a[12:15],v[108:111],v[32:35],a[12:15]
M a[16:19],v[112:115],v[32:35],a[16:19]
T v[66:67],v16 offset:1024
M a[20:23],v[116:119],v[32:35],a[20:23]
B %1,s[60:63],s41 offen lds
)"
R"(M a[24:27],v[120:123],v[32:35],a[24:27]
M a[28:31],v[124:127],v[32:35],a[28:31]
R v[32:35],v4 offset:0
T v[68:69],v17 offset:0
M a[32:35],v[96:99],v[36:39],a[32:35]
SA m0,s40,147456
M a[36:39],v[100:103],v[36:39],a[36:39]
M a[40:43],v[104:107],v[36:39],a[40:43]
)"
R"(T v[70:71],v17 offset:1024
SM s41,3670016
M a[44:47],v[108:111],v[36:39],a[44:47]
M a[48:51],v[112:115],v[36:39],a[48:51]
M a[52:55],v[116:119],v[36:39],a[52:55]
T v[72:73],v18 offset:0
B %1,s[64:67],s41 offen lds
M a[56:59],v[120:123],v[36:39],a[56:59]
)"
R"(M a[60:63],v[124:127],v[36:39],a[60:63]
R v[36:39],v4 offset:2048
M a[64:67],v[96:99],v[40:43],a[64:67]
T v[74:75],v18 offset:1024
SA m0,s40,135168
M a[68:71],v[100:103],v[40:43],a[68:71]
M a[72:75],v[104:107],v[40:43],a[72:75]
SM s41,3801088
)"
R"(M a[76:79],v[108:111],v[40:43],a[76:79]
T v[76:77],v19 offset:0
M a[80:83],v[112:115],v[40:43],a[80:83]
M a[84:87],v[116:119],v[40:43],a[84:87]
B %1,s[60:63],s41 offen lds
M a[88:91],v[120:123],v[40:43],a[88:91]
T v[78:79],v19 offset:1024
M a[92:95],v[124:127],v[40:43],a[92:95]
)"
R"(R v[40:43],v4 offset:4096
M a[96:99],v[96:99],v[44:47],a[96:99]
SA m0,s40,151552
M a[100:103],v[100:103],v[44:47],a[100:103]
T v[80:81],v20 offset:0
M a[104:107],v[104:107],v[44:47],a[104:107]
SM s41,3801088
M a[108:111],v[108:111],v[44:47],a[108:111]
)"
R"(M a[112:115],v[112:115],v[44:47],a[112:115]
T v[82:83],v20 offset:1024
M a[116:119],v[116:119],v[44:47],a[116:119]
B %1,s[64:67],s41 offen lds
M a[120:123],v[120:123],v[44:47],a[120:123]
M a[124:127],v[124:127],v[44:47],a[124:127]
R v[44:47],v4 offset:6144
T v[84:85],v21 offset:0
)"
R"(M a[128:131],v[96:99],v[48:51],a[128:131]
SA m0,s40,139264
M a[132:135],v[100:103],v[48:51],a[132:135]
M a[136:139],v[104:107],v[48:51],a[136:139]
T v[86:87],v21 offset:1024
SM s41,3932160
M a[140:143],v[108:111],v[48:51],a[140:143]
M a[144:147],v[112:115],v[48:51],a[144:147]
)"
R"(M a[148:151],v[116:119],v[48:51],a[148:151]
T v[88:89],v22 offset:0
B %1,s[60:63],s41 offen lds
M a[152:155],v[120:123],v[48:51],a[152:155]
M a[156:159],v[124:127],v[48:51],a[156:159]
R v[48:51],v4 offset:8192
M a[160:163],v[96:99],v[52:55],a[160:163]
T v[90:91],v22 offset:1024
)"
R"(SA m0,s40,155648
M a[164:167],v[100:103],v[52:55],a[164:167]
M a[168:171],v[104:107],v[52:55],a[168:171]
SM s41,3932160
M a[172:175],v[108:111],v[52:55],a[172:175]
T v[92:93],v23 offset:0
M a[176:179],v[112:115],v[52:55],a[176:179]
M a[180:183],v[116:119],v[52:55],a[180:183]
)"
R"(B %1,s[64:67],s41 offen lds
M a[184:187],v[120:123],v[52:55],a[184:187]
T v[94:95],v23 offset:1024
M a[188:191],v[124:127],v[52:55],a[188:191]
R v[52:55],v4 offset:10240
M a[192:195],v[96:99],v[56:59],a[192:195]
SA m0,s40,143360
M a[196:199],v[100:103],v[56:59],a[196:199]
)"
R"(M a[200:203],v[104:107],v[56:59],a[200:203]
SM s41,4063232
M a[204:207],v[108:111],v[56:59],a[204:207]
M a[208:211],v[112:115],v[56:59],a[208:211]
M a[212:215],v[116:119],v[56:59],a[212:215]
B %1,s[60:63],s41 offen lds
M a[216:219],v[120:123],v[56:59],a[216:219]
M a[220:223],v[124:127],v[56:59],a[220:223]
)"
R"(R v[56:59],v4 offset:12288
M a[224:227],v[96:99],v[60:63],a[224:227]
SA m0,s40,159744
M a[228:231],v[100:103],v[60:63],a[228:231]
M a[232:235],v[104:107],v[60:63],a[232:235]
SM s41,4063232
M a[236:239],v[108:111],v[60:63],a[236:239]
M a[240:243],v[112:115],v[60:63],a[240:243]
)"
R"(M a[244:247],v[116:119],v[60:63],a[244:247]
B %1,s[64:67],s41 offen lds
M a[248:251],v[120:123],v[60:63],a[248:251]
M a[252:255],v[124:127],v[60:63],a[252:255]
R v[60:63],v4 offset:14336
W lgkmcnt(14)
M a[0:3],v[64:67],v[32:35],a[0:3]
SA m0,s40,65536
)"
R"(M a[4:7],v[68:71],v[32:35],a[4:7]
T v[96:97],v16 offset:8192
M a[8:11],v[72:75],v[32:35],a[8:11]
SM s41,0
M a[12:15],v[76:79],v[32:35],a[12:15]
W lgkmcnt(4)
M a[16:19],v[80:83],v[32:35],a[16:19]
T v[98:99],v16 offset:9216
)"
R"(B %0,s[52:55],s41 offen lds
M a[20:23],v[84:87],v[32:35],a[20:23]
M a[24:27],v[88:91],v[32:35],a[24:27]
M a[28:31],v[92:95],v[32:35],a[28:31]
R v[32:35],v5 offset:0
T v[100:101],v17 offset:8192
M a[32:35],v[64:67],v[36:39],a[32:35]
SA m0,s40,69632
)"
R"(M a[36:39],v[68:71],v[36:39],a[36:39]
SM s41,32768
M a[40:43],v[72:75],v[36:39],a[40:43]
T v[102:103],v17 offset:9216
M a[44:47],v[76:79],v[36:39],a[44:47]
M a[48:51],v[80:83],v[36:39],a[48:51]
B %0,s[52:55],s41 offen lds
M a[52:55],v[84:87],v[36:39],a[52:55]
)"
R"(T v[104:105],v18 offset:8192
M a[56:59],v[88:91],v[36:39],a[56:59]
SA m0,s40,73728
M a[60:63],v[92:95],v[36:39],a[60:63]
R v[36:39],v5 offset:2048
M a[64:67],v[64:67],v[40:43],a[64:67]
T v[106:107],v18 offset:9216
SM s41,65536
)"
R"(M a[68:71],v[68:71],v[40:43],a[68:71]
M a[72:75],v[72:75],v[40:43],a[72:75]
M a[76:79],v[76:79],v[40:43],a[76:79]
T v[108:109],v19 offset:8192
B %0,s[52:55],s41 offen lds
M a[80:83],v[80:83],v[40:43],a[80:83]
M a[84:87],v[84:87],v[40:43],a[84:87]
SA m0,s40,77824
)"
R"(M a[88:91],v[88:91],v[40:43],a[88:91]
T v[110:111],v19 offset:9216
M a[92:95],v[92:95],v[40:43],a[92:95]
R v[40:43],v5 offset:4096
M a[96:99],v[64:67],v[44:47],a[96:99]
SM s41,98304
M a[100:103],v[68:71],v[44:47],a[100:103]
T v[112:113],v20 offset:8192
)"
R"(M a[104:107],v[72:75],v[44:47],a[104:107]
B %0,s[52:55],s41 offen lds
M a[108:111],v[76:79],v[44:47],a[108:111]
M a[112:115],v[80:83],v[44:47],a[112:115]
T v[114:115],v20 offset:9216
SA m0,s40,81920
M a[116:119],v[84:87],v[44:47],a[116:119]
M a[120:123],v[88:91],v[44:47],a[120:123]
)"
R"(M a[124:127],v[92:95],v[44:47],a[124:127]
R v[44:47],v5 offset:6144
T v[116:117],v21 offset:8192
M a[128:131],v[64:67],v[48:51],a[128:131]
SM s41,131072
M a[132:135],v[68:71],v[48:51],a[132:135]
B %0,s[52:55],s41 offen lds
M a[136:139],v[72:75],v[48:51],a[136:139]
)"
R"(T v[118:119],v21 offset:9216
M a[140:143],v[76:79],v[48:51],a[140:143]
M a[144:147],v[80:83],v[48:51],a[144:147]
SA m0,s40,86016
M a[148:151],v[84:87],v[48:51],a[148:151]
T v[120:121],v22 offset:8192
M a[152:155],v[88:91],v[48:51],a[152:155]
SM s41,163840
)"
R"(M a[156:159],v[92:95],v[48:51],a[156:159]
R v[48:51],v5 offset:8192
W lgkmcnt(15)
M a[160:163],v[64:67],v[52:55],a[160:163]
T v[122:123],v22 offset:9216
B %0,s[52:55],s41 offen lds
M a[164:167],v[68:71],v[52:55],a[164:167]
M a[168:171],v[72:75],v[52:55],a[168:171]
)"
R"(M a[172:175],v[76:79],v[52:55],a[172:175]
T v[124:125],v23 offset:8192
SA m0,s40,90112
M a[176:179],v[80:83],v[52:55],a[176:179]
M a[180:183],v[84:87],v[52:55],a[180:183]
SM s41,196608
M a[184:187],v[88:91],v[52:55],a[184:187]
T v[126:127],v23 offset:9216
)"
R"(M a[188:191],v[92:95],v[52:55],a[188:191]
R v[52:55],v5 offset:10240
M a[192:195],v[64:67],v[56:59],a[192:195]
B %0,s[52:55],s41 offen lds
M a[196:199],v[68:71],v[56:59],a[196:199]
M a[200:203],v[72:75],v[56:59],a[200:203]
SA m0,s40,94208
M a[204:207],v[76:79],v[56:59],a[204:207]
)"
R"(M a[208:211],v[80:83],v[56:59],a[208:211]
SM s41,229376
M a[212:215],v[84:87],v[56:59],a[212:215]
M a[216:219],v[88:91],v[56:59],a[216:219]
M a[220:223],v[92:95],v[56:59],a[220:223]
R v[56:59],v5 offset:12288
B %0,s[52:55],s41 offen lds
M a[224:227],v[64:67],v[60:63],a[224:227]
)"
R"(M a[228:231],v[68:71],v[60:63],a[228:231]
M a[232:235],v[72:75],v[60:63],a[232:235]
M a[236:239],v[76:79],v[60:63],a[236:239]
M a[240:243],v[80:83],v[60:63],a[240:243]
M a[244:247],v[84:87],v[60:63],a[244:247]
M a[248:251],v[88:91],v[60:63],a[248:251]
M a[252:255],v[92:95],v[60:63],a[252:255]
R v[60:63],v5 offset:14336
)"
R"(W vmcnt(8) lgkmcnt(0)
s_barrier
M a[0:3],v[96:99],v[32:35],a[0:3]
SA m0,s40,98304
M a[4:7],v[100:103],v[32:35],a[4:7]
T v[64:65],v16 offset:32768
M a[8:11],v[104:107],v[32:35],a[8:11]
M a[12:15],v[108:111],v[32:35],a[12:15]
)"
R"(SM s41,0
M a[16:19],v[112:115],v[32:35],a[16:19]
T v[66:67],v16 offset:33792
M a[20:23],v[116:119],v[32:35],a[20:23]
M a[24:27],v[120:123],v[32:35],a[24:27]
M a[28:31],v[124:127],v[32:35],a[28:31]
R v[32:35],v4 offset:32768
T v[68:69],v17 offset:32768
)"
R"(M a[32:35],v[96:99],v[36:39],a[32:35]
B %2,s[72:75],s41 offen lds
M a[36:39],v[100:103],v[36:39],a[36:39]
M a[40:43],v[104:107],v[36:39],a[40:43]
T v[70:71],v17 offset:33792
SA m0,s40,102400
M a[44:47],v[108:111],v[36:39],a[44:47]
M a[48:51],v[112:115],v[36:39],a[48:51]
)"
R"(M a[52:55],v[116:119],v[36:39],a[52:55]
T v[72:73],v18 offset:32768
M a[56:59],v[120:123],v[36:39],a[56:59]
SM s41,98304
M a[60:63],v[124:127],v[36:39],a[60:63]
R v[36:39],v4 offset:34816
M a[64:67],v[96:99],v[40:43],a[64:67]
T v[74:75],v18 offset:33792
)"
R"(M a[68:71],v[100:103],v[40:43],a[68:71]
B %2,s[72:75],s41 offen lds
M a[72:75],v[104:107],v[40:43],a[72:75]
M a[76:79],v[108:111],v[40:43],a[76:79]
T v[76:77],v19 offset:32768
M a[80:83],v[112:115],v[40:43],a[80:83]
M a[84:87],v[116:119],v[40:43],a[84:87]
SA m0,s40,106496
)"
R"(M a[88:91],v[120:123],v[40:43],a[88:91]
T v[78:79],v19 offset:33792
M a[92:95],v[124:127],v[40:43],a[92:95]
R v[40:43],v4 offset:36864
M a[96:99],v[96:99],v[44:47],a[96:99]
SM s41,196608
M a[100:103],v[100:103],v[44:47],a[100:103]
T v[80:81],v20 offset:32768
)"
R"(M a[104:107],v[104:107],v[44:47],a[104:107]
M a[108:111],v[108:111],v[44:47],a[108:111]
M a[112:115],v[112:115],v[44:47],a[112:115]
T v[82:83],v20 offset:33792
B %2,s[72:75],s41 offen lds
M a[116:119],v[116:119],v[44:47],a[116:119]
M a[120:123],v[120:123],v[44:47],a[120:123]
M a[124:127],v[124:127],v[44:47],a[124:127]
)"
R"(R v[44:47],v4 offset:38912
T v[84:85],v21 offset:32768
M a[128:131],v[96:99],v[48:51],a[128:131]
SA m0,s40,110592
M a[132:135],v[100:103],v[48:51],a[132:135]
M a[136:139],v[104:107],v[48:51],a[136:139]
T v[86:87],v21 offset:33792
M a[140:143],v[108:111],v[48:51],a[140:143]
)"
R"(SM s41,294912
M a[144:147],v[112:115],v[48:51],a[144:147]
M a[148:151],v[116:119],v[48:51],a[148:151]
T v[88:89],v22 offset:32768
M a[152:155],v[120:123],v[48:51],a[152:155]
M a[156:159],v[124:127],v[48:51],a[156:159]
R v[48:51],v4 offset:40960
B %2,s[72:75],s41 offen lds
)"
R"(M a[160:163],v[96:99],v[52:55],a[160:163]
T v[90:91],v22 offset:33792
M a[164:167],v[100:103],v[52:55],a[164:167]
M a[168:171],v[104:107],v[52:55],a[168:171]
SA m0,s40,0
M a[172:175],v[108:111],v[52:55],a[172:175]
T v[92:93],v23 offset:32768
M a[176:179],v[112:115],v[52:55],a[176:179]
)"
R"(M a[180:183],v[116:119],v[52:55],a[180:183]
M a[184:187],v[120:123],v[52:55],a[184:187]
T v[94:95],v23 offset:33792
SM s41,128
M a[188:191],v[124:127],v[52:55],a[188:191]
R v[52:55],v4 offset:43008
M a[192:195],v[96:99],v[56:59],a[192:195]
M a[196:199],v[100:103],v[56:59],a[196:199]
)"
R"(B %0,s[52:55],s41 offen lds
M a[200:203],v[104:107],v[56:59],a[200:203]
M a[204:207],v[108:111],v[56:59],a[204:207]
M a[208:211],v[112:115],v[56:59],a[208:211]
M a[212:215],v[116:119],v[56:59],a[212:215]
SA m0,s40,4096
M a[216:219],v[120:123],v[56:59],a[216:219]
M a[220:223],v[124:127],v[56:59],a[220:223]
)"
R"(R v[56:59],v4 offset:45056
M a[224:227],v[96:99],v[60:63],a[224:227]
SM s41,32896
M a[228:231],v[100:103],v[60:63],a[228:231]
M a[232:235],v[104:107],v[60:63],a[232:235]
M a[236:239],v[108:111],v[60:63],a[236:239]
M a[240:243],v[112:115],v[60:63],a[240:243]
B %0,s[52:55],s41 offen lds
)"
R"(M a[244:247],v[116:119],v[60:63],a[244:247]
M a[248:251],v[120:123],v[60:63],a[248:251]
M a[252:255],v[124:127],v[60:63],a[252:255]
R v[60:63],v4 offset:47104
W lgkmcnt(14)
M a[0:3],v[64:67],v[32:35],a[0:3]
SA m0,s40,8192
M a[4:7],v[68:71],v[32:35],a[4:7]
)"
R"(T v[96:97],v16 offset:40960
M a[8:11],v[72:75],v[32:35],a[8:11]
M a[12:15],v[76:79],v[32:35],a[12:15]
SM s41,65664
W lgkmcnt(4)
M a[16:19],v[80:83],v[32:35],a[16:19]
T v[98:99],v16 offset:41984
M a[20:23],v[84:87],v[32:35],a[20:23]
)"
R"(M a[24:27],v[88:91],v[32:35],a[24:27]
B %0,s[52:55],s41 offen lds
M a[28:31],v[92:95],v[32:35],a[28:31]
R v[32:35],v5 offset:32768
T v[100:101],v17 offset:40960
M a[32:35],v[64:67],v[36:39],a[32:35]
M a[36:39],v[68:71],v[36:39],a[36:39]
SA m0,s40,12288
)"
R"(M a[40:43],v[72:75],v[36:39],a[40:43]
T v[102:103],v17 offset:41984
M a[44:47],v[76:79],v[36:39],a[44:47]
M a[48:51],v[80:83],v[36:39],a[48:51]
SM s41,98432
M a[52:55],v[84:87],v[36:39],a[52:55]
T v[104:105],v18 offset:40960
M a[56:59],v[88:91],v[36:39],a[56:59]
)"
R"(M a[60:63],v[92:95],v[36:39],a[60:63]
R v[36:39],v5 offset:34816
M a[64:67],v[64:67],v[40:43],a[64:67]
T v[106:107],v18 offset:41984
B %0,s[52:55],s41 offen lds
M a[68:71],v[68:71],v[40:43],a[68:71]
M a[72:75],v[72:75],v[40:43],a[72:75]
M a[76:79],v[76:79],v[40:43],a[76:79]
)"
R"(T v[108:109],v19 offset:40960
SA m0,s40,16384
M a[80:83],v[80:83],v[40:43],a[80:83]
M a[84:87],v[84:87],v[40:43],a[84:87]
M a[88:91],v[88:91],v[40:43],a[88:91]
T v[110:111],v19 offset:41984
SM s41,131200
M a[92:95],v[92:95],v[40:43],a[92:95]
)"
R"(R v[40:43],v5 offset:36864
M a[96:99],v[64:67],v[44:47],a[96:99]
M a[100:103],v[68:71],v[44:47],a[100:103]
T v[112:113],v20 offset:40960
B %0,s[52:55],s41 offen lds
M a[104:107],v[72:75],v[44:47],a[104:107]
M a[108:111],v[76:79],v[44:47],a[108:111]
M a[112:115],v[80:83],v[44:47],a[112:115]
)"
R"(T v[114:115],v20 offset:41984
SA m0,s40,20480
M a[116:119],v[84:87],v[44:47],a[116:119]
M a[120:123],v[88:91],v[44:47],a[120:123]
M a[124:127],v[92:95],v[44:47],a[124:127]
R v[44:47],v5 offset:38912
T v[116:117],v21 offset:40960
M a[128:131],v[64:67],v[48:51],a[128:131]
)"
R"(SM s41,163968
M a[132:135],v[68:71],v[48:51],a[132:135]
M a[136:139],v[72:75],v[48:51],a[136:139]
T v[118:119],v21 offset:41984
M a[140:143],v[76:79],v[48:51],a[140:143]
B %0,s[52:55],s41 offen lds
M a[144:147],v[80:83],v[48:51],a[144:147]
M a[148:151],v[84:87],v[48:51],a[148:151]
)"
R"(T v[120:121],v22 offset:40960
M a[152:155],v[88:91],v[48:51],a[152:155]
SA m0,s40,24576
M a[156:159],v[92:95],v[48:51],a[156:159]
R v[48:51],v5 offset:40960
W lgkmcnt(15)
M a[160:163],v[64:67],v[52:55],a[160:163]
T v[122:123],v22 offset:41984
)"
R"(M a[164:167],v[68:71],v[52:55],a[164:167]
SM s41,196736
M a[168:171],v[72:75],v[52:55],a[168:171]
M a[172:175],v[76:79],v[52:55],a[172:175]
T v[124:125],v23 offset:40960
M a[176:179],v[80:83],v[52:55],a[176:179]
B %0,s[52:55],s41 offen lds
M a[180:183],v[84:87],v[52:55],a[180:183]
)"
R"(M a[184:187],v[88:91],v[52:55],a[184:187]
T v[126:127],v23 offset:41984
M a[188:191],v[92:95],v[52:55],a[188:191]
R v[52:55],v5 offset:43008
M a[192:195],v[64:67],v[56:59],a[192:195]
SA m0,s40,28672
M a[196:199],v[68:71],v[56:59],a[196:199]
M a[200:203],v[72:75],v[56:59],a[200:203]
)"
R"(M a[204:207],v[76:79],v[56:59],a[204:207]
SM s41,229504
M a[208:211],v[80:83],v[56:59],a[208:211]
M a[212:215],v[84:87],v[56:59],a[212:215]
M a[216:219],v[88:91],v[56:59],a[216:219]
B %0,s[52:55],s41 offen lds
M a[220:223],v[92:95],v[56:59],a[220:223]
R v[56:59],v5 offset:45056
)"
R"(M a[224:227],v[64:67],v[60:63],a[224:227]
M a[228:231],v[68:71],v[60:63],a[228:231]
M a[232:235],v[72:75],v[60:63],a[232:235]
M a[236:239],v[76:79],v[60:63],a[236:239]
M a[240:243],v[80:83],v[60:63],a[240:243]
M a[244:247],v[84:87],v[60:63],a[244:247]
M a[248:251],v[88:91],v[60:63],a[248:251]
M a[252:255],v[92:95],v[60:63],a[252:255]
)"
R"(R v[60:63],v5 offset:47104
W vmcnt(10) lgkmcnt(0)
s_barrier
M a[0:3],v[96:99],v[32:35],a[0:3]
SA m0,s40,114688
M a[4:7],v[100:103],v[32:35],a[4:7]
M a[8:11],v[104:107],v[32:35],a[8:11]
T v[64:65],v24 offset:0
)"
R"(M a[12:15],v[108:111],v[32:35],a[12:15]
M a[16:19],v[112:115],v[32:35],a[16:19]
SM s41,393216
M a[20:23],v[116:119],v[32:35],a[20:23]
T v[66:67],v24 offset:512
M a[24:27],v[120:123],v[32:35],a[24:27]
M a[28:31],v[124:127],v[32:35],a[28:31]
R v[32:35],v8 offset:0
)"
R"(M a[32:35],v[96:99],v[36:39],a[32:35]
B %2,s[72:75],s41 offen lds
M a[36:39],v[100:103],v[36:39],a[36:39]
M a[40:43],v[104:107],v[36:39],a[40:43]
T v[68:69],v25 offset:0
M a[44:47],v[108:111],v[36:39],a[44:47]
M a[48:51],v[112:115],v[36:39],a[48:51]
SA m0,s40,118784
)"
R"(M a[52:55],v[116:119],v[36:39],a[52:55]
T v[70:71],v25 offset:512
M a[56:59],v[120:123],v[36:39],a[56:59]
M a[60:63],v[124:127],v[36:39],a[60:63]
R v[36:39],v8 offset:2048
M a[64:67],v[96:99],v[40:43],a[64:67]
M a[68:71],v[100:103],v[40:43],a[68:71]
SM s41,491520
)"
R"(M a[72:75],v[104:107],v[40:43],a[72:75]
T v[72:73],v26 offset:0
M a[76:79],v[108:111],v[40:43],a[76:79]
M a[80:83],v[112:115],v[40:43],a[80:83]
M a[84:87],v[116:119],v[40:43],a[84:87]
T v[74:75],v26 offset:512
B %2,s[72:75],s41 offen lds
M a[88:91],v[120:123],v[40:43],a[88:91]
)"
R"(M a[92:95],v[124:127],v[40:43],a[92:95]
R v[40:43],v8 offset:4096
M a[96:99],v[96:99],v[44:47],a[96:99]
M a[100:103],v[100:103],v[44:47],a[100:103]
SA m0,s40,32768
M a[104:107],v[104:107],v[44:47],a[104:107]
M a[108:111],v[108:111],v[44:47],a[108:111]
M a[112:115],v[112:115],v[44:47],a[112:115]
)"
R"(M a[116:119],v[116:119],v[44:47],a[116:119]
SM s41,256
M a[120:123],v[120:123],v[44:47],a[120:123]
M a[124:127],v[124:127],v[44:47],a[124:127]
R v[44:47],v8 offset:6144
M a[128:131],v[96:99],v[48:51],a[128:131]
M a[132:135],v[100:103],v[48:51],a[132:135]
M a[136:139],v[104:107],v[48:51],a[136:139]
)"
R"(B %0,s[52:55],s41 offen lds
M a[140:143],v[108:111],v[48:51],a[140:143]
M a[144:147],v[112:115],v[48:51],a[144:147]
M a[148:151],v[116:119],v[48:51],a[148:151]
M a[152:155],v[120:123],v[48:51],a[152:155]
SA m0,s40,36864
M a[156:159],v[124:127],v[48:51],a[156:159]
M a[160:163],v[96:99],v[52:55],a[160:163]
)"
R"(M a[164:167],v[100:103],v[52:55],a[164:167]
M a[168:171],v[104:107],v[52:55],a[168:171]
SM s41,33024
M a[172:175],v[108:111],v[52:55],a[172:175]
M a[176:179],v[112:115],v[52:55],a[176:179]
M a[180:183],v[116:119],v[52:55],a[180:183]
M a[184:187],v[120:123],v[52:55],a[184:187]
B %0,s[52:55],s41 offen lds
)"
R"(M a[188:191],v[124:127],v[52:55],a[188:191]
M a[192:195],v[96:99],v[56:59],a[192:195]
M a[196:199],v[100:103],v[56:59],a[196:199]
M a[200:203],v[104:107],v[56:59],a[200:203]
M a[204:207],v[108:111],v[56:59],a[204:207]
SA m0,s40,40960
M a[208:211],v[112:115],v[56:59],a[208:211]
M a[212:215],v[116:119],v[56:59],a[212:215]
)"
R"(M a[216:219],v[120:123],v[56:59],a[216:219]
M a[220:223],v[124:127],v[56:59],a[220:223]
SM s41,65792
M a[224:227],v[96:99],v[60:63],a[224:227]
M a[228:231],v[100:103],v[60:63],a[228:231]
M a[232:235],v[104:107],v[60:63],a[232:235]
M a[236:239],v[108:111],v[60:63],a[236:239]
B %0,s[52:55],s41 offen lds
)"
R"(M a[240:243],v[112:115],v[60:63],a[240:243]
M a[244:247],v[116:119],v[60:63],a[244:247]
M a[248:251],v[120:123],v[60:63],a[248:251]
M a[252:255],v[124:127],v[60:63],a[252:255]
s_lshl_b32 s42,%17,13
v_add_u32 v4,s42,v27
v_xor_b32 v5,64,v4
v_add_u32 v6,0x10000,v4
)"
R"(v_add_u32 v7,0x10000,v5
W lgkmcnt(2)
M v[128:131],v[64:67],v[32:35],0
T v[96:97],v24 offset:4096
SA m0,s40,45056
SM s41,98560
M v[144:147],v[68:71],v[32:35],0
T v[98:99],v24 offset:4608
)"
R"(B %0,s[52:55],s41 offen lds
M v[160:163],v[32:35],v[72:75],0
R v[32:35],v7 offset:0
SA m0,s40,49152
SM s41,131328
M v[132:135],v[64:67],v[36:39],0
T v[100:101],v25 offset:4096
B %0,s[52:55],s41 offen lds
)"
R"(M v[148:151],v[68:71],v[36:39],0
T v[102:103],v25 offset:4608
SA m0,s40,53248
M v[164:167],v[36:39],v[72:75],0
R v[36:39],v7 offset:2048
SM s41,164096
B %0,s[52:55],s41 offen lds
W lgkmcnt(6)
)"
R"(M v[136:139],v[64:67],v[40:43],0
T v[104:105],v26 offset:4096
SA m0,s40,57344
M v[152:155],v[68:71],v[40:43],0
T v[106:107],v26 offset:4608
SM s41,196864
B %0,s[52:55],s41 offen lds
M v[168:171],v[40:43],v[72:75],0
)"
R"(R v[40:43],v7 offset:4096
SA m0,s40,61440
M v[140:143],v[64:67],v[44:47],0
SM s41,229632
M v[156:159],v[68:71],v[44:47],0
B %0,s[52:55],s41 offen lds
M v[172:175],v[44:47],v[72:75],0
R v[44:47],v7 offset:6144
)"
R"(W vmcnt(10) lgkmcnt(0)
s_barrier
M v[128:131],v[96:99],v[32:35],v[128:131]
T v[64:65],v24 offset:8192
SA m0,s40,122880
SM s41,589824
M v[144:147],v[100:103],v[32:35],v[144:147]
T v[66:67],v24 offset:8704
)"
R"(B %2,s[72:75],s41 offen lds
M v[160:163],v[32:35],v[104:107],v[160:163]
R v[32:35],v4 offset:0
SA m0,s40,126976
M v[132:135],v[96:99],v[36:39],v[132:135]
T v[68:69],v25 offset:8192
SM s41,688128
M v[148:151],v[100:103],v[36:39],v[148:151]
)"
R"(T v[70:71],v25 offset:8704
B %2,s[72:75],s41 offen lds
SA m0,s40,65536
M v[164:167],v[36:39],v[104:107],v[164:167]
R v[36:39],v4 offset:2048
SM s41,384
M v[136:139],v[96:99],v[40:43],v[136:139]
T v[72:73],v26 offset:8192
)"
R"(B %0,s[52:55],s41 offen lds
M v[152:155],v[100:103],v[40:43],v[152:155]
T v[74:75],v26 offset:8704
SA m0,s40,69632
M v[168:171],v[40:43],v[104:107],v[168:171]
R v[40:43],v4 offset:4096
SM s41,33152
B %0,s[52:55],s41 offen lds
)"
R"(M v[140:143],v[96:99],v[44:47],v[140:143]
SA m0,s40,73728
M v[156:159],v[100:103],v[44:47],v[156:159]
SM s41,65920
M v[172:175],v[44:47],v[104:107],v[172:175]
R v[44:47],v4 offset:6144
B %0,s[52:55],s41 offen lds
W lgkmcnt(2)
)"
R"(M v[128:131],v[64:67],v[32:35],v[128:131]
T v[96:97],v24 offset:12288
SA m0,s40,77824
SM s41,98688
AR v176,a0
M v[144:147],v[68:71],v[32:35],v[144:147]
T v[98:99],v24 offset:12800
B %0,s[52:55],s41 offen lds
)"
R"(AR v177,a1
M v[160:163],v[32:35],v[72:75],v[160:163]
R v[32:35],v5 offset:0
SA m0,s40,81920
SM s41,131456
AR v178,a2
M v[132:135],v[64:67],v[36:39],v[132:135]
T v[100:101],v25 offset:12288
)"
R"(B %0,s[52:55],s41 offen lds
AR v179,a3
M v[148:151],v[68:71],v[36:39],v[148:151]
T v[102:103],v25 offset:12800
SA m0,s40,86016
AR v180,a4
M v[164:167],v[36:39],v[72:75],v[164:167]
R v[36:39],v5 offset:2048
)"
R"(SM s41,164224
B %0,s[52:55],s41 offen lds
AR v181,a5
W lgkmcnt(6)
M v[136:139],v[64:67],v[40:43],v[136:139]
T v[104:105],v26 offset:12288
SA m0,s40,90112
AR v182,a6
)"
R"(AR v183,a7
M v[152:155],v[68:71],v[40:43],v[152:155]
T v[106:107],v26 offset:12800
SM s41,196992
B %0,s[52:55],s41 offen lds
CV v176,v176,v177
M v[168:171],v[40:43],v[72:75],v[168:171]
R v[40:43],v5 offset:4096
)"
R"(SA m0,s40,94208
CV v177,v178,v179
M v[140:143],v[64:67],v[44:47],v[140:143]
SM s41,229760
CV v178,v180,v181
M v[156:159],v[68:71],v[44:47],v[156:159]
B %0,s[52:55],s41 offen lds
CV v179,v182,v183
)"
R"(M v[172:175],v[44:47],v[72:75],v[172:175]
R v[44:47],v5 offset:6144
GS v3,v[176:179],s[44:45] offset:0 sc1
W vmcnt(11) lgkmcnt(0)
s_barrier
M v[128:131],v[96:99],v[32:35],v[128:131]
T v[64:65],v24 offset:16384
SA m0,s40,98304
)"
R"(SM s41,786432
AR v188,a8
M v[144:147],v[100:103],v[32:35],v[144:147]
T v[66:67],v24 offset:16896
B %2,s[72:75],s41 offen lds
AR v189,a9
M v[160:163],v[32:35],v[104:107],v[160:163]
R v[32:35],v4 offset:32768
)"
R"(SA m0,s40,102400
AR v190,a10
M v[132:135],v[96:99],v[36:39],v[132:135]
T v[68:69],v25 offset:16384
SM s41,884736
AR v191,a11
M v[148:151],v[100:103],v[36:39],v[148:151]
T v[70:71],v25 offset:16896
)"
R"(B %2,s[72:75],s41 offen lds
SA m0,s40,0
AR v192,a12
M v[164:167],v[36:39],v[104:107],v[164:167]
R v[36:39],v4 offset:34816
SM s41,512
AR v193,a13
AR v194,a14
)"
R"(M v[136:139],v[96:99],v[40:43],v[136:139]
T v[72:73],v26 offset:16384
B %0,s[52:55],s41 offen lds
AR v195,a15
M v[152:155],v[100:103],v[40:43],v[152:155]
T v[74:75],v26 offset:16896
SA m0,s40,4096
CV v188,v188,v189
)"
R"(M v[168:171],v[40:43],v[104:107],v[168:171]
R v[40:43],v4 offset:36864
SM s41,33280
B %0,s[52:55],s41 offen lds
CV v189,v190,v191
M v[140:143],v[96:99],v[44:47],v[140:143]
SA m0,s40,8192
CV v190,v192,v193
)"
R"(M v[156:159],v[100:103],v[44:47],v[156:159]
SM s41,66048
CV v191,v194,v195
M v[172:175],v[44:47],v[104:107],v[172:175]
R v[44:47],v4 offset:38912
B %0,s[52:55],s41 offen lds
GS v3,v[188:191],s[44:45] offset:1024 sc1
W lgkmcnt(2)
)"
R"(M v[128:131],v[64:67],v[32:35],v[128:131]
T v[96:97],v24 offset:20480
SA m0,s40,12288
SM s41,98816
M v[144:147],v[68:71],v[32:35],v[144:147]
T v[98:99],v24 offset:20992
B %0,s[52:55],s41 offen lds
M v[160:163],v[32:35],v[72:75],v[160:163]
)"
R"(R v[32:35],v5 offset:32768
SA m0,s40,16384
SM s41,131584
M v[132:135],v[64:67],v[36:39],v[132:135]
T v[100:101],v25 offset:20480
B %0,s[52:55],s41 offen lds
M v[148:151],v[68:71],v[36:39],v[148:151]
T v[102:103],v25 offset:20992
)"
R"(SA m0,s40,20480
M v[164:167],v[36:39],v[72:75],v[164:167]
R v[36:39],v5 offset:34816
SM s41,164352
B %0,s[52:55],s41 offen lds
W lgkmcnt(6)
M v[136:139],v[64:67],v[40:43],v[136:139]
T v[104:105],v26 offset:20480
)"
R"(SA m0,s40,24576
M v[152:155],v[68:71],v[40:43],v[152:155]
T v[106:107],v26 offset:20992
SM s41,197120
B %0,s[52:55],s41 offen lds
M v[168:171],v[40:43],v[72:75],v[168:171]
R v[40:43],v5 offset:36864
SA m0,s40,28672
)"
R"(M v[140:143],v[64:67],v[44:47],v[140:143]
SM s41,229888
M v[156:159],v[68:71],v[44:47],v[156:159]
B %0,s[52:55],s41 offen lds
M v[172:175],v[44:47],v[72:75],v[172:175]
R v[44:47],v5 offset:38912
W vmcnt(12) lgkmcnt(0)
s_barrier
)"
R"(M v[128:131],v[96:99],v[32:35],v[128:131]
T v[64:65],v24 offset:24576
SA m0,s40,106496
SM s41,983040
AR v176,a16
M v[144:147],v[100:103],v[32:35],v[144:147]
T v[66:67],v24 offset:25088
B %2,s[72:75],s41 offen lds
)"
R"(AR v177,a17
M v[160:163],v[32:35],v[104:107],v[160:163]
R v[32:35],v6 offset:0
SA m0,s40,110592
AR v178,a18
M v[132:135],v[96:99],v[36:39],v[132:135]
T v[68:69],v25 offset:24576
SM s41,1081344
)"
R"(AR v179,a19
M v[148:151],v[100:103],v[36:39],v[148:151]
T v[70:71],v25 offset:25088
B %2,s[72:75],s41 offen lds
SA m0,s40,32768
AR v180,a20
M v[164:167],v[36:39],v[104:107],v[164:167]
R v[36:39],v6 offset:2048
)"
R"(SM s41,640
AR v181,a21
AR v182,a22
M v[136:139],v[96:99],v[40:43],v[136:139]
T v[72:73],v26 offset:24576
B %0,s[52:55],s41 offen lds
AR v183,a23
M v[152:155],v[100:103],v[40:43],v[152:155]
)"
R"(T v[74:75],v26 offset:25088
SA m0,s40,36864
CV v176,v176,v177
M v[168:171],v[40:43],v[104:107],v[168:171]
R v[40:43],v6 offset:4096
SM s41,33408
B %0,s[52:55],s41 offen lds
CV v177,v178,v179
)"
R"(M v[140:143],v[96:99],v[44:47],v[140:143]
SA m0,s40,40960
CV v178,v180,v181
M v[156:159],v[100:103],v[44:47],v[156:159]
SM s41,66176
CV v179,v182,v183
M v[172:175],v[44:47],v[104:107],v[172:175]
R v[44:47],v6 offset:6144
)"
R"(B %0,s[52:55],s41 offen lds
GS v3,v[176:179],s[44:45] offset:2048 sc1
W lgkmcnt(2)
M v[128:131],v[64:67],v[32:35],v[128:131]
T v[96:97],v24 offset:28672
SA m0,s40,45056
SM s41,98944
AR v188,a24
)"
R"(M v[144:147],v[68:71],v[32:35],v[144:147]
T v[98:99],v24 offset:29184
B %0,s[52:55],s41 offen lds
AR v189,a25
M v[160:163],v[32:35],v[72:75],v[160:163]
R v[32:35],v7 offset:0
SA m0,s40,49152
SM s41,131712
)"
R"(AR v190,a26
M v[132:135],v[64:67],v[36:39],v[132:135]
T v[100:101],v25 offset:28672
B %0,s[52:55],s41 offen lds
AR v191,a27
M v[148:151],v[68:71],v[36:39],v[148:151]
T v[102:103],v25 offset:29184
SA m0,s40,53248
)"
R"(AR v192,a28
M v[164:167],v[36:39],v[72:75],v[164:167]
R v[36:39],v7 offset:2048
SM s41,164480
B %0,s[52:55],s41 offen lds
AR v193,a29
W lgkmcnt(6)
M v[136:139],v[64:67],v[40:43],v[136:139]
)"
R"(T v[104:105],v26 offset:28672
SA m0,s40,57344
AR v194,a30
AR v195,a31
M v[152:155],v[68:71],v[40:43],v[152:155]
T v[106:107],v26 offset:29184
SM s41,197248
B %0,s[52:55],s41 offen lds
)"
R"(CV v188,v188,v189
M v[168:171],v[40:43],v[72:75],v[168:171]
R v[40:43],v7 offset:4096
SA m0,s40,61440
CV v189,v190,v191
M v[140:143],v[64:67],v[44:47],v[140:143]
SM s41,230016
CV v190,v192,v193
)"
R"(M v[156:159],v[68:71],v[44:47],v[156:159]
B %0,s[52:55],s41 offen lds
CV v191,v194,v195
M v[172:175],v[44:47],v[72:75],v[172:175]
R v[44:47],v7 offset:6144
GS v3,v[188:191],s[44:45] offset:3072 sc1
W vmcnt(12) lgkmcnt(0)
s_barrier
)"
R"(M v[128:131],v[96:99],v[32:35],v[128:131]
T v[64:65],v24 offset:0
SA m0,s40,114688
SM s41,1179648
M v[144:147],v[100:103],v[32:35],v[144:147]
T v[66:67],v24 offset:512
B %2,s[72:75],s41 offen lds
M v[160:163],v[32:35],v[104:107],v[160:163]
)"
R"(R v[32:35],v4 offset:0
SA m0,s40,118784
M v[132:135],v[96:99],v[36:39],v[132:135]
T v[68:69],v25 offset:0
SM s41,1277952
M v[148:151],v[100:103],v[36:39],v[148:151]
T v[70:71],v25 offset:512
B %2,s[72:75],s41 offen lds
)"
R"(SA m0,s40,65536
M v[164:167],v[36:39],v[104:107],v[164:167]
R v[36:39],v4 offset:2048
SM s41,768
M v[136:139],v[96:99],v[40:43],v[136:139]
T v[72:73],v26 offset:0
B %0,s[52:55],s41 offen lds
M v[152:155],v[100:103],v[40:43],v[152:155]
)"
R"(T v[74:75],v26 offset:512
SA m0,s40,69632
M v[168:171],v[40:43],v[104:107],v[168:171]
R v[40:43],v4 offset:4096
SM s41,33536
B %0,s[52:55],s41 offen lds
M v[140:143],v[96:99],v[44:47],v[140:143]
SA m0,s40,73728
)"
R"(M v[156:159],v[100:103],v[44:47],v[156:159]
SM s41,66304
M v[172:175],v[44:47],v[104:107],v[172:175]
R v[44:47],v4 offset:6144
B %0,s[52:55],s41 offen lds
W lgkmcnt(2)
M v[128:131],v[64:67],v[32:35],v[128:131]
T v[96:97],v24 offset:4096
)"
R"(SA m0,s40,77824
SM s41,99072
AR v176,a32
M v[144:147],v[68:71],v[32:35],v[144:147]
T v[98:99],v24 offset:4608
B %0,s[52:55],s41 offen lds
AR v177,a33
AR v178,a34
)"
R"(M v[160:163],v[32:35],v[72:75],v[160:163]
R v[32:35],v5 offset:0
SA m0,s40,81920
SM s41,131840
AR v179,a35
M v[132:135],v[64:67],v[36:39],v[132:135]
T v[100:101],v25 offset:4096
B %0,s[52:55],s41 offen lds
)"
R"(AR v180,a36
M v[148:151],v[68:71],v[36:39],v[148:151]
T v[102:103],v25 offset:4608
SA m0,s40,86016
AR v181,a37
M v[164:167],v[36:39],v[72:75],v[164:167]
R v[36:39],v5 offset:2048
SM s41,164608
)"
R"(B %0,s[52:55],s41 offen lds
AR v182,a38
W lgkmcnt(6)
M v[136:139],v[64:67],v[40:43],v[136:139]
T v[104:105],v26 offset:4096
SA m0,s40,90112
AR v183,a39
CV v176,v176,v177
)"
R"(M v[152:155],v[68:71],v[40:43],v[152:155]
T v[106:107],v26 offset:4608
SM s41,197376
B %0,s[52:55],s41 offen lds
CV v177,v178,v179
M v[168:171],v[40:43],v[72:75],v[168:171]
R v[40:43],v5 offset:4096
SA m0,s40,94208
)"
R"(CV v178,v180,v181
M v[140:143],v[64:67],v[44:47],v[140:143]
SM s41,230144
CV v179,v182,v183
SA s44,s44,0x1000
M v[156:159],v[68:71],v[44:47],v[156:159]
B %0,s[52:55],s41 offen lds
s_addc_u32 s45,s45,0
)"
R"(M v[172:175],v[44:47],v[72:75],v[172:175]
R v[44:47],v5 offset:6144
GS v3,v[176:179],s[44:45] offset:0 sc1
W vmcnt(12) lgkmcnt(0)
s_barrier
M v[128:131],v[96:99],v[32:35],v[128:131]
T v[64:65],v24 offset:8192
SA m0,s40,122880
)"
R"(SM s41,1376256
AR v188,a40
M v[144:147],v[100:103],v[32:35],v[144:147]
T v[66:67],v24 offset:8704
B %2,s[72:75],s41 offen lds
AR v189,a41
M v[160:163],v[32:35],v[104:107],v[160:163]
R v[32:35],v4 offset:32768
)"
R"(SA m0,s40,126976
AR v190,a42
M v[132:135],v[96:99],v[36:39],v[132:135]
T v[68:69],v25 offset:8192
SM s41,1474560
AR v191,a43
M v[148:151],v[100:103],v[36:39],v[148:151]
T v[70:71],v25 offset:8704
)"
R"(B %2,s[72:75],s41 offen lds
SA m0,s40,0
AR v192,a44
M v[164:167],v[36:39],v[104:107],v[164:167]
R v[36:39],v4 offset:34816
SM s41,896
AR v193,a45
AR v194,a46
)"
R"(M v[136:139],v[96:99],v[40:43],v[136:139]
T v[72:73],v26 offset:8192
B %0,s[52:55],s41 offen lds
AR v195,a47
M v[152:155],v[100:103],v[40:43],v[152:155]
T v[74:75],v26 offset:8704
SA m0,s40,4096
CV v188,v188,v189
)"
R"(M v[168:171],v[40:43],v[104:107],v[168:171]
R v[40:43],v4 offset:36864
SM s41,33664
B %0,s[52:55],s41 offen lds
CV v189,v190,v191
M v[140:143],v[96:99],v[44:47],v[140:143]
SA m0,s40,8192
CV v190,v192,v193
)"
R"(M v[156:159],v[100:103],v[44:47],v[156:159]
SM s41,66432
CV v191,v194,v195
M v[172:175],v[44:47],v[104:107],v[172:175]
R v[44:47],v4 offset:38912
B %0,s[52:55],s41 offen lds
GS v3,v[188:191],s[44:45] offset:1024 sc1
W lgkmcnt(2)
)"
R"(M v[128:131],v[64:67],v[32:35],v[128:131]
T v[96:97],v24 offset:12288
SA m0,s40,12288
SM s41,99200
M v[144:147],v[68:71],v[32:35],v[144:147]
T v[98:99],v24 offset:12800
B %0,s[52:55],s41 offen lds
M v[160:163],v[32:35],v[72:75],v[160:163]
)"
R"(R v[32:35],v5 offset:32768
SA m0,s40,16384
SM s41,131968
M v[132:135],v[64:67],v[36:39],v[132:135]
T v[100:101],v25 offset:12288
B %0,s[52:55],s41 offen lds
M v[148:151],v[68:71],v[36:39],v[148:151]
T v[102:103],v25 offset:12800
)"
R"(SA m0,s40,20480
M v[164:167],v[36:39],v[72:75],v[164:167]
R v[36:39],v5 offset:34816
SM s41,164736
B %0,s[52:55],s41 offen lds
W lgkmcnt(6)
M v[136:139],v[64:67],v[40:43],v[136:139]
T v[104:105],v26 offset:12288
)"
R"(SA m0,s40,24576
M v[152:155],v[68:71],v[40:43],v[152:155]
T v[106:107],v26 offset:12800
SM s41,197504
B %0,s[52:55],s41 offen lds
M v[168:171],v[40:43],v[72:75],v[168:171]
R v[40:43],v5 offset:36864
SA m0,s40,28672
)"
R"(M v[140:143],v[64:67],v[44:47],v[140:143]
SM s41,230272
M v[156:159],v[68:71],v[44:47],v[156:159]
B %0,s[52:55],s41 offen lds
M v[172:175],v[44:47],v[72:75],v[172:175]
R v[44:47],v5 offset:38912
W vmcnt(12) lgkmcnt(0)
s_barrier
)"
R"(M v[128:131],v[96:99],v[32:35],v[128:131]
T v[64:65],v24 offset:16384
SA m0,s40,131072
SM s41,0
AR v176,a48
M v[144:147],v[100:103],v[32:35],v[144:147]
T v[66:67],v24 offset:16896
B %1,s[68:71],s41 offen lds
)"
R"(AR v177,a49
M v[160:163],v[32:35],v[104:107],v[160:163]
R v[32:35],v6 offset:0
SA m0,s40,135168
SM s41,131072
AR v178,a50
M v[132:135],v[96:99],v[36:39],v[132:135]
T v[68:69],v25 offset:16384
)"
R"(B %1,s[68:71],s41 offen lds
AR v179,a51
M v[148:151],v[100:103],v[36:39],v[148:151]
T v[70:71],v25 offset:16896
SA m0,s40,139264
SM s41,262144
AR v180,a52
M v[164:167],v[36:39],v[104:107],v[164:167]
)"
R"(R v[36:39],v6 offset:2048
B %1,s[68:71],s41 offen lds
AR v181,a53
AR v182,a54
M v[136:139],v[96:99],v[40:43],v[136:139]
T v[72:73],v26 offset:16384
SA m0,s40,143360
SM s41,393216
)"
R"(AR v183,a55
M v[152:155],v[100:103],v[40:43],v[152:155]
T v[74:75],v26 offset:16896
B %1,s[68:71],s41 offen lds
CV v176,v176,v177
M v[168:171],v[40:43],v[104:107],v[168:171]
R v[40:43],v6 offset:4096
SA m0,s40,32768
)"
R"(SA s41,s76,0
CV v177,v178,v179
M v[140:143],v[96:99],v[44:47],v[140:143]
B %0,s[56:59],s41 offen lds
CV v178,v180,v181
M v[156:159],v[100:103],v[44:47],v[156:159]
SA m0,s40,36864
SA s41,s76,32768
)"
R"(CV v179,v182,v183
M v[172:175],v[44:47],v[104:107],v[172:175]
R v[44:47],v6 offset:6144
B %0,s[56:59],s41 offen lds
GS v3,v[176:179],s[44:45] offset:2048 sc1
W lgkmcnt(2)
M v[128:131],v[64:67],v[32:35],v[128:131]
T v[96:97],v24 offset:20480
)"
R"(SA m0,s40,40960
SA s41,s76,65536
AR v188,a56
M v[144:147],v[68:71],v[32:35],v[144:147]
T v[98:99],v24 offset:20992
B %0,s[56:59],s41 offen lds
SA m0,s40,45056
AR v189,a57
)"
R"(M v[160:163],v[32:35],v[72:75],v[160:163]
R v[32:35],v7 offset:0
SA s41,s76,98304
AR v190,a58
M v[132:135],v[64:67],v[36:39],v[132:135]
T v[100:101],v25 offset:20480
B %0,s[56:59],s41 offen lds
SA m0,s40,49152
)"
R"(AR v191,a59
M v[148:151],v[68:71],v[36:39],v[148:151]
T v[102:103],v25 offset:20992
SA s41,s77,0
B %0,s[56:59],s41 offen lds
AR v192,a60
M v[164:167],v[36:39],v[72:75],v[164:167]
R v[36:39],v7 offset:2048
)"
R"(SA m0,s40,53248
AR v193,a61
AR v194,a62
W lgkmcnt(6)
M v[136:139],v[64:67],v[40:43],v[136:139]
T v[104:105],v26 offset:20480
SA s41,s77,32768
B %0,s[56:59],s41 offen lds
)"
R"(AR v195,a63
M v[152:155],v[68:71],v[40:43],v[152:155]
T v[106:107],v26 offset:20992
SA m0,s40,57344
SA s41,s77,65536
CV v188,v188,v189
M v[168:171],v[40:43],v[72:75],v[168:171]
R v[40:43],v7 offset:4096
)"
R"(B %0,s[56:59],s41 offen lds
CV v189,v190,v191
M v[140:143],v[64:67],v[44:47],v[140:143]
SA m0,s40,61440
SA s41,s77,98304
CV v190,v192,v193
M v[156:159],v[68:71],v[44:47],v[156:159]
B %0,s[56:59],s41 offen lds
)"
R"(CV v191,v194,v195
M v[172:175],v[44:47],v[72:75],v[172:175]
R v[44:47],v7 offset:6144
GS v3,v[188:191],s[44:45] offset:3072 sc1
W vmcnt(14) lgkmcnt(0)
s_barrier
M v[128:131],v[96:99],v[32:35],v[128:131]
T v[64:65],v24 offset:24576
)"
R"(SA m0,s40,147456
SM s41,524288
M v[144:147],v[100:103],v[32:35],v[144:147]
T v[66:67],v24 offset:25088
B %1,s[68:71],s41 offen lds
M v[160:163],v[32:35],v[104:107],v[160:163]
R v[32:35],v4 offset:0
SA m0,s40,151552
)"
R"(SM s41,655360
M v[132:135],v[96:99],v[36:39],v[132:135]
T v[68:69],v25 offset:24576
B %1,s[68:71],s41 offen lds
M v[148:151],v[100:103],v[36:39],v[148:151]
T v[70:71],v25 offset:25088
SA m0,s40,155648
SM s41,786432
)"
R"(M v[164:167],v[36:39],v[104:107],v[164:167]
R v[36:39],v4 offset:2048
B %1,s[68:71],s41 offen lds
M v[136:139],v[96:99],v[40:43],v[136:139]
T v[72:73],v26 offset:24576
SA m0,s40,159744
SM s41,917504
M v[152:155],v[100:103],v[40:43],v[152:155]
)"
R"(T v[74:75],v26 offset:25088
B %1,s[68:71],s41 offen lds
M v[168:171],v[40:43],v[104:107],v[168:171]
R v[40:43],v4 offset:4096
SA m0,s40,65536
SA s41,s76,128
M v[140:143],v[96:99],v[44:47],v[140:143]
B %0,s[56:59],s41 offen lds
)"
R"(M v[156:159],v[100:103],v[44:47],v[156:159]
SA m0,s40,69632
SA s41,s76,32896
M v[172:175],v[44:47],v[104:107],v[172:175]
R v[44:47],v4 offset:6144
B %0,s[56:59],s41 offen lds
W lgkmcnt(2)
M v[128:131],v[64:67],v[32:35],v[128:131]
)"
R"(T v[96:97],v24 offset:28672
SA m0,s40,73728
SA s41,s76,65664
AR v176,a64
M v[144:147],v[68:71],v[32:35],v[144:147]
T v[98:99],v24 offset:29184
B %0,s[56:59],s41 offen lds
SA m0,s40,77824
)"
R"(AR v177,a65
M v[160:163],v[32:35],v[72:75],v[160:163]
R v[32:35],v5 offset:0
SA s41,s76,98432
AR v178,a66
AR v179,a67
M v[132:135],v[64:67],v[36:39],v[132:135]
T v[100:101],v25 offset:28672
)"
R"(B %0,s[56:59],s41 offen lds
SA m0,s40,81920
AR v180,a68
M v[148:151],v[68:71],v[36:39],v[148:151]
T v[102:103],v25 offset:29184
SA s41,s77,128
B %0,s[56:59],s41 offen lds
AR v181,a69
)"
R"(M v[164:167],v[36:39],v[72:75],v[164:167]
R v[36:39],v5 offset:2048
SA m0,s40,86016
AR v182,a70
AR v183,a71
W lgkmcnt(6)
M v[136:139],v[64:67],v[40:43],v[136:139]
T v[104:105],v26 offset:28672
)"
R"(SA s41,s77,32896
B %0,s[56:59],s41 offen lds
CV v176,v176,v177
M v[152:155],v[68:71],v[40:43],v[152:155]
T v[106:107],v26 offset:29184
SA m0,s40,90112
SA s41,s77,65664
CV v177,v178,v179
)"
R"(M v[168:171],v[40:43],v[72:75],v[168:171]
R v[40:43],v5 offset:4096
B %0,s[56:59],s41 offen lds
CV v178,v180,v181
M v[140:143],v[64:67],v[44:47],v[140:143]
SA m0,s40,94208
SA s41,s77,98432
CV v179,v182,v183
)"
R"(SA s44,s44,0x1000
M v[156:159],v[68:71],v[44:47],v[156:159]
B %0,s[56:59],s41 offen lds
s_addc_u32 s45,s45,0
M v[172:175],v[44:47],v[72:75],v[172:175]
R v[44:47],v5 offset:6144
GS v3,v[176:179],s[44:45] offset:0 sc1
W lgkmcnt(2)
)"
R"(M v[128:131],v[96:99],v[32:35],v[128:131]
AR v188,a72
M v[144:147],v[100:103],v[32:35],v[144:147]
AR v189,a73
M v[160:163],v[32:35],v[104:107],v[160:163]
AR v190,a74
M v[132:135],v[96:99],v[36:39],v[132:135]
AR v191,a75
)"
R"(M v[148:151],v[100:103],v[36:39],v[148:151]
AR v192,a76
M v[164:167],v[36:39],v[104:107],v[164:167]
AR v193,a77
AR v194,a78
W lgkmcnt(0)
M v[136:139],v[96:99],v[40:43],v[136:139]
AR v195,a79
)"
R"(M v[152:155],v[100:103],v[40:43],v[152:155]
CV v188,v188,v189
M v[168:171],v[40:43],v[104:107],v[168:171]
CV v189,v190,v191
M v[140:143],v[96:99],v[44:47],v[140:143]
CV v190,v192,v193
M v[156:159],v[100:103],v[44:47],v[156:159]
CV v191,v194,v195
)"
R"(M v[172:175],v[44:47],v[104:107],v[172:175]
GS v3,v[188:191],s[44:45] offset:1024 sc1
v_lshl_or_b32 v216,v209,16,v208
v_lshl_or_b32 v217,v211,16,v210
v_mov_b32 v218,0
v_mov_b32 v219,0
v_mov_b32 v220,0
v_mov_b32 v221,0
)"
R"(v_mov_b32 v222,v216
v_mov_b32 v223,v217
s_nop 7
s_nop 1
CV v32,v128,v129
AR v232,a80
CV v33,v130,v131
CV v34,v144,v145
)"
R"(CV v35,v146,v147
CV v36,v132,v133
AR v233,a81
CV v37,v134,v135
CV v38,v148,v149
CV v39,v150,v151
CV v40,v136,v137
CV v41,v138,v139
)"
R"(AR v234,a82
CV v42,v152,v153
CV v43,v154,v155
CV v44,v140,v141
CV v45,v142,v143
AR v235,a83
CV v46,v156,v157
CV v47,v158,v159
)"
R"(CV v48,v160,v161
CV v49,v162,v163
CV v50,v164,v165
AR v236,a84
CV v51,v166,v167
CV v52,v168,v169
CV v53,v170,v171
CV v54,v172,v173
)"
R"(CV v55,v174,v175
AR v237,a85
M v[64:67],v[216:219],v[32:35],0
M v[68:71],v[220:223],v[32:35],0
M v[72:75],v[216:219],v[36:39],0
M v[76:79],v[220:223],v[36:39],0
AR v238,a86
M v[80:83],v[216:219],v[40:43],0
)"
R"(M v[84:87],v[220:223],v[40:43],0
M v[88:91],v[216:219],v[44:47],0
M v[92:95],v[220:223],v[44:47],0
MX v208,v64,v65,v66
AR v239,a87
MX v209,v68,v69,v70
MX v210,v72,v73,v74
MX v211,v76,v77,v78
)"
R"(MX v212,v80,v81,v82
MX v213,v84,v85,v86
CV v232,v232,v233
MX v214,v88,v89,v90
MX v215,v92,v93,v94
v_min3_f32 v216,v64,v65,v66
v_min3_f32 v217,v68,v69,v70
CV v233,v234,v235
)"
R"(v_min3_f32 v218,v72,v73,v74
v_min3_f32 v219,v76,v77,v78
v_min3_f32 v220,v80,v81,v82
v_min3_f32 v221,v84,v85,v86
v_min3_f32 v222,v88,v89,v90
CV v234,v236,v237
v_min3_f32 v223,v92,v93,v94
v_max_f32 v208,v208,v67
)"
R"(v_max_f32 v209,v209,v71
v_max_f32 v210,v210,v75
v_max_f32 v211,v211,v79
CV v235,v238,v239
v_max_f32 v212,v212,v83
v_max_f32 v213,v213,v87
v_max_f32 v214,v214,v91
v_max_f32 v215,v215,v95
)"
R"(GS v3,v[232:235],s[44:45] offset:2048 sc1
v_min_f32 v216,v216,v67
v_min_f32 v217,v217,v71
v_min_f32 v218,v218,v75
v_min_f32 v219,v219,v79
v_min_f32 v220,v220,v83
AR v232,a88
v_min_f32 v221,v221,v87
)"
R"(v_min_f32 v222,v222,v91
v_min_f32 v223,v223,v95
v_mov_b32 v224,v208
v_mov_b32 v225,v209
AR v233,a89
v_mov_b32 v226,v210
v_mov_b32 v227,v211
v_mov_b32 v228,v212
)"
R"(v_mov_b32 v229,v213
AR v234,a90
v_mov_b32 v230,v214
v_mov_b32 v231,v215
v_permlane32_swap_b32 v208,v224
v_permlane32_swap_b32 v209,v225
v_permlane32_swap_b32 v210,v226
AR v235,a91
)"
R"(v_permlane32_swap_b32 v211,v227
v_permlane32_swap_b32 v212,v228
v_permlane32_swap_b32 v213,v229
v_permlane32_swap_b32 v214,v230
v_permlane32_swap_b32 v215,v231
AR v236,a92
v_max_f32 v208,v208,v224
v_max_f32 v209,v209,v225
)"
R"(v_max_f32 v210,v210,v226
v_max_f32 v211,v211,v227
AR v237,a93
v_max_f32 v212,v212,v228
v_max_f32 v213,v213,v229
v_max_f32 v214,v214,v230
v_max_f32 v215,v215,v231
v_mov_b32 v224,v208
)"
R"(AR v238,a94
v_mov_b32 v225,v209
v_mov_b32 v226,v210
v_mov_b32 v227,v211
v_mov_b32 v228,v212
v_mov_b32 v229,v213
AR v239,a95
v_mov_b32 v230,v214
)"
R"(v_mov_b32 v231,v215
v_permlane16_swap_b32 v208,v224
v_permlane16_swap_b32 v209,v225
CV v232,v232,v233
v_permlane16_swap_b32 v210,v226
v_permlane16_swap_b32 v211,v227
v_permlane16_swap_b32 v212,v228
v_permlane16_swap_b32 v213,v229
)"
R"(v_permlane16_swap_b32 v214,v230
CV v233,v234,v235
v_permlane16_swap_b32 v215,v231
v_max_f32 v208,v208,v224
v_max_f32 v209,v209,v225
v_max_f32 v210,v210,v226
v_max_f32 v211,v211,v227
CV v234,v236,v237
)"
R"(v_max_f32 v212,v212,v228
v_max_f32 v213,v213,v229
v_max_f32 v214,v214,v230
v_max_f32 v215,v215,v231
CV v235,v238,v239
v_mov_b32 v224,v216
v_mov_b32 v225,v217
v_mov_b32 v226,v218
)"
R"(v_mov_b32 v227,v219
v_mov_b32 v228,v220
GS v3,v[232:235],s[44:45] offset:3072 sc1
v_mov_b32 v229,v221
v_mov_b32 v230,v222
v_mov_b32 v231,v223
v_permlane32_swap_b32 v216,v224
v_permlane32_swap_b32 v217,v225
)"
R"(AR v232,a96
v_permlane32_swap_b32 v218,v226
v_permlane32_swap_b32 v219,v227
v_permlane32_swap_b32 v220,v228
v_permlane32_swap_b32 v221,v229
AR v233,a97
v_permlane32_swap_b32 v222,v230
v_permlane32_swap_b32 v223,v231
)"
R"(v_min_f32 v216,v216,v224
v_min_f32 v217,v217,v225
v_min_f32 v218,v218,v226
AR v234,a98
v_min_f32 v219,v219,v227
v_min_f32 v220,v220,v228
v_min_f32 v221,v221,v229
v_min_f32 v222,v222,v230
)"
R"(v_min_f32 v223,v223,v231
AR v235,a99
v_mov_b32 v224,v216
v_mov_b32 v225,v217
v_mov_b32 v226,v218
v_mov_b32 v227,v219
AR v236,a100
v_mov_b32 v228,v220
)"
R"(v_mov_b32 v229,v221
v_mov_b32 v230,v222
v_mov_b32 v231,v223
v_permlane16_swap_b32 v216,v224
AR v237,a101
v_permlane16_swap_b32 v217,v225
v_permlane16_swap_b32 v218,v226
v_permlane16_swap_b32 v219,v227
)"
R"(v_permlane16_swap_b32 v220,v228
v_permlane16_swap_b32 v221,v229
AR v238,a102
v_permlane16_swap_b32 v222,v230
v_permlane16_swap_b32 v223,v231
v_min_f32 v216,v216,v224
v_min_f32 v217,v217,v225
AR v239,a103
)"
R"(v_min_f32 v218,v218,v226
v_min_f32 v219,v219,v227
v_min_f32 v220,v220,v228
v_min_f32 v221,v221,v229
v_min_f32 v222,v222,v230
CV v232,v232,v233
v_min_f32 v223,v223,v231
MU v208,0xbfb8aa3b,v208
)"
R"(MU v209,0xbfb8aa3b,v209
MU v210,0xbfb8aa3b,v210
MU v211,0xbfb8aa3b,v211
CV v233,v234,v235
MU v212,0xbfb8aa3b,v212
MU v213,0xbfb8aa3b,v213
MU v214,0xbfb8aa3b,v214
MU v215,0xbfb8aa3b,v215
)"
R"(CV v234,v236,v237
MU v216,0x3fb8aa3b,v216
MU v217,0x3fb8aa3b,v217
MU v218,0x3fb8aa3b,v218
MU v219,0x3fb8aa3b,v219
MU v220,0x3fb8aa3b,v220
CV v235,v238,v239
MU v221,0x3fb8aa3b,v221
)"
R"(MU v222,0x3fb8aa3b,v222
MU v223,0x3fb8aa3b,v223
v_fma_f32 v96,v64,v30,v208
v_fma_f32 v100,v68,v30,v209
SA s44,s44,0x1000
v_fma_f32 v104,v72,v30,v210
v_fma_f32 v108,v76,v30,v211
v_fma_f32 v112,v80,v30,v212
)"
R"(v_fma_f32 v116,v84,v30,v213
s_addc_u32 s45,s45,0
v_fma_f32 v120,v88,v30,v214
v_fma_f32 v124,v92,v30,v215
v_fma_f32 v176,-v64,v30,v216
v_fma_f32 v180,-v68,v30,v217
v_fma_f32 v184,-v72,v30,v218
GS v3,v[232:235],s[44:45] offset:0 sc1
)"
R"(v_fma_f32 v188,-v76,v30,v219
v_fma_f32 v192,-v80,v30,v220
v_fma_f32 v196,-v84,v30,v221
v_fma_f32 v200,-v88,v30,v222
v_fma_f32 v204,-v92,v30,v223
AR v232,a104
v_fma_f32 v97,v65,v30,v208
v_fma_f32 v101,v69,v30,v209
)"
R"(v_fma_f32 v105,v73,v30,v210
v_fma_f32 v109,v77,v30,v211
AR v233,a105
v_fma_f32 v113,v81,v30,v212
v_fma_f32 v117,v85,v30,v213
v_fma_f32 v121,v89,v30,v214
v_fma_f32 v125,v93,v30,v215
v_fma_f32 v177,-v65,v30,v216
)"
R"(AR v234,a106
v_fma_f32 v181,-v69,v30,v217
v_fma_f32 v185,-v73,v30,v218
v_fma_f32 v189,-v77,v30,v219
v_fma_f32 v193,-v81,v30,v220
v_fma_f32 v197,-v85,v30,v221
AR v235,a107
v_fma_f32 v201,-v89,v30,v222
)"
R"(v_fma_f32 v205,-v93,v30,v223
v_fma_f32 v98,v66,v30,v208
v_fma_f32 v102,v70,v30,v209
AR v236,a108
v_fma_f32 v106,v74,v30,v210
v_fma_f32 v110,v78,v30,v211
v_fma_f32 v114,v82,v30,v212
v_fma_f32 v118,v86,v30,v213
)"
R"(v_fma_f32 v122,v90,v30,v214
AR v237,a109
v_fma_f32 v126,v94,v30,v215
v_fma_f32 v178,-v66,v30,v216
v_fma_f32 v182,-v70,v30,v217
v_fma_f32 v186,-v74,v30,v218
v_fma_f32 v190,-v78,v30,v219
AR v238,a110
)"
R"(v_fma_f32 v194,-v82,v30,v220
v_fma_f32 v198,-v86,v30,v221
v_fma_f32 v202,-v90,v30,v222
v_fma_f32 v206,-v94,v30,v223
AR v239,a111
v_fma_f32 v99,v67,v30,v208
v_fma_f32 v103,v71,v30,v209
v_fma_f32 v107,v75,v30,v210
)"
R"(v_fma_f32 v111,v79,v30,v211
v_fma_f32 v115,v83,v30,v212
CV v232,v232,v233
v_fma_f32 v119,v87,v30,v213
v_fma_f32 v123,v91,v30,v214
v_fma_f32 v127,v95,v30,v215
v_fma_f32 v179,-v67,v30,v216
v_fma_f32 v183,-v71,v30,v217
)"
R"(CV v233,v234,v235
v_fma_f32 v187,-v75,v30,v218
v_fma_f32 v191,-v79,v30,v219
v_fma_f32 v195,-v83,v30,v220
v_fma_f32 v199,-v87,v30,v221
CV v234,v236,v237
v_fma_f32 v203,-v91,v30,v222
v_fma_f32 v207,-v95,v30,v223
)"
R"(EX v96,v96
EX v100,v100
EX v104,v104
CV v235,v238,v239
EX v108,v108
EX v112,v112
EX v116,v116
EX v120,v120
)"
R"(EX v124,v124
GS v3,v[232:235],s[44:45] offset:1024 sc1
EX v176,v176
EX v180,v180
EX v184,v184
EX v188,v188
AR v232,a112
EX v192,v192
)"
R"(EX v196,v196
EX v200,v200
EX v204,v204
EX v97,v97
AR v233,a113
EX v101,v101
EX v105,v105
EX v109,v109
)"
R"(EX v113,v113
EX v117,v117
AR v234,a114
EX v121,v121
EX v125,v125
EX v177,v177
EX v181,v181
AR v235,a115
)"
R"(EX v185,v185
EX v189,v189
EX v193,v193
EX v197,v197
EX v201,v201
AR v236,a116
EX v205,v205
EX v98,v98
)"
R"(EX v102,v102
EX v106,v106
EX v110,v110
AR v237,a117
EX v114,v114
EX v118,v118
EX v122,v122
EX v126,v126
)"
R"(AR v238,a118
EX v178,v178
EX v182,v182
EX v186,v186
EX v190,v190
EX v194,v194
AR v239,a119
EX v198,v198
)"
R"(EX v202,v202
EX v206,v206
EX v99,v99
CV v232,v232,v233
EX v103,v103
EX v107,v107
EX v111,v111
EX v115,v115
)"
R"(EX v119,v119
CV v233,v234,v235
EX v123,v123
EX v127,v127
EX v179,v179
EX v183,v183
EX v187,v187
CV v234,v236,v237
)"
R"(EX v191,v191
EX v195,v195
EX v199,v199
EX v203,v203
CV v235,v238,v239
EX v207,v207
AD v208,v96,v97
AD v209,v100,v101
)"
R"(AD v210,v104,v105
AD v211,v108,v109
GS v3,v[232:235],s[44:45] offset:2048 sc1
AD v212,v112,v113
AD v213,v116,v117
AD v214,v120,v121
AD v215,v124,v125
AD v216,v176,v177
)"
R"(AR v232,a120
AD v217,v180,v181
AD v218,v184,v185
AD v219,v188,v189
AD v220,v192,v193
AR v233,a121
AD v221,v196,v197
AD v222,v200,v201
)"
R"(AD v223,v204,v205
AD v208,v208,v98
AD v209,v209,v102
AR v234,a122
AD v210,v210,v106
AD v211,v211,v110
AD v212,v212,v114
AD v213,v213,v118
)"
R"(AD v214,v214,v122
AR v235,a123
AD v215,v215,v126
AD v216,v216,v178
AD v217,v217,v182
AD v218,v218,v186
AR v236,a124
AD v219,v219,v190
)"
R"(AD v220,v220,v194
AD v221,v221,v198
AD v222,v222,v202
AD v223,v223,v206
AR v237,a125
AD v208,v208,v99
AD v209,v209,v103
AD v210,v210,v107
)"
R"(AD v211,v211,v111
AD v212,v212,v115
AR v238,a126
AD v213,v213,v119
AD v214,v214,v123
AD v215,v215,v127
AD v216,v216,v179
AR v239,a127
)"
R"(AD v217,v217,v183
AD v218,v218,v187
AD v219,v219,v191
AD v220,v220,v195
AD v221,v221,v199
CV v232,v232,v233
AD v222,v222,v203
AD v223,v223,v207
)"
R"(v_mov_b32 v224,v208
v_mov_b32 v225,v209
v_mov_b32 v226,v210
CV v233,v234,v235
v_mov_b32 v227,v211
v_mov_b32 v228,v212
v_mov_b32 v229,v213
v_mov_b32 v230,v214
)"
R"(CV v234,v236,v237
v_mov_b32 v231,v215
v_permlane32_swap_b32 v208,v224
v_permlane32_swap_b32 v209,v225
v_permlane32_swap_b32 v210,v226
v_permlane32_swap_b32 v211,v227
CV v235,v238,v239
v_permlane32_swap_b32 v212,v228
)"
R"(v_permlane32_swap_b32 v213,v229
v_permlane32_swap_b32 v214,v230
v_permlane32_swap_b32 v215,v231
AD v208,v208,v224
GS v3,v[232:235],s[44:45] offset:3072 sc1
AD v209,v209,v225
AD v210,v210,v226
AD v211,v211,v227
)"
R"(AD v212,v212,v228
AR v232,a128
AD v213,v213,v229
AD v214,v214,v230
AD v215,v215,v231
v_mov_b32 v224,v208
v_mov_b32 v225,v209
AR v233,a129
)"
R"(v_mov_b32 v226,v210
v_mov_b32 v227,v211
v_mov_b32 v228,v212
v_mov_b32 v229,v213
v_mov_b32 v230,v214
AR v234,a130
v_mov_b32 v231,v215
v_permlane16_swap_b32 v208,v224
)"
R"(v_permlane16_swap_b32 v209,v225
v_permlane16_swap_b32 v210,v226
AR v235,a131
v_permlane16_swap_b32 v211,v227
v_permlane16_swap_b32 v212,v228
v_permlane16_swap_b32 v213,v229
v_permlane16_swap_b32 v214,v230
v_permlane16_swap_b32 v215,v231
)"
R"(AR v236,a132
AD v208,v208,v224
AD v209,v209,v225
AD v210,v210,v226
AD v211,v211,v227
AD v212,v212,v228
AR v237,a133
AD v213,v213,v229
)"
R"(AD v214,v214,v230
AD v215,v215,v231
v_mov_b32 v224,v216
AR v238,a134
v_mov_b32 v225,v217
v_mov_b32 v226,v218
v_mov_b32 v227,v219
v_mov_b32 v228,v220
)"
R"(v_mov_b32 v229,v221
AR v239,a135
v_mov_b32 v230,v222
v_mov_b32 v231,v223
v_permlane32_swap_b32 v216,v224
v_permlane32_swap_b32 v217,v225
v_permlane32_swap_b32 v218,v226
CV v232,v232,v233
)"
R"(v_permlane32_swap_b32 v219,v227
v_permlane32_swap_b32 v220,v228
v_permlane32_swap_b32 v221,v229
v_permlane32_swap_b32 v222,v230
CV v233,v234,v235
v_permlane32_swap_b32 v223,v231
AD v216,v216,v224
AD v217,v217,v225
)"
R"(AD v218,v218,v226
AD v219,v219,v227
CV v234,v236,v237
AD v220,v220,v228
AD v221,v221,v229
AD v222,v222,v230
AD v223,v223,v231
v_mov_b32 v224,v216
)"
R"(CV v235,v238,v239
v_mov_b32 v225,v217
v_mov_b32 v226,v218
v_mov_b32 v227,v219
v_mov_b32 v228,v220
SA s44,s44,0x1000
v_mov_b32 v229,v221
v_mov_b32 v230,v222
)"
R"(v_mov_b32 v231,v223
v_permlane16_swap_b32 v216,v224
v_permlane16_swap_b32 v217,v225
s_addc_u32 s45,s45,0
v_permlane16_swap_b32 v218,v226
v_permlane16_swap_b32 v219,v227
v_permlane16_swap_b32 v220,v228
v_permlane16_swap_b32 v221,v229
)"
R"(v_permlane16_swap_b32 v222,v230
GS v3,v[232:235],s[44:45] offset:0 sc1
v_permlane16_swap_b32 v223,v231
AD v216,v216,v224
AD v217,v217,v225
AD v218,v218,v226
AR v232,a136
AD v219,v219,v227
)"
R"(AD v220,v220,v228
AD v221,v221,v229
AD v222,v222,v230
AD v223,v223,v231
AR v233,a137
v_rcp_f32 v208,v208
v_rcp_f32 v209,v209
v_rcp_f32 v210,v210
)"
R"(v_rcp_f32 v211,v211
v_rcp_f32 v212,v212
AR v234,a138
v_rcp_f32 v213,v213
v_rcp_f32 v214,v214
v_rcp_f32 v215,v215
v_rcp_f32 v216,v216
AR v235,a139
)"
R"(v_rcp_f32 v217,v217
v_rcp_f32 v218,v218
v_rcp_f32 v219,v219
v_rcp_f32 v220,v220
v_rcp_f32 v221,v221
AR v236,a140
v_rcp_f32 v222,v222
v_rcp_f32 v223,v223
)"
R"(MU v96,v96,v208
MU v100,v100,v209
MU v104,v104,v210
AR v237,a141
MU v108,v108,v211
MU v112,v112,v212
MU v116,v116,v213
MU v120,v120,v214
)"
R"(AR v238,a142
MU v124,v124,v215
MU v176,v176,v216
MU v180,v180,v217
MU v184,v184,v218
MU v188,v188,v219
AR v239,a143
MU v192,v192,v220
)"
R"(MU v196,v196,v221
MU v200,v200,v222
MU v204,v204,v223
MU v97,v97,v208
CV v232,v232,v233
MU v101,v101,v209
MU v105,v105,v210
MU v109,v109,v211
)"
R"(MU v113,v113,v212
CV v233,v234,v235
MU v117,v117,v213
MU v121,v121,v214
MU v125,v125,v215
MU v177,v177,v216
MU v181,v181,v217
CV v234,v236,v237
)"
R"(MU v185,v185,v218
MU v189,v189,v219
MU v193,v193,v220
MU v197,v197,v221
MU v201,v201,v222
CV v235,v238,v239
MU v205,v205,v223
MU v98,v98,v208
)"
R"(MU v102,v102,v209
MU v106,v106,v210
GS v3,v[232:235],s[44:45] offset:1024 sc1
MU v110,v110,v211
MU v114,v114,v212
MU v118,v118,v213
MU v122,v122,v214
MU v126,v126,v215
)"
R"(AR v232,a144
MU v178,v178,v216
MU v182,v182,v217
MU v186,v186,v218
MU v190,v190,v219
MU v194,v194,v220
AR v233,a145
MU v198,v198,v221
)"
R"(MU v202,v202,v222
MU v206,v206,v223
MU v99,v99,v208
AR v234,a146
MU v103,v103,v209
MU v107,v107,v210
MU v111,v111,v211
MU v115,v115,v212
)"
R"(MU v119,v119,v213
AR v235,a147
MU v123,v123,v214
MU v127,v127,v215
MU v179,v179,v216
MU v183,v183,v217
MU v187,v187,v218
AR v236,a148
)"
R"(MU v191,v191,v219
MU v195,v195,v220
MU v199,v199,v221
MU v203,v203,v222
AR v237,a149
MU v207,v207,v223
CV v64,v96,v97
CV v68,v100,v101
)"
R"(CV v72,v104,v105
CV v76,v108,v109
AR v238,a150
CV v80,v112,v113
CV v84,v116,v117
CV v88,v120,v121
CV v92,v124,v125
CV v66,v176,v177
)"
R"(AR v239,a151
CV v70,v180,v181
CV v74,v184,v185
CV v78,v188,v189
CV v82,v192,v193
CV v232,v232,v233
CV v86,v196,v197
CV v90,v200,v201
)"
R"(CV v94,v204,v205
CV v65,v98,v99
CV v69,v102,v103
CV v233,v234,v235
CV v73,v106,v107
CV v77,v110,v111
CV v81,v114,v115
CV v85,v118,v119
)"
R"(CV v89,v122,v123
CV v234,v236,v237
CV v93,v126,v127
CV v67,v178,v179
CV v71,v182,v183
CV v75,v186,v187
CV v235,v238,v239
CV v79,v190,v191
)"
R"(CV v83,v194,v195
CV v87,v198,v199
CV v91,v202,v203
CV v95,v206,v207
GS v3,v[232:235],s[44:45] offset:2048 sc1
GS v3,v[64:67],s[78:79] offset:0
GS v3,v[72:75],s[78:79] offset:1024
GS v3,v[80:83],s[78:79] offset:2048
)"
R"(GS v3,v[88:91],s[78:79] offset:3072
ds_write_b64 v10,v[68:69] offset:0
ds_write_b64 v10,v[70:71] offset:2048
ds_write_b64 v10,v[76:77] offset:512
ds_write_b64 v10,v[78:79] offset:2560
ds_write_b64 v10,v[84:85] offset:1024
ds_write_b64 v10,v[86:87] offset:3072
ds_write_b64 v10,v[92:93] offset:1536
)"
R"(ds_write_b64 v10,v[94:95] offset:3584
W lgkmcnt(0)
T v[96:97],v11 offset:0
T v[98:99],v11 offset:512
T v[100:101],v11 offset:2048
T v[102:103],v11 offset:2560
T v[104:105],v11 offset:1024
T v[106:107],v11 offset:1536
)"
R"(T v[108:109],v11 offset:3072
T v[110:111],v11 offset:3584
v_mov_b32 v112,0x3f803f80
v_mov_b32 v113,0x3f803f80
v_mov_b32 v114,0x3f803f80
v_mov_b32 v115,0x3f803f80
W lgkmcnt(6)
M v[116:119],v[96:99],v[48:51],0
)"
R"(W lgkmcnt(4)
M v[120:123],v[100:103],v[48:51],0
M v[124:127],v[96:99],v[112:115],0
M v[128:131],v[100:103],v[112:115],0
W lgkmcnt(2)
M v[116:119],v[104:107],v[52:55],v[116:119]
W lgkmcnt(0)
M v[120:123],v[108:111],v[52:55],v[120:123]
)"
R"(M v[124:127],v[104:107],v[112:115],v[124:127]
M v[128:131],v[108:111],v[112:115],v[128:131]
s_nop 3
ds_write_b128 v12,v[116:119] offset:0
s_nop 0
ds_write_b128 v12,v[120:123] offset:1024
ds_write_b128 v12,v[124:127] offset:2048
ds_write_b128 v12,v[128:131] offset:3072
)"
R"(W lgkmcnt(0)
s_barrier
R v[132:135],v13 offset:0
R v[136:139],v13 offset:4096
R v[140:143],v13 offset:8192
R v[144:147],v13 offset:12288
W lgkmcnt(2)
AD v132,v132,v136
)"
R"(AD v133,v133,v137
AD v134,v134,v138
AD v135,v135,v139
W lgkmcnt(1)
AD v132,v132,v140
AD v133,v133,v141
AD v134,v134,v142
AD v135,v135,v143
)"
R"(W lgkmcnt(0)
AD v132,v132,v144
AD v133,v133,v145
AD v134,v134,v146
AD v135,v135,v147
GS v3,v[132:135],s[80:81] sc1
W lgkmcnt(0)
s_barrier
)"
R"(v_xor_b32 v16,0,v28
v_xor_b32 v17,32,v28
v_xor_b32 v18,64,v28
v_xor_b32 v19,96,v28
v_xor_b32 v20,128,v28
v_xor_b32 v21,160,v28
v_xor_b32 v22,192,v28
v_xor_b32 v23,224,v28
)"
R"(s_lshl_b32 s42,%17,12
v_add_u32 v4,s42,v27
v_xor_b32 v5,64,v4
v_add_u32 v6,0x10000,v4
v_add_u32 v7,0x10000,v5
SA m0,s40,98304
SM s41,1048576
B %1,s[68:71],s41 offen lds
)"
R"(SA m0,s40,102400
SM s41,1179648
B %1,s[68:71],s41 offen lds
SA m0,s40,106496
SM s41,1310720
B %1,s[68:71],s41 offen lds
SA m0,s40,110592
SM s41,1441792
)"
R"(B %1,s[68:71],s41 offen lds
SA m0,s40,0
SA s41,s76,256
B %0,s[56:59],s41 offen lds
SA m0,s40,4096
SA s41,s76,33024
B %0,s[56:59],s41 offen lds
SA m0,s40,8192
)"
R"(SA s41,s76,65792
B %0,s[56:59],s41 offen lds
SA m0,s40,12288
SA s41,s76,98560
B %0,s[56:59],s41 offen lds
SA m0,s40,16384
SA s41,s77,256
B %0,s[56:59],s41 offen lds
)"
R"(SA m0,s40,20480
SA s41,s77,33024
B %0,s[56:59],s41 offen lds
SA m0,s40,24576
SA s41,s77,65792
B %0,s[56:59],s41 offen lds
SA m0,s40,28672
SA s41,s77,98560
)"
R"(B %0,s[56:59],s41 offen lds
W vmcnt(41) lgkmcnt(0)
s_barrier
T v[64:65],v16 offset:32768
T v[66:67],v16 offset:33792
T v[68:69],v17 offset:32768
T v[70:71],v17 offset:33792
T v[72:73],v18 offset:32768
)"
R"(T v[74:75],v18 offset:33792
T v[76:77],v19 offset:32768
T v[78:79],v19 offset:33792
T v[80:81],v20 offset:32768
T v[82:83],v20 offset:33792
T v[84:85],v21 offset:32768
T v[86:87],v21 offset:33792
T v[88:89],v22 offset:32768
)"
R"(T v[90:91],v22 offset:33792
T v[92:93],v23 offset:32768
T v[94:95],v23 offset:33792
R v[32:35],v4 offset:32768
R v[36:39],v4 offset:34816
R v[40:43],v4 offset:49152
R v[44:47],v4 offset:51200
W lgkmcnt(3)
)"
R"(M v[48:51],v[64:67],v[32:35],0
T v[96:97],v16 offset:40960
M v[52:55],v[68:71],v[32:35],0
T v[98:99],v16 offset:41984
AR v8,a152
M v[56:59],v[72:75],v[32:35],0
M v[60:63],v[76:79],v[32:35],0
T v[100:101],v17 offset:40960
)"
R"(AR v9,a153
M v[128:131],v[80:83],v[32:35],0
T v[102:103],v17 offset:41984
M v[132:135],v[84:87],v[32:35],0
M v[136:139],v[88:91],v[32:35],0
T v[104:105],v18 offset:40960
AR v10,a154
M v[140:143],v[92:95],v[32:35],0
)"
R"(R v[32:35],v5 offset:32768
T v[106:107],v18 offset:41984
W lgkmcnt(9)
M v[144:147],v[64:67],v[36:39],0
AR v11,a155
M v[148:151],v[68:71],v[36:39],0
T v[108:109],v19 offset:40960
M v[152:155],v[72:75],v[36:39],0
)"
R"(T v[110:111],v19 offset:41984
M v[156:159],v[76:79],v[36:39],0
AR v12,a156
M v[160:163],v[80:83],v[36:39],0
T v[112:113],v20 offset:40960
M v[164:167],v[84:87],v[36:39],0
T v[114:115],v20 offset:41984
AR v13,a157
)"
R"(M v[168:171],v[88:91],v[36:39],0
M v[172:175],v[92:95],v[36:39],0
R v[36:39],v5 offset:34816
T v[116:117],v21 offset:40960
W lgkmcnt(14)
M v[176:179],v[64:67],v[40:43],0
T v[118:119],v21 offset:41984
AR v14,a158
)"
R"(M v[180:183],v[68:71],v[40:43],0
M v[184:187],v[72:75],v[40:43],0
T v[120:121],v22 offset:40960
AR v15,a159
M v[188:191],v[76:79],v[40:43],0
T v[122:123],v22 offset:41984
M v[192:195],v[80:83],v[40:43],0
CV v8,v8,v9
)"
R"(M v[196:199],v[84:87],v[40:43],0
T v[124:125],v23 offset:40960
M v[200:203],v[88:91],v[40:43],0
T v[126:127],v23 offset:41984
M v[204:207],v[92:95],v[40:43],0
R v[40:43],v5 offset:49152
CV v9,v10,v11
W lgkmcnt(15)
)"
R"(M v[208:211],v[64:67],v[44:47],0
M v[212:215],v[68:71],v[44:47],0
CV v10,v12,v13
M v[216:219],v[72:75],v[44:47],0
M v[220:223],v[76:79],v[44:47],0
M v[224:227],v[80:83],v[44:47],0
CV v11,v14,v15
M v[228:231],v[84:87],v[44:47],0
)"
R"(M v[232:235],v[88:91],v[44:47],0
GS v3,v[8:11],s[44:45] offset:3072 sc1
M v[236:239],v[92:95],v[44:47],0
R v[44:47],v5 offset:51200
W vmcnt(29) lgkmcnt(0)
s_barrier
M v[48:51],v[96:99],v[32:35],v[48:51]
T v[64:65],v16 offset:49152
)"
R"(SA m0,s40,114688
M v[52:55],v[100:103],v[32:35],v[52:55]
T v[66:67],v16 offset:50176
SM s41,1572864
M v[56:59],v[104:107],v[32:35],v[56:59]
AR v8,a160
AR v9,a161
M v[60:63],v[108:111],v[32:35],v[60:63]
)"
R"(T v[68:69],v17 offset:49152
B %1,s[68:71],s41 offen lds
M v[128:131],v[112:115],v[32:35],v[128:131]
T v[70:71],v17 offset:50176
AR v10,a162
M v[132:135],v[116:119],v[32:35],v[132:135]
SA m0,s40,118784
AR v11,a163
)"
R"(M v[136:139],v[120:123],v[32:35],v[136:139]
T v[72:73],v18 offset:49152
AR v12,a164
M v[140:143],v[124:127],v[32:35],v[140:143]
R v[32:35],v6 offset:0
T v[74:75],v18 offset:50176
M v[144:147],v[96:99],v[36:39],v[144:147]
SM s41,1703936
)"
R"(B %1,s[68:71],s41 offen lds
M v[148:151],v[100:103],v[36:39],v[148:151]
T v[76:77],v19 offset:49152
AR v13,a165
M v[152:155],v[104:107],v[36:39],v[152:155]
T v[78:79],v19 offset:50176
SA m0,s40,122880
M v[156:159],v[108:111],v[36:39],v[156:159]
)"
R"(AR v14,a166
AR v15,a167
M v[160:163],v[112:115],v[36:39],v[160:163]
T v[80:81],v20 offset:49152
SM s41,1835008
M v[164:167],v[116:119],v[36:39],v[164:167]
T v[82:83],v20 offset:50176
CV v8,v8,v9
)"
R"(M v[168:171],v[120:123],v[36:39],v[168:171]
B %1,s[68:71],s41 offen lds
CV v9,v10,v11
M v[172:175],v[124:127],v[36:39],v[172:175]
R v[36:39],v6 offset:2048
T v[84:85],v21 offset:49152
M v[176:179],v[96:99],v[40:43],v[176:179]
T v[86:87],v21 offset:50176
)"
R"(SA m0,s40,126976
CV v10,v12,v13
M v[180:183],v[100:103],v[40:43],v[180:183]
SM s41,1966080
CV v11,v14,v15
M v[184:187],v[104:107],v[40:43],v[184:187]
T v[88:89],v22 offset:49152
SA s44,s44,0x1000
)"
R"(M v[188:191],v[108:111],v[40:43],v[188:191]
T v[90:91],v22 offset:50176
B %1,s[68:71],s41 offen lds
s_addc_u32 s45,s45,0
M v[192:195],v[112:115],v[40:43],v[192:195]
GS v3,v[8:11],s[44:45] offset:0 sc1
s_nop 1
AR v8,a168
)"
R"(M v[196:199],v[116:119],v[40:43],v[196:199]
T v[92:93],v23 offset:49152
SA m0,s40,32768
M v[200:203],v[120:123],v[40:43],v[200:203]
T v[94:95],v23 offset:50176
AR v9,a169
M v[204:207],v[124:127],v[40:43],v[204:207]
R v[40:43],v6 offset:16384
)"
R"(SA s41,s76,384
M v[208:211],v[96:99],v[44:47],v[208:211]
B %0,s[56:59],s41 offen lds
AR v10,a170
M v[212:215],v[100:103],v[44:47],v[212:215]
AR v11,a171
AR v12,a172
M v[216:219],v[104:107],v[44:47],v[216:219]
)"
R"(SA m0,s40,36864
AR v13,a173
M v[220:223],v[108:111],v[44:47],v[220:223]
AR v14,a174
AR v15,a175
M v[224:227],v[112:115],v[44:47],v[224:227]
SA s41,s76,33152
CV v8,v8,v9
)"
R"(M v[228:231],v[116:119],v[44:47],v[228:231]
CV v9,v10,v11
CV v10,v12,v13
M v[232:235],v[120:123],v[44:47],v[232:235]
B %0,s[56:59],s41 offen lds
CV v11,v14,v15
M v[236:239],v[124:127],v[44:47],v[236:239]
R v[44:47],v6 offset:18432
)"
R"(GS v3,v[8:11],s[44:45] offset:1024 sc1
W lgkmcnt(11)
M v[48:51],v[64:67],v[32:35],v[48:51]
T v[96:97],v16 offset:57344
SA m0,s40,40960
M v[52:55],v[68:71],v[32:35],v[52:55]
T v[98:99],v16 offset:58368
SA s41,s76,65920
)"
R"(M v[56:59],v[72:75],v[32:35],v[56:59]
B %0,s[56:59],s41 offen lds
SA m0,s40,45056
M v[60:63],v[76:79],v[32:35],v[60:63]
T v[100:101],v17 offset:57344
SA s41,s76,98688
W lgkmcnt(5)
M v[128:131],v[80:83],v[32:35],v[128:131]
)"
R"(T v[102:103],v17 offset:58368
B %0,s[56:59],s41 offen lds
M v[132:135],v[84:87],v[32:35],v[132:135]
SA m0,s40,49152
SA s41,s77,384
M v[136:139],v[88:91],v[32:35],v[136:139]
T v[104:105],v18 offset:57344
B %0,s[56:59],s41 offen lds
)"
R"(M v[140:143],v[92:95],v[32:35],v[140:143]
R v[32:35],v7 offset:0
T v[106:107],v18 offset:58368
M v[144:147],v[64:67],v[36:39],v[144:147]
SA m0,s40,53248
SA s41,s77,33152
M v[148:151],v[68:71],v[36:39],v[148:151]
T v[108:109],v19 offset:57344
)"
R"(B %0,s[56:59],s41 offen lds
AR v8,a176
M v[152:155],v[72:75],v[36:39],v[152:155]
T v[110:111],v19 offset:58368
SA m0,s40,57344
M v[156:159],v[76:79],v[36:39],v[156:159]
SA s41,s77,65920
B %0,s[56:59],s41 offen lds
)"
R"(M v[160:163],v[80:83],v[36:39],v[160:163]
T v[112:113],v20 offset:57344
SA m0,s40,61440
AR v9,a177
M v[164:167],v[84:87],v[36:39],v[164:167]
T v[114:115],v20 offset:58368
SA s41,s77,98688
M v[168:171],v[88:91],v[36:39],v[168:171]
)"
R"(B %0,s[56:59],s41 offen lds
s_sub_u32 s92,15,%20
AR v10,a178
M v[172:175],v[92:95],v[36:39],v[172:175]
R v[36:39],v7 offset:2048
T v[116:117],v21 offset:57344
s_lshl_b32 s92,s92,1
W lgkmcnt(14)
)"
R"(M v[176:179],v[64:67],v[40:43],v[176:179]
T v[118:119],v21 offset:58368
SA s92,s92,2
s_lshl_b32 s92,s92,14
M v[180:183],v[68:71],v[40:43],v[180:183]
s_lshl_b32 s42,%19,19
s_mov_b64 s[60:61],%16
AR v11,a179
)"
R"(M v[184:187],v[72:75],v[40:43],v[184:187]
T v[120:121],v22 offset:57344
SA s60,s60,s42
M v[188:191],v[76:79],v[40:43],v[188:191]
T v[122:123],v22 offset:58368
s_addc_u32 s61,s61,0
AR v12,a180
M v[192:195],v[80:83],v[40:43],v[192:195]
)"
R"(SA s60,s60,0x1000000
s_addc_u32 s61,s61,0
M v[196:199],v[84:87],v[40:43],v[196:199]
T v[124:125],v23 offset:57344
s_and_b32 s61,s61,0xffff
M v[200:203],v[88:91],v[40:43],v[200:203]
T v[126:127],v23 offset:58368
SM s62,s92
)"
R"(AR v13,a181
M v[204:207],v[92:95],v[40:43],v[204:207]
R v[40:43],v7 offset:16384
SM s63,0x20000
W lgkmcnt(15)
M v[208:211],v[64:67],v[44:47],v[208:211]
s_mov_b64 s[64:65],%16
SA s64,s64,s42
)"
R"(AR v14,a182
M v[212:215],v[68:71],v[44:47],v[212:215]
s_addc_u32 s65,s65,0
SA s64,s64,0x2000000
M v[216:219],v[72:75],v[44:47],v[216:219]
s_addc_u32 s65,s65,0
s_and_b32 s65,s65,0xffff
M v[220:223],v[76:79],v[44:47],v[220:223]
)"
R"(SM s66,s92
AR v15,a183
M v[224:227],v[80:83],v[44:47],v[224:227]
SM s67,0x20000
CV v8,v8,v9
M v[228:231],v[84:87],v[44:47],v[228:231]
CV v9,v10,v11
CV v10,v12,v13
)"
R"(M v[232:235],v[88:91],v[44:47],v[232:235]
CV v11,v14,v15
GS v3,v[8:11],s[44:45] offset:2048 sc1
M v[236:239],v[92:95],v[44:47],v[236:239]
R v[44:47],v7 offset:18432
W vmcnt(16) lgkmcnt(0)
s_barrier
M v[48:51],v[96:99],v[32:35],v[48:51]
)"
R"(T v[64:65],v16 offset:0
SA m0,s40,131072
M v[52:55],v[100:103],v[32:35],v[52:55]
T v[66:67],v16 offset:1024
SM s41,2097152
M v[56:59],v[104:107],v[32:35],v[56:59]
AR v8,a184
AR v9,a185
)"
R"(M v[60:63],v[108:111],v[32:35],v[60:63]
T v[68:69],v17 offset:0
B %1,s[68:71],s41 offen lds
M v[128:131],v[112:115],v[32:35],v[128:131]
T v[70:71],v17 offset:1024
AR v10,a186
M v[132:135],v[116:119],v[32:35],v[132:135]
SA m0,s40,135168
)"
R"(AR v11,a187
M v[136:139],v[120:123],v[32:35],v[136:139]
T v[72:73],v18 offset:0
AR v12,a188
M v[140:143],v[124:127],v[32:35],v[140:143]
R v[32:35],v4 offset:0
T v[74:75],v18 offset:1024
M v[144:147],v[96:99],v[36:39],v[144:147]
)"
R"(SM s41,2228224
B %1,s[68:71],s41 offen lds
M v[148:151],v[100:103],v[36:39],v[148:151]
T v[76:77],v19 offset:0
AR v13,a189
M v[152:155],v[104:107],v[36:39],v[152:155]
T v[78:79],v19 offset:1024
SA m0,s40,139264
)"
R"(M v[156:159],v[108:111],v[36:39],v[156:159]
AR v14,a190
AR v15,a191
M v[160:163],v[112:115],v[36:39],v[160:163]
T v[80:81],v20 offset:0
SM s41,2359296
M v[164:167],v[116:119],v[36:39],v[164:167]
T v[82:83],v20 offset:1024
)"
R"(CV v8,v8,v9
M v[168:171],v[120:123],v[36:39],v[168:171]
B %1,s[68:71],s41 offen lds
CV v9,v10,v11
M v[172:175],v[124:127],v[36:39],v[172:175]
R v[36:39],v4 offset:2048
T v[84:85],v21 offset:0
M v[176:179],v[96:99],v[40:43],v[176:179]
)"
R"(T v[86:87],v21 offset:1024
SA m0,s40,143360
CV v10,v12,v13
M v[180:183],v[100:103],v[40:43],v[180:183]
SM s41,2490368
CV v11,v14,v15
M v[184:187],v[104:107],v[40:43],v[184:187]
T v[88:89],v22 offset:0
)"
R"(GS v3,v[8:11],s[44:45] offset:3072 sc1
M v[188:191],v[108:111],v[40:43],v[188:191]
T v[90:91],v22 offset:1024
B %1,s[68:71],s41 offen lds
AR v8,a192
M v[192:195],v[112:115],v[40:43],v[192:195]
AR v9,a193
AR v10,a194
)"
R"(M v[196:199],v[116:119],v[40:43],v[196:199]
T v[92:93],v23 offset:0
SA m0,s40,65536
M v[200:203],v[120:123],v[40:43],v[200:203]
T v[94:95],v23 offset:1024
AR v11,a195
M v[204:207],v[124:127],v[40:43],v[204:207]
R v[40:43],v4 offset:16384
)"
R"(SA s41,s76,512
M v[208:211],v[96:99],v[44:47],v[208:211]
B %0,s[56:59],s41 offen lds
AR v12,a196
M v[212:215],v[100:103],v[44:47],v[212:215]
AR v13,a197
AR v14,a198
M v[216:219],v[104:107],v[44:47],v[216:219]
)"
R"(SA m0,s40,69632
AR v15,a199
M v[220:223],v[108:111],v[44:47],v[220:223]
CV v8,v8,v9
CV v9,v10,v11
M v[224:227],v[112:115],v[44:47],v[224:227]
SA s41,s76,33280
CV v10,v12,v13
)"
R"(M v[228:231],v[116:119],v[44:47],v[228:231]
CV v11,v14,v15
SA s44,s44,0x1000
M v[232:235],v[120:123],v[44:47],v[232:235]
B %0,s[56:59],s41 offen lds
s_addc_u32 s45,s45,0
M v[236:239],v[124:127],v[44:47],v[236:239]
R v[44:47],v4 offset:18432
)"
R"(GS v3,v[8:11],s[44:45] offset:0 sc1
W lgkmcnt(11)
M v[48:51],v[64:67],v[32:35],v[48:51]
T v[96:97],v16 offset:8192
SA m0,s40,73728
M v[52:55],v[68:71],v[32:35],v[52:55]
T v[98:99],v16 offset:9216
SA s41,s76,66048
)"
R"(M v[56:59],v[72:75],v[32:35],v[56:59]
B %0,s[56:59],s41 offen lds
AR v8,a200
M v[60:63],v[76:79],v[32:35],v[60:63]
T v[100:101],v17 offset:8192
AR v9,a201
W lgkmcnt(5)
M v[128:131],v[80:83],v[32:35],v[128:131]
)"
R"(T v[102:103],v17 offset:9216
SA m0,s40,77824
M v[132:135],v[84:87],v[32:35],v[132:135]
SA s41,s76,98816
AR v10,a202
M v[136:139],v[88:91],v[32:35],v[136:139]
T v[104:105],v18 offset:8192
B %0,s[56:59],s41 offen lds
)"
R"(M v[140:143],v[92:95],v[32:35],v[140:143]
R v[32:35],v5 offset:0
T v[106:107],v18 offset:9216
M v[144:147],v[64:67],v[36:39],v[144:147]
SA m0,s40,81920
AR v11,a203
M v[148:151],v[68:71],v[36:39],v[148:151]
T v[108:109],v19 offset:8192
)"
R"(SA s41,s77,512
AR v12,a204
M v[152:155],v[72:75],v[36:39],v[152:155]
T v[110:111],v19 offset:9216
B %0,s[56:59],s41 offen lds
M v[156:159],v[76:79],v[36:39],v[156:159]
AR v13,a205
AR v14,a206
)"
R"(M v[160:163],v[80:83],v[36:39],v[160:163]
T v[112:113],v20 offset:8192
SA m0,s40,86016
M v[164:167],v[84:87],v[36:39],v[164:167]
T v[114:115],v20 offset:9216
SA s41,s77,33280
AR v15,a207
M v[168:171],v[88:91],v[36:39],v[168:171]
)"
R"(CV v8,v8,v9
CV v9,v10,v11
M v[172:175],v[92:95],v[36:39],v[172:175]
R v[36:39],v5 offset:2048
T v[116:117],v21 offset:8192
W lgkmcnt(14)
M v[176:179],v[64:67],v[40:43],v[176:179]
T v[118:119],v21 offset:9216
)"
R"(B %0,s[56:59],s41 offen lds
M v[180:183],v[68:71],v[40:43],v[180:183]
SA m0,s40,90112
SA s41,s77,66048
CV v10,v12,v13
M v[184:187],v[72:75],v[40:43],v[184:187]
T v[120:121],v22 offset:8192
CV v11,v14,v15
)"
R"(M v[188:191],v[76:79],v[40:43],v[188:191]
T v[122:123],v22 offset:9216
B %0,s[56:59],s41 offen lds
GS v3,v[8:11],s[44:45] offset:1024 sc1
M v[192:195],v[80:83],v[40:43],v[192:195]
SA m0,s40,94208
AR v8,a208
M v[196:199],v[84:87],v[40:43],v[196:199]
)"
R"(T v[124:125],v23 offset:8192
SA s41,s77,98816
M v[200:203],v[88:91],v[40:43],v[200:203]
T v[126:127],v23 offset:9216
AR v9,a209
M v[204:207],v[92:95],v[40:43],v[204:207]
R v[40:43],v5 offset:16384
B %0,s[56:59],s41 offen lds
)"
R"(AR v10,a210
W lgkmcnt(15)
M v[208:211],v[64:67],v[44:47],v[208:211]
s_lshl_b32 s92,%20,12
AR v11,a211
M v[212:215],v[68:71],v[44:47],v[212:215]
AR v12,a212
AR v13,a213
)"
R"(M v[216:219],v[72:75],v[44:47],v[216:219]
s_sub_u32 s80,s80,s92
AR v14,a214
M v[220:223],v[76:79],v[44:47],v[220:223]
s_subb_u32 s81,s81,0
AR v15,a215
M v[224:227],v[80:83],v[44:47],v[224:227]
CV v8,v8,v9
)"
R"(CV v9,v10,v11
M v[228:231],v[84:87],v[44:47],v[228:231]
CV v10,v12,v13
M v[232:235],v[88:91],v[44:47],v[232:235]
CV v11,v14,v15
M v[236:239],v[92:95],v[44:47],v[236:239]
R v[44:47],v5 offset:18432
GS v3,v[8:11],s[44:45] offset:2048 sc1
)"
R"(W vmcnt(17) lgkmcnt(0)
s_barrier
M v[48:51],v[96:99],v[32:35],v[48:51]
T v[64:65],v16 offset:16384
SA m0,s40,147456
M v[52:55],v[100:103],v[32:35],v[52:55]
T v[66:67],v16 offset:17408
SM s41,2621440
)"
R"(M v[56:59],v[104:107],v[32:35],v[56:59]
AR v8,a216
M v[60:63],v[108:111],v[32:35],v[60:63]
T v[68:69],v17 offset:16384
B %1,s[68:71],s41 offen lds
M v[128:131],v[112:115],v[32:35],v[128:131]
T v[70:71],v17 offset:17408
AR v9,a217
)"
R"(M v[132:135],v[116:119],v[32:35],v[132:135]
SA m0,s40,151552
M v[136:139],v[120:123],v[32:35],v[136:139]
T v[72:73],v18 offset:16384
AR v10,a218
M v[140:143],v[124:127],v[32:35],v[140:143]
R v[32:35],v4 offset:32768
T v[74:75],v18 offset:17408
)"
R"(M v[144:147],v[96:99],v[36:39],v[144:147]
SM s41,2752512
B %1,s[68:71],s41 offen lds
M v[148:151],v[100:103],v[36:39],v[148:151]
T v[76:77],v19 offset:16384
AR v11,a219
M v[152:155],v[104:107],v[36:39],v[152:155]
T v[78:79],v19 offset:17408
)"
R"(SA m0,s40,155648
M v[156:159],v[108:111],v[36:39],v[156:159]
AR v12,a220
M v[160:163],v[112:115],v[36:39],v[160:163]
T v[80:81],v20 offset:16384
SM s41,2883584
M v[164:167],v[116:119],v[36:39],v[164:167]
T v[82:83],v20 offset:17408
)"
R"(AR v13,a221
M v[168:171],v[120:123],v[36:39],v[168:171]
B %1,s[68:71],s41 offen lds
M v[172:175],v[124:127],v[36:39],v[172:175]
R v[36:39],v4 offset:34816
T v[84:85],v21 offset:16384
M v[176:179],v[96:99],v[40:43],v[176:179]
T v[86:87],v21 offset:17408
)"
R"(SA m0,s40,159744
M v[180:183],v[100:103],v[40:43],v[180:183]
SM s41,3014656
AR v14,a222
M v[184:187],v[104:107],v[40:43],v[184:187]
T v[88:89],v22 offset:16384
AR v15,a223
M v[188:191],v[108:111],v[40:43],v[188:191]
)"
R"(T v[90:91],v22 offset:17408
B %1,s[68:71],s41 offen lds
M v[192:195],v[112:115],v[40:43],v[192:195]
CV v8,v8,v9
M v[196:199],v[116:119],v[40:43],v[196:199]
T v[92:93],v23 offset:16384
SA m0,s40,0
M v[200:203],v[120:123],v[40:43],v[200:203]
)"
R"(T v[94:95],v23 offset:17408
M v[204:207],v[124:127],v[40:43],v[204:207]
R v[40:43],v4 offset:49152
SA s41,s76,640
M v[208:211],v[96:99],v[44:47],v[208:211]
B %0,s[56:59],s41 offen lds
CV v9,v10,v11
M v[212:215],v[100:103],v[44:47],v[212:215]
)"
R"(CV v10,v12,v13
M v[216:219],v[104:107],v[44:47],v[216:219]
SA m0,s40,4096
M v[220:223],v[108:111],v[44:47],v[220:223]
M v[224:227],v[112:115],v[44:47],v[224:227]
SA s41,s76,33408
CV v11,v14,v15
M v[228:231],v[116:119],v[44:47],v[228:231]
)"
R"(M v[232:235],v[120:123],v[44:47],v[232:235]
B %0,s[56:59],s41 offen lds
GS v3,v[8:11],s[44:45] offset:3072 sc1
M v[236:239],v[124:127],v[44:47],v[236:239]
R v[44:47],v4 offset:51200
W lgkmcnt(11)
M v[48:51],v[64:67],v[32:35],v[48:51]
T v[96:97],v16 offset:24576
)"
R"(SA m0,s40,8192
M v[52:55],v[68:71],v[32:35],v[52:55]
T v[98:99],v16 offset:25600
SA s41,s76,66176
M v[56:59],v[72:75],v[32:35],v[56:59]
AR v8,a224
AR v9,a225
M v[60:63],v[76:79],v[32:35],v[60:63]
)"
R"(T v[100:101],v17 offset:24576
B %0,s[56:59],s41 offen lds
W lgkmcnt(5)
M v[128:131],v[80:83],v[32:35],v[128:131]
T v[102:103],v17 offset:25600
SA m0,s40,12288
M v[132:135],v[84:87],v[32:35],v[132:135]
AR v10,a226
)"
R"(AR v11,a227
M v[136:139],v[88:91],v[32:35],v[136:139]
T v[104:105],v18 offset:24576
SA s41,s76,98944
M v[140:143],v[92:95],v[32:35],v[140:143]
R v[32:35],v5 offset:32768
T v[106:107],v18 offset:25600
M v[144:147],v[64:67],v[36:39],v[144:147]
)"
R"(B %0,s[56:59],s41 offen lds
AR v12,a228
M v[148:151],v[68:71],v[36:39],v[148:151]
T v[108:109],v19 offset:24576
SA m0,s40,16384
M v[152:155],v[72:75],v[36:39],v[152:155]
T v[110:111],v19 offset:25600
AR v13,a229
)"
R"(M v[156:159],v[76:79],v[36:39],v[156:159]
SA s41,s77,640
AR v14,a230
M v[160:163],v[80:83],v[36:39],v[160:163]
T v[112:113],v20 offset:24576
B %0,s[56:59],s41 offen lds
AR v15,a231
M v[164:167],v[84:87],v[36:39],v[164:167]
)"
R"(T v[114:115],v20 offset:25600
CV v8,v8,v9
M v[168:171],v[88:91],v[36:39],v[168:171]
SA m0,s40,20480
CV v9,v10,v11
M v[172:175],v[92:95],v[36:39],v[172:175]
R v[36:39],v5 offset:34816
T v[116:117],v21 offset:24576
)"
R"(W lgkmcnt(14)
M v[176:179],v[64:67],v[40:43],v[176:179]
T v[118:119],v21 offset:25600
SA s41,s77,33408
CV v10,v12,v13
M v[180:183],v[68:71],v[40:43],v[180:183]
B %0,s[56:59],s41 offen lds
CV v11,v14,v15
)"
R"(M v[184:187],v[72:75],v[40:43],v[184:187]
T v[120:121],v22 offset:24576
SA s44,s44,0x1000
M v[188:191],v[76:79],v[40:43],v[188:191]
T v[122:123],v22 offset:25600
SA m0,s40,24576
s_addc_u32 s45,s45,0
M v[192:195],v[80:83],v[40:43],v[192:195]
)"
R"(SA s41,s77,66176
GS v3,v[8:11],s[44:45] offset:0 sc1
M v[196:199],v[84:87],v[40:43],v[196:199]
T v[124:125],v23 offset:24576
AR v8,a232
M v[200:203],v[88:91],v[40:43],v[200:203]
T v[126:127],v23 offset:25600
B %0,s[56:59],s41 offen lds
)"
R"(AR v9,a233
M v[204:207],v[92:95],v[40:43],v[204:207]
R v[40:43],v5 offset:49152
AR v10,a234
W lgkmcnt(15)
M v[208:211],v[64:67],v[44:47],v[208:211]
SA m0,s40,28672
AR v11,a235
)"
R"(M v[212:215],v[68:71],v[44:47],v[212:215]
SA s41,s77,98944
AR v12,a236
M v[216:219],v[72:75],v[44:47],v[216:219]
AR v13,a237
AR v14,a238
M v[220:223],v[76:79],v[44:47],v[220:223]
B %0,s[56:59],s41 offen lds
)"
R"(AR v15,a239
M v[224:227],v[80:83],v[44:47],v[224:227]
CV v8,v8,v9
CV v9,v10,v11
M v[228:231],v[84:87],v[44:47],v[228:231]
CV v10,v12,v13
M v[232:235],v[88:91],v[44:47],v[232:235]
CV v11,v14,v15
)"
R"(M v[236:239],v[92:95],v[44:47],v[236:239]
R v[44:47],v5 offset:51200
GS v3,v[8:11],s[44:45] offset:1024 sc1
W vmcnt(16) lgkmcnt(0)
s_barrier
M v[48:51],v[96:99],v[32:35],v[48:51]
T v[64:65],v16 offset:32768
SA m0,s40,98304
)"
R"(M v[52:55],v[100:103],v[32:35],v[52:55]
T v[66:67],v16 offset:33792
SM s41,3145728
M v[56:59],v[104:107],v[32:35],v[56:59]
AR v8,a240
AR v9,a241
M v[60:63],v[108:111],v[32:35],v[60:63]
T v[68:69],v17 offset:32768
)"
R"(B %1,s[68:71],s41 offen lds
M v[128:131],v[112:115],v[32:35],v[128:131]
T v[70:71],v17 offset:33792
AR v10,a242
M v[132:135],v[116:119],v[32:35],v[132:135]
SA m0,s40,102400
AR v11,a243
M v[136:139],v[120:123],v[32:35],v[136:139]
)"
R"(T v[72:73],v18 offset:32768
AR v12,a244
M v[140:143],v[124:127],v[32:35],v[140:143]
R v[32:35],v6 offset:0
T v[74:75],v18 offset:33792
M v[144:147],v[96:99],v[36:39],v[144:147]
SM s41,3276800
B %1,s[68:71],s41 offen lds
)"
R"(M v[148:151],v[100:103],v[36:39],v[148:151]
T v[76:77],v19 offset:32768
AR v13,a245
M v[152:155],v[104:107],v[36:39],v[152:155]
T v[78:79],v19 offset:33792
SA m0,s40,106496
M v[156:159],v[108:111],v[36:39],v[156:159]
AR v14,a246
)"
R"(AR v15,a247
M v[160:163],v[112:115],v[36:39],v[160:163]
T v[80:81],v20 offset:32768
SM s41,3407872
M v[164:167],v[116:119],v[36:39],v[164:167]
T v[82:83],v20 offset:33792
CV v8,v8,v9
M v[168:171],v[120:123],v[36:39],v[168:171]
)"
R"(B %1,s[68:71],s41 offen lds
CV v9,v10,v11
M v[172:175],v[124:127],v[36:39],v[172:175]
R v[36:39],v6 offset:2048
T v[84:85],v21 offset:32768
M v[176:179],v[96:99],v[40:43],v[176:179]
T v[86:87],v21 offset:33792
SA m0,s40,110592
)"
R"(M v[180:183],v[100:103],v[40:43],v[180:183]
SM s41,3538944
CV v10,v12,v13
M v[184:187],v[104:107],v[40:43],v[184:187]
T v[88:89],v22 offset:32768
CV v11,v14,v15
M v[188:191],v[108:111],v[40:43],v[188:191]
T v[90:91],v22 offset:33792
)"
R"(B %1,s[68:71],s41 offen lds
GS v3,v[8:11],s[44:45] offset:2048 sc1
M v[192:195],v[112:115],v[40:43],v[192:195]
s_nop 0
AR v8,a248
AR v9,a249
M v[196:199],v[116:119],v[40:43],v[196:199]
T v[92:93],v23 offset:32768
)"
R"(SA m0,s40,32768
M v[200:203],v[120:123],v[40:43],v[200:203]
T v[94:95],v23 offset:33792
AR v10,a250
M v[204:207],v[124:127],v[40:43],v[204:207]
R v[40:43],v6 offset:16384
SA s41,s76,768
M v[208:211],v[96:99],v[44:47],v[208:211]
)"
R"(B %0,s[56:59],s41 offen lds
AR v11,a251
M v[212:215],v[100:103],v[44:47],v[212:215]
AR v12,a252
AR v13,a253
M v[216:219],v[104:107],v[44:47],v[216:219]
SA m0,s40,36864
AR v14,a254
)"
R"(M v[220:223],v[108:111],v[44:47],v[220:223]
AR v15,a255
CV v8,v8,v9
M v[224:227],v[112:115],v[44:47],v[224:227]
SA s41,s76,33536
CV v9,v10,v11
M v[228:231],v[116:119],v[44:47],v[228:231]
CV v10,v12,v13
)"
R"(M v[232:235],v[120:123],v[44:47],v[232:235]
B %0,s[56:59],s41 offen lds
CV v11,v14,v15
M v[236:239],v[124:127],v[44:47],v[236:239]
R v[44:47],v6 offset:18432
GS v3,v[8:11],s[44:45] offset:3072 sc1
W lgkmcnt(11)
M v[48:51],v[64:67],v[32:35],v[48:51]
)"
R"(T v[96:97],v16 offset:40960
SA m0,s40,40960
M v[52:55],v[68:71],v[32:35],v[52:55]
T v[98:99],v16 offset:41984
SA s41,s76,66304
M v[56:59],v[72:75],v[32:35],v[56:59]
AW a0,0
AW a1,0
)"
R"(M v[60:63],v[76:79],v[32:35],v[60:63]
T v[100:101],v17 offset:40960
B %0,s[56:59],s41 offen lds
W lgkmcnt(5)
M v[128:131],v[80:83],v[32:35],v[128:131]
T v[102:103],v17 offset:41984
SA m0,s40,45056
M v[132:135],v[84:87],v[32:35],v[132:135]
)"
R"(AW a2,0
M v[136:139],v[88:91],v[32:35],v[136:139]
T v[104:105],v18 offset:40960
SA s41,s76,99072
M v[140:143],v[92:95],v[32:35],v[140:143]
R v[32:35],v7 offset:0
T v[106:107],v18 offset:41984
M v[144:147],v[64:67],v[36:39],v[144:147]
)"
R"(B %0,s[56:59],s41 offen lds
AW a3,0
M v[148:151],v[68:71],v[36:39],v[148:151]
T v[108:109],v19 offset:40960
SA m0,s40,49152
M v[152:155],v[72:75],v[36:39],v[152:155]
T v[110:111],v19 offset:41984
AW a4,0
)"
R"(M v[156:159],v[76:79],v[36:39],v[156:159]
SA s41,s77,768
AW a5,0
M v[160:163],v[80:83],v[36:39],v[160:163]
T v[112:113],v20 offset:40960
B %0,s[56:59],s41 offen lds
M v[164:167],v[84:87],v[36:39],v[164:167]
T v[114:115],v20 offset:41984
)"
R"(AW a6,0
M v[168:171],v[88:91],v[36:39],v[168:171]
SA m0,s40,53248
AW a7,0
M v[172:175],v[92:95],v[36:39],v[172:175]
R v[36:39],v7 offset:2048
T v[116:117],v21 offset:40960
W lgkmcnt(14)
)"
R"(M v[176:179],v[64:67],v[40:43],v[176:179]
T v[118:119],v21 offset:41984
SA s41,s77,33536
M v[180:183],v[68:71],v[40:43],v[180:183]
B %0,s[56:59],s41 offen lds
AW a8,0
M v[184:187],v[72:75],v[40:43],v[184:187]
T v[120:121],v22 offset:40960
)"
R"(AW a9,0
M v[188:191],v[76:79],v[40:43],v[188:191]
T v[122:123],v22 offset:41984
SA m0,s40,57344
M v[192:195],v[80:83],v[40:43],v[192:195]
SA s41,s77,66304
AW a10,0
M v[196:199],v[84:87],v[40:43],v[196:199]
)"
R"(T v[124:125],v23 offset:40960
M v[200:203],v[88:91],v[40:43],v[200:203]
T v[126:127],v23 offset:41984
B %0,s[56:59],s41 offen lds
M v[204:207],v[92:95],v[40:43],v[204:207]
R v[40:43],v7 offset:16384
AW a11,0
W lgkmcnt(15)
)"
R"(M v[208:211],v[64:67],v[44:47],v[208:211]
SA m0,s40,61440
AW a12,0
M v[212:215],v[68:71],v[44:47],v[212:215]
SA s41,s77,99072
M v[216:219],v[72:75],v[44:47],v[216:219]
AW a13,0
M v[220:223],v[76:79],v[44:47],v[220:223]
)"
R"(B %0,s[56:59],s41 offen lds
M v[224:227],v[80:83],v[44:47],v[224:227]
AW a14,0
M v[228:231],v[84:87],v[44:47],v[228:231]
M v[232:235],v[88:91],v[44:47],v[232:235]
AW a15,0
M v[236:239],v[92:95],v[44:47],v[236:239]
R v[44:47],v7 offset:18432
)"
R"(W vmcnt(6) lgkmcnt(0)
s_barrier
s_cmp_lg_u32 %17,0
s_cbranch_scc1 NP1_%=
s_lshl_b32 s92,s95,6
s_mov_b64 s[90:91],%16
SA s90,s90,s92
s_addc_u32 s91,s91,0
)"
R"(SA s90,s90,0x5001000
s_addc_u32 s91,s91,0
v_mov_b32 v8,s88
v_mov_b32 v9,0
s_mov_b64 exec,1
s_nop 1
global_store_dword v9,v8,s[90:91] sc1
s_mov_b64 exec,-1
)"
R"(s_nop 1
NP1_%=:
M v[48:51],v[96:99],v[32:35],v[48:51]
T v[64:65],v16 offset:49152
SA m0,s40,114688
M v[52:55],v[100:103],v[32:35],v[52:55]
T v[66:67],v16 offset:50176
SM s41,3670016
)"
R"(M v[56:59],v[104:107],v[32:35],v[56:59]
AW a16,0
AW a17,0
M v[60:63],v[108:111],v[32:35],v[60:63]
T v[68:69],v17 offset:49152
B %1,s[68:71],s41 offen lds
M v[128:131],v[112:115],v[32:35],v[128:131]
T v[70:71],v17 offset:50176
)"
R"(AW a18,0
M v[132:135],v[116:119],v[32:35],v[132:135]
SA m0,s40,118784
M v[136:139],v[120:123],v[32:35],v[136:139]
T v[72:73],v18 offset:49152
AW a19,0
M v[140:143],v[124:127],v[32:35],v[140:143]
R v[32:35],v4 offset:0
)"
R"(T v[74:75],v18 offset:50176
M v[144:147],v[96:99],v[36:39],v[144:147]
SM s41,3801088
B %1,s[68:71],s41 offen lds
M v[148:151],v[100:103],v[36:39],v[148:151]
T v[76:77],v19 offset:49152
AW a20,0
M v[152:155],v[104:107],v[36:39],v[152:155]
)"
R"(T v[78:79],v19 offset:50176
SA m0,s40,122880
M v[156:159],v[108:111],v[36:39],v[156:159]
AW a21,0
M v[160:163],v[112:115],v[36:39],v[160:163]
T v[80:81],v20 offset:49152
SM s41,3932160
M v[164:167],v[116:119],v[36:39],v[164:167]
)"
R"(T v[82:83],v20 offset:50176
AW a22,0
M v[168:171],v[120:123],v[36:39],v[168:171]
B %1,s[68:71],s41 offen lds
AW a23,0
M v[172:175],v[124:127],v[36:39],v[172:175]
R v[36:39],v4 offset:2048
T v[84:85],v21 offset:49152
)"
R"(M v[176:179],v[96:99],v[40:43],v[176:179]
T v[86:87],v21 offset:50176
SA m0,s40,126976
M v[180:183],v[100:103],v[40:43],v[180:183]
SM s41,4063232
AW a24,0
M v[184:187],v[104:107],v[40:43],v[184:187]
T v[88:89],v22 offset:49152
)"
R"(AW a25,0
M v[188:191],v[108:111],v[40:43],v[188:191]
T v[90:91],v22 offset:50176
B %1,s[68:71],s41 offen lds
M v[192:195],v[112:115],v[40:43],v[192:195]
AW a26,0
M v[196:199],v[116:119],v[40:43],v[196:199]
T v[92:93],v23 offset:49152
)"
R"(SA m0,s40,65536
M v[200:203],v[120:123],v[40:43],v[200:203]
T v[94:95],v23 offset:50176
AW a27,0
M v[204:207],v[124:127],v[40:43],v[204:207]
R v[40:43],v4 offset:16384
SA s41,s76,896
M v[208:211],v[96:99],v[44:47],v[208:211]
)"
R"(B %0,s[56:59],s41 offen lds
AW a28,0
M v[212:215],v[100:103],v[44:47],v[212:215]
M v[216:219],v[104:107],v[44:47],v[216:219]
SA m0,s40,69632
AW a29,0
M v[220:223],v[108:111],v[44:47],v[220:223]
M v[224:227],v[112:115],v[44:47],v[224:227]
)"
R"(SA s41,s76,33664
AW a30,0
M v[228:231],v[116:119],v[44:47],v[228:231]
M v[232:235],v[120:123],v[44:47],v[232:235]
B %0,s[56:59],s41 offen lds
AW a31,0
M v[236:239],v[124:127],v[44:47],v[236:239]
R v[44:47],v4 offset:18432
)"
R"(W lgkmcnt(11)
M v[48:51],v[64:67],v[32:35],v[48:51]
T v[96:97],v16 offset:57344
SA m0,s40,73728
M v[52:55],v[68:71],v[32:35],v[52:55]
T v[98:99],v16 offset:58368
SA s41,s76,66432
M v[56:59],v[72:75],v[32:35],v[56:59]
)"
R"(AW a32,0
AW a33,0
M v[60:63],v[76:79],v[32:35],v[60:63]
T v[100:101],v17 offset:57344
B %0,s[56:59],s41 offen lds
W lgkmcnt(5)
M v[128:131],v[80:83],v[32:35],v[128:131]
T v[102:103],v17 offset:58368
)"
R"(SA m0,s40,77824
M v[132:135],v[84:87],v[32:35],v[132:135]
AW a34,0
M v[136:139],v[88:91],v[32:35],v[136:139]
T v[104:105],v18 offset:57344
SA s41,s76,99200
M v[140:143],v[92:95],v[32:35],v[140:143]
R v[32:35],v5 offset:0
)"
R"(T v[106:107],v18 offset:58368
M v[144:147],v[64:67],v[36:39],v[144:147]
B %0,s[56:59],s41 offen lds
AW a35,0
M v[148:151],v[68:71],v[36:39],v[148:151]
T v[108:109],v19 offset:57344
SA m0,s40,81920
M v[152:155],v[72:75],v[36:39],v[152:155]
)"
R"(T v[110:111],v19 offset:58368
AW a36,0
M v[156:159],v[76:79],v[36:39],v[156:159]
SA s41,s77,896
AW a37,0
M v[160:163],v[80:83],v[36:39],v[160:163]
T v[112:113],v20 offset:57344
B %0,s[56:59],s41 offen lds
)"
R"(M v[164:167],v[84:87],v[36:39],v[164:167]
T v[114:115],v20 offset:58368
AW a38,0
M v[168:171],v[88:91],v[36:39],v[168:171]
SA m0,s40,86016
AW a39,0
M v[172:175],v[92:95],v[36:39],v[172:175]
R v[36:39],v5 offset:2048
)"
R"(T v[116:117],v21 offset:57344
W lgkmcnt(14)
M v[176:179],v[64:67],v[40:43],v[176:179]
T v[118:119],v21 offset:58368
SA s41,s77,33664
M v[180:183],v[68:71],v[40:43],v[180:183]
B %0,s[56:59],s41 offen lds
AW a40,0
)"
R"(M v[184:187],v[72:75],v[40:43],v[184:187]
T v[120:121],v22 offset:57344
AW a41,0
M v[188:191],v[76:79],v[40:43],v[188:191]
T v[122:123],v22 offset:58368
SA m0,s40,90112
M v[192:195],v[80:83],v[40:43],v[192:195]
SA s41,s77,66432
)"
R"(AW a42,0
M v[196:199],v[84:87],v[40:43],v[196:199]
T v[124:125],v23 offset:57344
M v[200:203],v[88:91],v[40:43],v[200:203]
T v[126:127],v23 offset:58368
B %0,s[56:59],s41 offen lds
M v[204:207],v[92:95],v[40:43],v[204:207]
R v[40:43],v5 offset:16384
)"
R"(AW a43,0
W lgkmcnt(15)
M v[208:211],v[64:67],v[44:47],v[208:211]
SA m0,s40,94208
AW a44,0
M v[212:215],v[68:71],v[44:47],v[212:215]
SA s41,s77,99200
M v[216:219],v[72:75],v[44:47],v[216:219]
)"
R"(AW a45,0
M v[220:223],v[76:79],v[44:47],v[220:223]
B %0,s[56:59],s41 offen lds
M v[224:227],v[80:83],v[44:47],v[224:227]
AW a46,0
M v[228:231],v[84:87],v[44:47],v[228:231]
M v[232:235],v[88:91],v[44:47],v[232:235]
AW a47,0
)"
R"(M v[236:239],v[92:95],v[44:47],v[236:239]
R v[44:47],v5 offset:18432
W vmcnt(12) lgkmcnt(0)
s_barrier
M v[48:51],v[96:99],v[32:35],v[48:51]
T v[64:65],v16 offset:0
s_mov_b64 s[90:91],%16
M v[52:55],v[100:103],v[32:35],v[52:55]
)"
R"(T v[66:67],v16 offset:1024
AW a48,0
M v[56:59],v[104:107],v[32:35],v[56:59]
AW a49,0
M v[60:63],v[108:111],v[32:35],v[60:63]
T v[68:69],v17 offset:0
M v[128:131],v[112:115],v[32:35],v[128:131]
T v[70:71],v17 offset:1024
)"
R"(AW a50,0
M v[132:135],v[116:119],v[32:35],v[132:135]
M v[136:139],v[120:123],v[32:35],v[136:139]
T v[72:73],v18 offset:0
AW a51,0
M v[140:143],v[124:127],v[32:35],v[140:143]
R v[32:35],v4 offset:32768
T v[74:75],v18 offset:1024
)"
R"(M v[144:147],v[96:99],v[36:39],v[144:147]
SA s90,s90,0x5000000
AW a52,0
M v[148:151],v[100:103],v[36:39],v[148:151]
T v[76:77],v19 offset:0
M v[152:155],v[104:107],v[36:39],v[152:155]
T v[78:79],v19 offset:1024
AW a53,0
)"
R"(M v[156:159],v[108:111],v[36:39],v[156:159]
M v[160:163],v[112:115],v[36:39],v[160:163]
T v[80:81],v20 offset:0
AW a54,0
M v[164:167],v[116:119],v[36:39],v[164:167]
T v[82:83],v20 offset:1024
M v[168:171],v[120:123],v[36:39],v[168:171]
AW a55,0
)"
R"(M v[172:175],v[124:127],v[36:39],v[172:175]
R v[36:39],v4 offset:34816
T v[84:85],v21 offset:0
M v[176:179],v[96:99],v[40:43],v[176:179]
T v[86:87],v21 offset:1024
s_addc_u32 s91,s91,0
M v[180:183],v[100:103],v[40:43],v[180:183]
AW a56,0
)"
R"(M v[184:187],v[104:107],v[40:43],v[184:187]
T v[88:89],v22 offset:0
AW a57,0
M v[188:191],v[108:111],v[40:43],v[188:191]
T v[90:91],v22 offset:1024
M v[192:195],v[112:115],v[40:43],v[192:195]
AW a58,0
M v[196:199],v[116:119],v[40:43],v[196:199]
)"
R"(T v[92:93],v23 offset:0
M v[200:203],v[120:123],v[40:43],v[200:203]
T v[94:95],v23 offset:1024
AW a59,0
M v[204:207],v[124:127],v[40:43],v[204:207]
R v[40:43],v4 offset:49152
M v[208:211],v[96:99],v[44:47],v[208:211]
global_load_dword v2,v1,s[90:91] sc1
)"
R"(AW a60,0
M v[212:215],v[100:103],v[44:47],v[212:215]
M v[216:219],v[104:107],v[44:47],v[216:219]
AW a61,0
M v[220:223],v[108:111],v[44:47],v[220:223]
M v[224:227],v[112:115],v[44:47],v[224:227]
AW a62,0
M v[228:231],v[116:119],v[44:47],v[228:231]
)"
R"(M v[232:235],v[120:123],v[44:47],v[232:235]
AW a63,0
M v[236:239],v[124:127],v[44:47],v[236:239]
R v[44:47],v4 offset:51200
W lgkmcnt(11)
M v[48:51],v[64:67],v[32:35],v[48:51]
T v[96:97],v16 offset:8192
AW a64,0
)"
R"(M v[52:55],v[68:71],v[32:35],v[52:55]
T v[98:99],v16 offset:9216
M v[56:59],v[72:75],v[32:35],v[56:59]
AW a65,0
M v[60:63],v[76:79],v[32:35],v[60:63]
T v[100:101],v17 offset:8192
W lgkmcnt(5)
M v[128:131],v[80:83],v[32:35],v[128:131]
)"
R"(T v[102:103],v17 offset:9216
AW a66,0
M v[132:135],v[84:87],v[32:35],v[132:135]
M v[136:139],v[88:91],v[32:35],v[136:139]
T v[104:105],v18 offset:8192
AW a67,0
M v[140:143],v[92:95],v[32:35],v[140:143]
R v[32:35],v5 offset:32768
)"
R"(T v[106:107],v18 offset:9216
M v[144:147],v[64:67],v[36:39],v[144:147]
AW a68,0
M v[148:151],v[68:71],v[36:39],v[148:151]
T v[108:109],v19 offset:8192
M v[152:155],v[72:75],v[36:39],v[152:155]
T v[110:111],v19 offset:9216
AW a69,0
)"
R"(M v[156:159],v[76:79],v[36:39],v[156:159]
M v[160:163],v[80:83],v[36:39],v[160:163]
T v[112:113],v20 offset:8192
AW a70,0
M v[164:167],v[84:87],v[36:39],v[164:167]
T v[114:115],v20 offset:9216
M v[168:171],v[88:91],v[36:39],v[168:171]
AW a71,0
)"
R"(M v[172:175],v[92:95],v[36:39],v[172:175]
R v[36:39],v5 offset:34816
T v[116:117],v21 offset:8192
W lgkmcnt(14)
M v[176:179],v[64:67],v[40:43],v[176:179]
T v[118:119],v21 offset:9216
AW a72,0
M v[180:183],v[68:71],v[40:43],v[180:183]
)"
R"(M v[184:187],v[72:75],v[40:43],v[184:187]
T v[120:121],v22 offset:8192
AW a73,0
M v[188:191],v[76:79],v[40:43],v[188:191]
T v[122:123],v22 offset:9216
M v[192:195],v[80:83],v[40:43],v[192:195]
AW a74,0
M v[196:199],v[84:87],v[40:43],v[196:199]
)"
R"(T v[124:125],v23 offset:8192
M v[200:203],v[88:91],v[40:43],v[200:203]
T v[126:127],v23 offset:9216
AW a75,0
M v[204:207],v[92:95],v[40:43],v[204:207]
R v[40:43],v5 offset:49152
W lgkmcnt(15)
M v[208:211],v[64:67],v[44:47],v[208:211]
)"
R"(AW a76,0
M v[212:215],v[68:71],v[44:47],v[212:215]
M v[216:219],v[72:75],v[44:47],v[216:219]
AW a77,0
M v[220:223],v[76:79],v[44:47],v[220:223]
M v[224:227],v[80:83],v[44:47],v[224:227]
AW a78,0
M v[228:231],v[84:87],v[44:47],v[228:231]
)"
R"(M v[232:235],v[88:91],v[44:47],v[232:235]
AW a79,0
M v[236:239],v[92:95],v[44:47],v[236:239]
R v[44:47],v5 offset:51200
W vmcnt(0) lgkmcnt(0)
s_barrier
v_cmp_ne_u32 vcc,s88,v2
s_nop 4
)"
R"(s_cbranch_vccnz CK2_%=
CB2_%=:
M v[48:51],v[96:99],v[32:35],v[48:51]
T v[64:65],v16 offset:16384
SA m0,s46,0
M v[52:55],v[100:103],v[32:35],v[52:55]
T v[66:67],v16 offset:17408
AW a80,0
)"
R"(M v[56:59],v[104:107],v[32:35],v[56:59]
SM s41,0
AW a81,0
M v[60:63],v[108:111],v[32:35],v[60:63]
T v[68:69],v17 offset:16384
M v[128:131],v[112:115],v[32:35],v[128:131]
T v[70:71],v17 offset:17408
B v0,s[60:63],s41 offen offset:0 sc1 lds
)"
R"(M v[132:135],v[116:119],v[32:35],v[132:135]
AW a82,0
M v[136:139],v[120:123],v[32:35],v[136:139]
T v[72:73],v18 offset:16384
B v0,s[60:63],s41 offen offset:1024 sc1 lds
M v[140:143],v[124:127],v[32:35],v[140:143]
R v[32:35],v6 offset:0
T v[74:75],v18 offset:17408
)"
R"(M v[144:147],v[96:99],v[36:39],v[144:147]
B v0,s[60:63],s41 offen offset:2048 sc1 lds
AW a83,0
M v[148:151],v[100:103],v[36:39],v[148:151]
T v[76:77],v19 offset:16384
AW a84,0
M v[152:155],v[104:107],v[36:39],v[152:155]
T v[78:79],v19 offset:17408
)"
R"(B v0,s[60:63],s41 offen offset:3072 sc1 lds
M v[156:159],v[108:111],v[36:39],v[156:159]
AW a85,0
M v[160:163],v[112:115],v[36:39],v[160:163]
T v[80:81],v20 offset:16384
SA m0,s46,131072
M v[164:167],v[116:119],v[36:39],v[164:167]
T v[82:83],v20 offset:17408
)"
R"(AW a86,0
M v[168:171],v[120:123],v[36:39],v[168:171]
SM s41,0
AW a87,0
M v[172:175],v[124:127],v[36:39],v[172:175]
R v[36:39],v6 offset:2048
T v[84:85],v21 offset:16384
M v[176:179],v[96:99],v[40:43],v[176:179]
)"
R"(T v[86:87],v21 offset:17408
B v0,s[64:67],s41 offen offset:0 sc1 lds
M v[180:183],v[100:103],v[40:43],v[180:183]
AW a88,0
M v[184:187],v[104:107],v[40:43],v[184:187]
T v[88:89],v22 offset:16384
B v0,s[64:67],s41 offen offset:1024 sc1 lds
M v[188:191],v[108:111],v[40:43],v[188:191]
)"
R"(T v[90:91],v22 offset:17408
AW a89,0
M v[192:195],v[112:115],v[40:43],v[192:195]
B v0,s[64:67],s41 offen offset:2048 sc1 lds
AW a90,0
M v[196:199],v[116:119],v[40:43],v[196:199]
T v[92:93],v23 offset:16384
M v[200:203],v[120:123],v[40:43],v[200:203]
)"
R"(T v[94:95],v23 offset:17408
B v0,s[64:67],s41 offen offset:3072 sc1 lds
M v[204:207],v[124:127],v[40:43],v[204:207]
R v[40:43],v6 offset:16384
AW a91,0
M v[208:211],v[96:99],v[44:47],v[208:211]
SA m0,s46,16384
AW a92,0
)"
R"(M v[212:215],v[100:103],v[44:47],v[212:215]
M v[216:219],v[104:107],v[44:47],v[216:219]
SM s41,16384
AW a93,0
M v[220:223],v[108:111],v[44:47],v[220:223]
M v[224:227],v[112:115],v[44:47],v[224:227]
B v0,s[60:63],s41 offen offset:0 sc1 lds
AW a94,0
)"
R"(M v[228:231],v[116:119],v[44:47],v[228:231]
M v[232:235],v[120:123],v[44:47],v[232:235]
B v0,s[60:63],s41 offen offset:1024 sc1 lds
AW a95,0
M v[236:239],v[124:127],v[44:47],v[236:239]
R v[44:47],v6 offset:18432
W lgkmcnt(11)
M v[48:51],v[64:67],v[32:35],v[48:51]
)"
R"(T v[96:97],v16 offset:24576
B v0,s[60:63],s41 offen offset:2048 sc1 lds
M v[52:55],v[68:71],v[32:35],v[52:55]
T v[98:99],v16 offset:25600
AW a96,0
M v[56:59],v[72:75],v[32:35],v[56:59]
B v0,s[60:63],s41 offen offset:3072 sc1 lds
AW a97,0
)"
R"(M v[60:63],v[76:79],v[32:35],v[60:63]
T v[100:101],v17 offset:24576
W lgkmcnt(5)
M v[128:131],v[80:83],v[32:35],v[128:131]
T v[102:103],v17 offset:25600
SA m0,s46,147456
M v[132:135],v[84:87],v[32:35],v[132:135]
AW a98,0
)"
R"(M v[136:139],v[88:91],v[32:35],v[136:139]
T v[104:105],v18 offset:24576
SM s41,16384
M v[140:143],v[92:95],v[32:35],v[140:143]
R v[32:35],v7 offset:0
T v[106:107],v18 offset:25600
M v[144:147],v[64:67],v[36:39],v[144:147]
B v0,s[64:67],s41 offen offset:0 sc1 lds
)"
R"(AW a99,0
M v[148:151],v[68:71],v[36:39],v[148:151]
T v[108:109],v19 offset:24576
AW a100,0
M v[152:155],v[72:75],v[36:39],v[152:155]
T v[110:111],v19 offset:25600
B v0,s[64:67],s41 offen offset:1024 sc1 lds
M v[156:159],v[76:79],v[36:39],v[156:159]
)"
R"(AW a101,0
M v[160:163],v[80:83],v[36:39],v[160:163]
T v[112:113],v20 offset:24576
B v0,s[64:67],s41 offen offset:2048 sc1 lds
M v[164:167],v[84:87],v[36:39],v[164:167]
T v[114:115],v20 offset:25600
AW a102,0
M v[168:171],v[88:91],v[36:39],v[168:171]
)"
R"(B v0,s[64:67],s41 offen offset:3072 sc1 lds
AW a103,0
M v[172:175],v[92:95],v[36:39],v[172:175]
R v[36:39],v7 offset:2048
T v[116:117],v21 offset:24576
W lgkmcnt(14)
M v[176:179],v[64:67],v[40:43],v[176:179]
T v[118:119],v21 offset:25600
)"
R"(AW a104,0
M v[180:183],v[68:71],v[40:43],v[180:183]
GL a[192:195],v3,s[80:81] sc1
M v[184:187],v[72:75],v[40:43],v[184:187]
T v[120:121],v22 offset:24576
AW a105,0
M v[188:191],v[76:79],v[40:43],v[188:191]
T v[122:123],v22 offset:25600
)"
R"(SA s80,s80,0x1000
M v[192:195],v[80:83],v[40:43],v[192:195]
AW a106,0
M v[196:199],v[84:87],v[40:43],v[196:199]
T v[124:125],v23 offset:24576
s_addc_u32 s81,s81,0
M v[200:203],v[88:91],v[40:43],v[200:203]
T v[126:127],v23 offset:25600
)"
R"(AW a107,0
M v[204:207],v[92:95],v[40:43],v[204:207]
R v[40:43],v7 offset:16384
GL a[196:199],v3,s[80:81] sc1
W lgkmcnt(15)
M v[208:211],v[64:67],v[44:47],v[208:211]
AW a108,0
M v[212:215],v[68:71],v[44:47],v[212:215]
)"
R"(SA s80,s80,0x1000
M v[216:219],v[72:75],v[44:47],v[216:219]
AW a109,0
M v[220:223],v[76:79],v[44:47],v[220:223]
s_addc_u32 s81,s81,0
M v[224:227],v[80:83],v[44:47],v[224:227]
AW a110,0
M v[228:231],v[84:87],v[44:47],v[228:231]
)"
R"(GL a[200:203],v3,s[80:81] sc1
M v[232:235],v[88:91],v[44:47],v[232:235]
AW a111,0
M v[236:239],v[92:95],v[44:47],v[236:239]
R v[44:47],v7 offset:18432
W lgkmcnt(11)
M v[48:51],v[96:99],v[32:35],v[48:51]
SA s80,s80,0x1000
)"
R"(AW a112,0
M v[52:55],v[100:103],v[32:35],v[52:55]
s_addc_u32 s81,s81,0
M v[56:59],v[104:107],v[32:35],v[56:59]
AW a113,0
M v[60:63],v[108:111],v[32:35],v[60:63]
GL a[204:207],v3,s[80:81] sc1
W lgkmcnt(2)
)"
R"(M v[128:131],v[112:115],v[32:35],v[128:131]
SA s80,s80,0x1000
AW a114,0
M v[132:135],v[116:119],v[32:35],v[132:135]
M v[136:139],v[120:123],v[32:35],v[136:139]
s_addc_u32 s81,s81,0
AW a115,0
M v[140:143],v[124:127],v[32:35],v[140:143]
)"
R"(GL a[208:211],v3,s[80:81] sc1
M v[144:147],v[96:99],v[36:39],v[144:147]
AW a116,0
M v[148:151],v[100:103],v[36:39],v[148:151]
SA s80,s80,0x1000
M v[152:155],v[104:107],v[36:39],v[152:155]
s_addc_u32 s81,s81,0
AW a117,0
)"
R"(M v[156:159],v[108:111],v[36:39],v[156:159]
M v[160:163],v[112:115],v[36:39],v[160:163]
GL a[212:215],v3,s[80:81] sc1
AW a118,0
M v[164:167],v[116:119],v[36:39],v[164:167]
SA s80,s80,0x1000
M v[168:171],v[120:123],v[36:39],v[168:171]
AW a119,0
)"
R"(M v[172:175],v[124:127],v[36:39],v[172:175]
s_addc_u32 s81,s81,0
W lgkmcnt(1)
M v[176:179],v[96:99],v[40:43],v[176:179]
GL a[216:219],v3,s[80:81] sc1
AW a120,0
M v[180:183],v[100:103],v[40:43],v[180:183]
M v[184:187],v[104:107],v[40:43],v[184:187]
)"
R"(SA s80,s80,0x1000
AW a121,0
M v[188:191],v[108:111],v[40:43],v[188:191]
s_addc_u32 s81,s81,0
M v[192:195],v[112:115],v[40:43],v[192:195]
AW a122,0
M v[196:199],v[116:119],v[40:43],v[196:199]
GL a[220:223],v3,s[80:81] sc1
)"
R"(M v[200:203],v[120:123],v[40:43],v[200:203]
GL a[224:227],v3,s[78:79] offset:0
AW a123,0
M v[204:207],v[124:127],v[40:43],v[204:207]
W lgkmcnt(0)
M v[208:211],v[96:99],v[44:47],v[208:211]
GL a[228:231],v3,s[78:79] offset:1024
AW a124,0
)"
R"(M v[212:215],v[100:103],v[44:47],v[212:215]
GL a[232:235],v3,s[78:79] offset:2048
M v[216:219],v[104:107],v[44:47],v[216:219]
AW a125,0
M v[220:223],v[108:111],v[44:47],v[220:223]
GL a[236:239],v3,s[78:79] offset:3072
M v[224:227],v[112:115],v[44:47],v[224:227]
AW a126,0
)"
R"(M v[228:231],v[116:119],v[44:47],v[228:231]
M v[232:235],v[120:123],v[44:47],v[232:235]
AW a127,0
M v[236:239],v[124:127],v[44:47],v[236:239]
s_branch KE0_%=
CK2_%=:
s_mov_b64 s[90:91],%16
SA s90,s90,0x5000000
)"
R"(s_addc_u32 s91,s91,0
SM s89,60000
s_mov_b64 exec,-1
s_nop 1
PL3_%=:
global_load_dword v2,v1,s[90:91] sc1
W vmcnt(0)
v_cmp_ne_u32 vcc,s88,v2
)"
R"(s_sub_u32 s89,s89,1
s_cbranch_scc1 PD4_%=
s_cbranch_vccz PD4_%=
s_sleep 1
s_branch PL3_%=
PD4_%=:
s_mov_b64 exec,-1
s_nop 1
)"
R"(s_branch CB2_%=
KE0_%=:
s_getpc_b64 s[90:91]
JE0_%=:
SA s90,s90,KVJ_%=-JE0_%=
s_addc_u32 s91,s91,0
s_setpc_b64 s[90:91]
KV1_%=:
)"
R"(v_xor_b32 v16,0,v28
v_add_u32 v16,16384,v16
v_xor_b32 v17,32,v28
v_add_u32 v17,16384,v17
v_xor_b32 v18,64,v28
v_add_u32 v18,16384,v18
v_xor_b32 v19,96,v28
v_add_u32 v19,16384,v19
)"
R"(v_xor_b32 v20,128,v28
v_add_u32 v20,16384,v20
v_xor_b32 v21,160,v28
v_add_u32 v21,16384,v21
v_xor_b32 v22,192,v28
v_add_u32 v22,16384,v22
v_xor_b32 v23,224,v28
v_add_u32 v23,16384,v23
)"
R"(W vmcnt(18) lgkmcnt(0)
s_barrier
T v[64:65],v16 offset:0
T v[66:67],v16 offset:1024
T v[68:69],v17 offset:0
T v[70:71],v17 offset:1024
T v[72:73],v18 offset:0
T v[74:75],v18 offset:1024
)"
R"(T v[76:77],v19 offset:0
T v[78:79],v19 offset:1024
T v[80:81],v20 offset:0
T v[82:83],v20 offset:1024
T v[84:85],v21 offset:0
T v[86:87],v21 offset:1024
T v[88:89],v22 offset:0
T v[90:91],v22 offset:1024
)"
R"(T v[92:93],v23 offset:0
T v[94:95],v23 offset:1024
R v[32:35],v4 offset:0
R v[36:39],v4 offset:2048
R v[40:43],v4 offset:4096
R v[44:47],v4 offset:6144
R v[48:51],v4 offset:8192
R v[52:55],v4 offset:10240
)"
R"(R v[56:59],v4 offset:12288
R v[60:63],v4 offset:14336
global_load_ushort v208,v9,%7 offset:0
global_load_ushort v209,v9,%7 offset:32
global_load_ushort v210,v9,%7 offset:64
global_load_ushort v211,v9,%7 offset:96
W lgkmcnt(7)
M a[0:3],v[32:35],v[64:67],0
)"
R"(SA m0,s40,65536
M a[4:7],v[32:35],v[68:71],0
T v[96:97],v16 offset:8192
M a[8:11],v[32:35],v[72:75],0
SM s41,256
M a[12:15],v[32:35],v[76:79],0
M a[16:19],v[32:35],v[80:83],0
T v[98:99],v16 offset:9216
)"
R"(B %0,s[48:51],s41 offen lds
M a[20:23],v[32:35],v[84:87],0
M a[24:27],v[32:35],v[88:91],0
M a[28:31],v[32:35],v[92:95],0
R v[32:35],v5 offset:0
T v[100:101],v17 offset:8192
W lgkmcnt(10)
M a[32:35],v[36:39],v[64:67],0
)"
R"(SA m0,s40,69632
M a[36:39],v[36:39],v[68:71],0
SM s41,33024
M a[40:43],v[36:39],v[72:75],0
T v[102:103],v17 offset:9216
M a[44:47],v[36:39],v[76:79],0
M a[48:51],v[36:39],v[80:83],0
B %0,s[48:51],s41 offen lds
)"
R"(M a[52:55],v[36:39],v[84:87],0
T v[104:105],v18 offset:8192
M a[56:59],v[36:39],v[88:91],0
SA m0,s40,73728
M a[60:63],v[36:39],v[92:95],0
R v[36:39],v5 offset:2048
W lgkmcnt(12)
M a[64:67],v[40:43],v[64:67],0
)"
R"(T v[106:107],v18 offset:9216
SM s41,65792
M a[68:71],v[40:43],v[68:71],0
M a[72:75],v[40:43],v[72:75],0
M a[76:79],v[40:43],v[76:79],0
T v[108:109],v19 offset:8192
B %0,s[48:51],s41 offen lds
M a[80:83],v[40:43],v[80:83],0
)"
R"(M a[84:87],v[40:43],v[84:87],0
SA m0,s40,77824
M a[88:91],v[40:43],v[88:91],0
T v[110:111],v19 offset:9216
M a[92:95],v[40:43],v[92:95],0
R v[40:43],v5 offset:4096
W lgkmcnt(15)
M a[96:99],v[44:47],v[64:67],0
)"
R"(SM s41,98560
M a[100:103],v[44:47],v[68:71],0
T v[112:113],v20 offset:8192
M a[104:107],v[44:47],v[72:75],0
B %0,s[48:51],s41 offen lds
M a[108:111],v[44:47],v[76:79],0
M a[112:115],v[44:47],v[80:83],0
T v[114:115],v20 offset:9216
)"
R"(SA m0,s40,81920
M a[116:119],v[44:47],v[84:87],0
M a[120:123],v[44:47],v[88:91],0
M a[124:127],v[44:47],v[92:95],0
R v[44:47],v5 offset:6144
T v[116:117],v21 offset:8192
W lgkmcnt(15)
M a[128:131],v[48:51],v[64:67],0
)"
R"(SM s41,131328
M a[132:135],v[48:51],v[68:71],0
B %0,s[48:51],s41 offen lds
M a[136:139],v[48:51],v[72:75],0
T v[118:119],v21 offset:9216
M a[140:143],v[48:51],v[76:79],0
M a[144:147],v[48:51],v[80:83],0
SA m0,s40,86016
)"
R"(M a[148:151],v[48:51],v[84:87],0
T v[120:121],v22 offset:8192
M a[152:155],v[48:51],v[88:91],0
SM s41,164096
M a[156:159],v[48:51],v[92:95],0
R v[48:51],v5 offset:8192
M a[160:163],v[52:55],v[64:67],0
T v[122:123],v22 offset:9216
)"
R"(B %0,s[48:51],s41 offen lds
M a[164:167],v[52:55],v[68:71],0
M a[168:171],v[52:55],v[72:75],0
M a[172:175],v[52:55],v[76:79],0
T v[124:125],v23 offset:8192
SA m0,s40,90112
M a[176:179],v[52:55],v[80:83],0
M a[180:183],v[52:55],v[84:87],0
)"
R"(SM s41,196864
M a[184:187],v[52:55],v[88:91],0
T v[126:127],v23 offset:9216
M a[188:191],v[52:55],v[92:95],0
R v[52:55],v5 offset:10240
M a[192:195],v[56:59],v[64:67],0
B %0,s[48:51],s41 offen lds
M a[196:199],v[56:59],v[68:71],0
)"
R"(M a[200:203],v[56:59],v[72:75],0
SA m0,s40,94208
M a[204:207],v[56:59],v[76:79],0
M a[208:211],v[56:59],v[80:83],0
SM s41,229632
M a[212:215],v[56:59],v[84:87],0
M a[216:219],v[56:59],v[88:91],0
M a[220:223],v[56:59],v[92:95],0
)"
R"(R v[56:59],v5 offset:12288
B %0,s[48:51],s41 offen lds
M a[224:227],v[60:63],v[64:67],0
M a[228:231],v[60:63],v[68:71],0
M a[232:235],v[60:63],v[72:75],0
M a[236:239],v[60:63],v[76:79],0
M a[240:243],v[60:63],v[80:83],0
M a[244:247],v[60:63],v[84:87],0
)"
R"(M a[248:251],v[60:63],v[88:91],0
M a[252:255],v[60:63],v[92:95],0
R v[60:63],v5 offset:14336
W vmcnt(12) lgkmcnt(0)
s_barrier
M a[0:3],v[32:35],v[96:99],a[0:3]
SA m0,s40,98304
M a[4:7],v[32:35],v[100:103],a[4:7]
)"
R"(T v[64:65],v16 offset:32768
M a[8:11],v[32:35],v[104:107],a[8:11]
SM s41,1048576
M a[12:15],v[32:35],v[108:111],a[12:15]
M a[16:19],v[32:35],v[112:115],a[16:19]
T v[66:67],v16 offset:33792
M a[20:23],v[32:35],v[116:119],a[20:23]
B %1,s[60:63],s41 offen lds
)"
R"(M a[24:27],v[32:35],v[120:123],a[24:27]
M a[28:31],v[32:35],v[124:127],a[28:31]
R v[32:35],v4 offset:32768
T v[68:69],v17 offset:32768
M a[32:35],v[36:39],v[96:99],a[32:35]
SA m0,s40,114688
M a[36:39],v[36:39],v[100:103],a[36:39]
M a[40:43],v[36:39],v[104:107],a[40:43]
)"
R"(T v[70:71],v17 offset:33792
SM s41,1048576
M a[44:47],v[36:39],v[108:111],a[44:47]
M a[48:51],v[36:39],v[112:115],a[48:51]
M a[52:55],v[36:39],v[116:119],a[52:55]
T v[72:73],v18 offset:32768
B %1,s[64:67],s41 offen lds
M a[56:59],v[36:39],v[120:123],a[56:59]
)"
R"(M a[60:63],v[36:39],v[124:127],a[60:63]
R v[36:39],v4 offset:34816
M a[64:67],v[40:43],v[96:99],a[64:67]
T v[74:75],v18 offset:33792
SA m0,s40,102400
M a[68:71],v[40:43],v[100:103],a[68:71]
M a[72:75],v[40:43],v[104:107],a[72:75]
SM s41,1179648
)"
R"(M a[76:79],v[40:43],v[108:111],a[76:79]
T v[76:77],v19 offset:32768
M a[80:83],v[40:43],v[112:115],a[80:83]
M a[84:87],v[40:43],v[116:119],a[84:87]
B %1,s[60:63],s41 offen lds
M a[88:91],v[40:43],v[120:123],a[88:91]
T v[78:79],v19 offset:33792
M a[92:95],v[40:43],v[124:127],a[92:95]
)"
R"(R v[40:43],v4 offset:36864
M a[96:99],v[44:47],v[96:99],a[96:99]
SA m0,s40,118784
M a[100:103],v[44:47],v[100:103],a[100:103]
T v[80:81],v20 offset:32768
M a[104:107],v[44:47],v[104:107],a[104:107]
SM s41,1179648
M a[108:111],v[44:47],v[108:111],a[108:111]
)"
R"(M a[112:115],v[44:47],v[112:115],a[112:115]
T v[82:83],v20 offset:33792
M a[116:119],v[44:47],v[116:119],a[116:119]
B %1,s[64:67],s41 offen lds
M a[120:123],v[44:47],v[120:123],a[120:123]
M a[124:127],v[44:47],v[124:127],a[124:127]
R v[44:47],v4 offset:38912
T v[84:85],v21 offset:32768
)"
R"(M a[128:131],v[48:51],v[96:99],a[128:131]
SA m0,s40,106496
M a[132:135],v[48:51],v[100:103],a[132:135]
M a[136:139],v[48:51],v[104:107],a[136:139]
T v[86:87],v21 offset:33792
SM s41,1310720
M a[140:143],v[48:51],v[108:111],a[140:143]
M a[144:147],v[48:51],v[112:115],a[144:147]
)"
R"(M a[148:151],v[48:51],v[116:119],a[148:151]
T v[88:89],v22 offset:32768
B %1,s[60:63],s41 offen lds
M a[152:155],v[48:51],v[120:123],a[152:155]
M a[156:159],v[48:51],v[124:127],a[156:159]
R v[48:51],v4 offset:40960
M a[160:163],v[52:55],v[96:99],a[160:163]
T v[90:91],v22 offset:33792
)"
R"(SA m0,s40,122880
M a[164:167],v[52:55],v[100:103],a[164:167]
M a[168:171],v[52:55],v[104:107],a[168:171]
SM s41,1310720
M a[172:175],v[52:55],v[108:111],a[172:175]
T v[92:93],v23 offset:32768
M a[176:179],v[52:55],v[112:115],a[176:179]
M a[180:183],v[52:55],v[116:119],a[180:183]
)"
R"(B %1,s[64:67],s41 offen lds
M a[184:187],v[52:55],v[120:123],a[184:187]
T v[94:95],v23 offset:33792
M a[188:191],v[52:55],v[124:127],a[188:191]
R v[52:55],v4 offset:43008
M a[192:195],v[56:59],v[96:99],a[192:195]
SA m0,s40,110592
M a[196:199],v[56:59],v[100:103],a[196:199]
)"
R"(M a[200:203],v[56:59],v[104:107],a[200:203]
SM s41,1441792
M a[204:207],v[56:59],v[108:111],a[204:207]
M a[208:211],v[56:59],v[112:115],a[208:211]
M a[212:215],v[56:59],v[116:119],a[212:215]
B %1,s[60:63],s41 offen lds
M a[216:219],v[56:59],v[120:123],a[216:219]
M a[220:223],v[56:59],v[124:127],a[220:223]
)"
R"(R v[56:59],v4 offset:45056
M a[224:227],v[60:63],v[96:99],a[224:227]
SA m0,s40,126976
M a[228:231],v[60:63],v[100:103],a[228:231]
M a[232:235],v[60:63],v[104:107],a[232:235]
SM s41,1441792
M a[236:239],v[60:63],v[108:111],a[236:239]
M a[240:243],v[60:63],v[112:115],a[240:243]
)"
R"(M a[244:247],v[60:63],v[116:119],a[244:247]
B %1,s[64:67],s41 offen lds
M a[248:251],v[60:63],v[120:123],a[248:251]
M a[252:255],v[60:63],v[124:127],a[252:255]
R v[60:63],v4 offset:47104
W lgkmcnt(14)
M a[0:3],v[32:35],v[64:67],a[0:3]
SA m0,s40,0
)"
R"(M a[4:7],v[32:35],v[68:71],a[4:7]
T v[96:97],v16 offset:40960
M a[8:11],v[32:35],v[72:75],a[8:11]
SM s41,384
M a[12:15],v[32:35],v[76:79],a[12:15]
W lgkmcnt(4)
M a[16:19],v[32:35],v[80:83],a[16:19]
T v[98:99],v16 offset:41984
)"
R"(B %0,s[48:51],s41 offen lds
M a[20:23],v[32:35],v[84:87],a[20:23]
M a[24:27],v[32:35],v[88:91],a[24:27]
M a[28:31],v[32:35],v[92:95],a[28:31]
R v[32:35],v5 offset:32768
T v[100:101],v17 offset:40960
M a[32:35],v[36:39],v[64:67],a[32:35]
SA m0,s40,4096
)"
R"(M a[36:39],v[36:39],v[68:71],a[36:39]
SM s41,33152
M a[40:43],v[36:39],v[72:75],a[40:43]
T v[102:103],v17 offset:41984
M a[44:47],v[36:39],v[76:79],a[44:47]
M a[48:51],v[36:39],v[80:83],a[48:51]
B %0,s[48:51],s41 offen lds
M a[52:55],v[36:39],v[84:87],a[52:55]
)"
R"(T v[104:105],v18 offset:40960
M a[56:59],v[36:39],v[88:91],a[56:59]
SA m0,s40,8192
M a[60:63],v[36:39],v[92:95],a[60:63]
R v[36:39],v5 offset:34816
M a[64:67],v[40:43],v[64:67],a[64:67]
T v[106:107],v18 offset:41984
SM s41,65920
)"
R"(M a[68:71],v[40:43],v[68:71],a[68:71]
M a[72:75],v[40:43],v[72:75],a[72:75]
M a[76:79],v[40:43],v[76:79],a[76:79]
T v[108:109],v19 offset:40960
B %0,s[48:51],s41 offen lds
M a[80:83],v[40:43],v[80:83],a[80:83]
M a[84:87],v[40:43],v[84:87],a[84:87]
SA m0,s40,12288
)"
R"(M a[88:91],v[40:43],v[88:91],a[88:91]
T v[110:111],v19 offset:41984
M a[92:95],v[40:43],v[92:95],a[92:95]
R v[40:43],v5 offset:36864
M a[96:99],v[44:47],v[64:67],a[96:99]
SM s41,98688
M a[100:103],v[44:47],v[68:71],a[100:103]
T v[112:113],v20 offset:40960
)"
R"(M a[104:107],v[44:47],v[72:75],a[104:107]
B %0,s[48:51],s41 offen lds
M a[108:111],v[44:47],v[76:79],a[108:111]
M a[112:115],v[44:47],v[80:83],a[112:115]
T v[114:115],v20 offset:41984
SA m0,s40,16384
M a[116:119],v[44:47],v[84:87],a[116:119]
M a[120:123],v[44:47],v[88:91],a[120:123]
)"
R"(M a[124:127],v[44:47],v[92:95],a[124:127]
R v[44:47],v5 offset:38912
T v[116:117],v21 offset:40960
M a[128:131],v[48:51],v[64:67],a[128:131]
SM s41,131456
M a[132:135],v[48:51],v[68:71],a[132:135]
B %0,s[48:51],s41 offen lds
M a[136:139],v[48:51],v[72:75],a[136:139]
)"
R"(T v[118:119],v21 offset:41984
M a[140:143],v[48:51],v[76:79],a[140:143]
M a[144:147],v[48:51],v[80:83],a[144:147]
SA m0,s40,20480
M a[148:151],v[48:51],v[84:87],a[148:151]
T v[120:121],v22 offset:40960
M a[152:155],v[48:51],v[88:91],a[152:155]
SM s41,164224
)"
R"(M a[156:159],v[48:51],v[92:95],a[156:159]
R v[48:51],v5 offset:40960
W lgkmcnt(15)
M a[160:163],v[52:55],v[64:67],a[160:163]
T v[122:123],v22 offset:41984
B %0,s[48:51],s41 offen lds
M a[164:167],v[52:55],v[68:71],a[164:167]
M a[168:171],v[52:55],v[72:75],a[168:171]
)"
R"(M a[172:175],v[52:55],v[76:79],a[172:175]
T v[124:125],v23 offset:40960
SA m0,s40,24576
M a[176:179],v[52:55],v[80:83],a[176:179]
M a[180:183],v[52:55],v[84:87],a[180:183]
SM s41,196992
M a[184:187],v[52:55],v[88:91],a[184:187]
T v[126:127],v23 offset:41984
)"
R"(M a[188:191],v[52:55],v[92:95],a[188:191]
R v[52:55],v5 offset:43008
M a[192:195],v[56:59],v[64:67],a[192:195]
B %0,s[48:51],s41 offen lds
M a[196:199],v[56:59],v[68:71],a[196:199]
M a[200:203],v[56:59],v[72:75],a[200:203]
SA m0,s40,28672
M a[204:207],v[56:59],v[76:79],a[204:207]
)"
R"(M a[208:211],v[56:59],v[80:83],a[208:211]
SM s41,229760
M a[212:215],v[56:59],v[84:87],a[212:215]
M a[216:219],v[56:59],v[88:91],a[216:219]
M a[220:223],v[56:59],v[92:95],a[220:223]
R v[56:59],v5 offset:45056
B %0,s[48:51],s41 offen lds
M a[224:227],v[60:63],v[64:67],a[224:227]
)"
R"(M a[228:231],v[60:63],v[68:71],a[228:231]
M a[232:235],v[60:63],v[72:75],a[232:235]
M a[236:239],v[60:63],v[76:79],a[236:239]
M a[240:243],v[60:63],v[80:83],a[240:243]
M a[244:247],v[60:63],v[84:87],a[244:247]
M a[248:251],v[60:63],v[88:91],a[248:251]
M a[252:255],v[60:63],v[92:95],a[252:255]
R v[60:63],v5 offset:47104
)"
R"(W vmcnt(8) lgkmcnt(0)
s_barrier
M a[0:3],v[32:35],v[96:99],a[0:3]
SA m0,s40,131072
M a[4:7],v[32:35],v[100:103],a[4:7]
T v[64:65],v16 offset:0
M a[8:11],v[32:35],v[104:107],a[8:11]
SM s41,1572864
)"
R"(M a[12:15],v[32:35],v[108:111],a[12:15]
M a[16:19],v[32:35],v[112:115],a[16:19]
T v[66:67],v16 offset:1024
M a[20:23],v[32:35],v[116:119],a[20:23]
B %1,s[60:63],s41 offen lds
M a[24:27],v[32:35],v[120:123],a[24:27]
M a[28:31],v[32:35],v[124:127],a[28:31]
R v[32:35],v6 offset:0
)"
R"(T v[68:69],v17 offset:0
M a[32:35],v[36:39],v[96:99],a[32:35]
SA m0,s40,147456
M a[36:39],v[36:39],v[100:103],a[36:39]
M a[40:43],v[36:39],v[104:107],a[40:43]
T v[70:71],v17 offset:1024
SM s41,1572864
M a[44:47],v[36:39],v[108:111],a[44:47]
)"
R"(M a[48:51],v[36:39],v[112:115],a[48:51]
M a[52:55],v[36:39],v[116:119],a[52:55]
T v[72:73],v18 offset:0
B %1,s[64:67],s41 offen lds
M a[56:59],v[36:39],v[120:123],a[56:59]
M a[60:63],v[36:39],v[124:127],a[60:63]
R v[36:39],v6 offset:2048
M a[64:67],v[40:43],v[96:99],a[64:67]
)"
R"(T v[74:75],v18 offset:1024
SA m0,s40,135168
M a[68:71],v[40:43],v[100:103],a[68:71]
M a[72:75],v[40:43],v[104:107],a[72:75]
SM s41,1703936
M a[76:79],v[40:43],v[108:111],a[76:79]
T v[76:77],v19 offset:0
M a[80:83],v[40:43],v[112:115],a[80:83]
)"
R"(M a[84:87],v[40:43],v[116:119],a[84:87]
B %1,s[60:63],s41 offen lds
M a[88:91],v[40:43],v[120:123],a[88:91]
T v[78:79],v19 offset:1024
M a[92:95],v[40:43],v[124:127],a[92:95]
R v[40:43],v6 offset:4096
M a[96:99],v[44:47],v[96:99],a[96:99]
SA m0,s40,151552
)"
R"(M a[100:103],v[44:47],v[100:103],a[100:103]
T v[80:81],v20 offset:0
M a[104:107],v[44:47],v[104:107],a[104:107]
SM s41,1703936
M a[108:111],v[44:47],v[108:111],a[108:111]
M a[112:115],v[44:47],v[112:115],a[112:115]
T v[82:83],v20 offset:1024
M a[116:119],v[44:47],v[116:119],a[116:119]
)"
R"(B %1,s[64:67],s41 offen lds
M a[120:123],v[44:47],v[120:123],a[120:123]
M a[124:127],v[44:47],v[124:127],a[124:127]
R v[44:47],v6 offset:6144
T v[84:85],v21 offset:0
M a[128:131],v[48:51],v[96:99],a[128:131]
SA m0,s40,139264
M a[132:135],v[48:51],v[100:103],a[132:135]
)"
R"(M a[136:139],v[48:51],v[104:107],a[136:139]
T v[86:87],v21 offset:1024
SM s41,1835008
M a[140:143],v[48:51],v[108:111],a[140:143]
M a[144:147],v[48:51],v[112:115],a[144:147]
M a[148:151],v[48:51],v[116:119],a[148:151]
T v[88:89],v22 offset:0
B %1,s[60:63],s41 offen lds
)"
R"(M a[152:155],v[48:51],v[120:123],a[152:155]
M a[156:159],v[48:51],v[124:127],a[156:159]
R v[48:51],v6 offset:8192
M a[160:163],v[52:55],v[96:99],a[160:163]
T v[90:91],v22 offset:1024
SA m0,s40,155648
M a[164:167],v[52:55],v[100:103],a[164:167]
M a[168:171],v[52:55],v[104:107],a[168:171]
)"
R"(SM s41,1835008
M a[172:175],v[52:55],v[108:111],a[172:175]
T v[92:93],v23 offset:0
M a[176:179],v[52:55],v[112:115],a[176:179]
M a[180:183],v[52:55],v[116:119],a[180:183]
B %1,s[64:67],s41 offen lds
M a[184:187],v[52:55],v[120:123],a[184:187]
T v[94:95],v23 offset:1024
)"
R"(M a[188:191],v[52:55],v[124:127],a[188:191]
R v[52:55],v6 offset:10240
M a[192:195],v[56:59],v[96:99],a[192:195]
SA m0,s40,143360
M a[196:199],v[56:59],v[100:103],a[196:199]
M a[200:203],v[56:59],v[104:107],a[200:203]
SM s41,1966080
M a[204:207],v[56:59],v[108:111],a[204:207]
)"
R"(M a[208:211],v[56:59],v[112:115],a[208:211]
M a[212:215],v[56:59],v[116:119],a[212:215]
B %1,s[60:63],s41 offen lds
M a[216:219],v[56:59],v[120:123],a[216:219]
M a[220:223],v[56:59],v[124:127],a[220:223]
R v[56:59],v6 offset:12288
M a[224:227],v[60:63],v[96:99],a[224:227]
SA m0,s40,159744
)"
R"(M a[228:231],v[60:63],v[100:103],a[228:231]
M a[232:235],v[60:63],v[104:107],a[232:235]
SM s41,1966080
M a[236:239],v[60:63],v[108:111],a[236:239]
M a[240:243],v[60:63],v[112:115],a[240:243]
M a[244:247],v[60:63],v[116:119],a[244:247]
B %1,s[64:67],s41 offen lds
M a[248:251],v[60:63],v[120:123],a[248:251]
)"
R"(M a[252:255],v[60:63],v[124:127],a[252:255]
R v[60:63],v6 offset:14336
W lgkmcnt(14)
M a[0:3],v[32:35],v[64:67],a[0:3]
SA m0,s40,32768
M a[4:7],v[32:35],v[68:71],a[4:7]
T v[96:97],v16 offset:8192
M a[8:11],v[32:35],v[72:75],a[8:11]
)"
R"(SM s41,512
M a[12:15],v[32:35],v[76:79],a[12:15]
W lgkmcnt(4)
M a[16:19],v[32:35],v[80:83],a[16:19]
T v[98:99],v16 offset:9216
B %0,s[48:51],s41 offen lds
M a[20:23],v[32:35],v[84:87],a[20:23]
M a[24:27],v[32:35],v[88:91],a[24:27]
)"
R"(M a[28:31],v[32:35],v[92:95],a[28:31]
R v[32:35],v7 offset:0
T v[100:101],v17 offset:8192
M a[32:35],v[36:39],v[64:67],a[32:35]
SA m0,s40,36864
M a[36:39],v[36:39],v[68:71],a[36:39]
SM s41,33280
M a[40:43],v[36:39],v[72:75],a[40:43]
)"
R"(T v[102:103],v17 offset:9216
M a[44:47],v[36:39],v[76:79],a[44:47]
M a[48:51],v[36:39],v[80:83],a[48:51]
B %0,s[48:51],s41 offen lds
M a[52:55],v[36:39],v[84:87],a[52:55]
T v[104:105],v18 offset:8192
M a[56:59],v[36:39],v[88:91],a[56:59]
SA m0,s40,40960
)"
R"(M a[60:63],v[36:39],v[92:95],a[60:63]
R v[36:39],v7 offset:2048
M a[64:67],v[40:43],v[64:67],a[64:67]
T v[106:107],v18 offset:9216
SM s41,66048
M a[68:71],v[40:43],v[68:71],a[68:71]
M a[72:75],v[40:43],v[72:75],a[72:75]
M a[76:79],v[40:43],v[76:79],a[76:79]
)"
R"(T v[108:109],v19 offset:8192
B %0,s[48:51],s41 offen lds
M a[80:83],v[40:43],v[80:83],a[80:83]
M a[84:87],v[40:43],v[84:87],a[84:87]
SA m0,s40,45056
M a[88:91],v[40:43],v[88:91],a[88:91]
T v[110:111],v19 offset:9216
M a[92:95],v[40:43],v[92:95],a[92:95]
)"
R"(R v[40:43],v7 offset:4096
M a[96:99],v[44:47],v[64:67],a[96:99]
SM s41,98816
M a[100:103],v[44:47],v[68:71],a[100:103]
T v[112:113],v20 offset:8192
M a[104:107],v[44:47],v[72:75],a[104:107]
B %0,s[48:51],s41 offen lds
M a[108:111],v[44:47],v[76:79],a[108:111]
)"
R"(M a[112:115],v[44:47],v[80:83],a[112:115]
T v[114:115],v20 offset:9216
SA m0,s40,49152
M a[116:119],v[44:47],v[84:87],a[116:119]
M a[120:123],v[44:47],v[88:91],a[120:123]
M a[124:127],v[44:47],v[92:95],a[124:127]
R v[44:47],v7 offset:6144
T v[116:117],v21 offset:8192
)"
R"(M a[128:131],v[48:51],v[64:67],a[128:131]
SM s41,131584
M a[132:135],v[48:51],v[68:71],a[132:135]
B %0,s[48:51],s41 offen lds
M a[136:139],v[48:51],v[72:75],a[136:139]
T v[118:119],v21 offset:9216
M a[140:143],v[48:51],v[76:79],a[140:143]
M a[144:147],v[48:51],v[80:83],a[144:147]
)"
R"(SA m0,s40,53248
M a[148:151],v[48:51],v[84:87],a[148:151]
T v[120:121],v22 offset:8192
M a[152:155],v[48:51],v[88:91],a[152:155]
SM s41,164352
M a[156:159],v[48:51],v[92:95],a[156:159]
R v[48:51],v7 offset:8192
W lgkmcnt(15)
)"
R"(M a[160:163],v[52:55],v[64:67],a[160:163]
T v[122:123],v22 offset:9216
B %0,s[48:51],s41 offen lds
M a[164:167],v[52:55],v[68:71],a[164:167]
M a[168:171],v[52:55],v[72:75],a[168:171]
M a[172:175],v[52:55],v[76:79],a[172:175]
T v[124:125],v23 offset:8192
SA m0,s40,57344
)"
R"(M a[176:179],v[52:55],v[80:83],a[176:179]
M a[180:183],v[52:55],v[84:87],a[180:183]
SM s41,197120
M a[184:187],v[52:55],v[88:91],a[184:187]
T v[126:127],v23 offset:9216
M a[188:191],v[52:55],v[92:95],a[188:191]
R v[52:55],v7 offset:10240
M a[192:195],v[56:59],v[64:67],a[192:195]
)"
R"(B %0,s[48:51],s41 offen lds
M a[196:199],v[56:59],v[68:71],a[196:199]
M a[200:203],v[56:59],v[72:75],a[200:203]
SA m0,s40,61440
M a[204:207],v[56:59],v[76:79],a[204:207]
M a[208:211],v[56:59],v[80:83],a[208:211]
SM s41,229888
M a[212:215],v[56:59],v[84:87],a[212:215]
)"
R"(M a[216:219],v[56:59],v[88:91],a[216:219]
M a[220:223],v[56:59],v[92:95],a[220:223]
R v[56:59],v7 offset:12288
B %0,s[48:51],s41 offen lds
M a[224:227],v[60:63],v[64:67],a[224:227]
M a[228:231],v[60:63],v[68:71],a[228:231]
M a[232:235],v[60:63],v[72:75],a[232:235]
M a[236:239],v[60:63],v[76:79],a[236:239]
)"
R"(M a[240:243],v[60:63],v[80:83],a[240:243]
M a[244:247],v[60:63],v[84:87],a[244:247]
M a[248:251],v[60:63],v[88:91],a[248:251]
M a[252:255],v[60:63],v[92:95],a[252:255]
R v[60:63],v7 offset:14336
W vmcnt(8) lgkmcnt(0)
s_barrier
M a[0:3],v[32:35],v[96:99],a[0:3]
)"
R"(SA m0,s40,98304
M a[4:7],v[32:35],v[100:103],a[4:7]
T v[64:65],v16 offset:32768
M a[8:11],v[32:35],v[104:107],a[8:11]
SM s41,2097152
M a[12:15],v[32:35],v[108:111],a[12:15]
M a[16:19],v[32:35],v[112:115],a[16:19]
T v[66:67],v16 offset:33792
)"
R"(M a[20:23],v[32:35],v[116:119],a[20:23]
B %1,s[60:63],s41 offen lds
M a[24:27],v[32:35],v[120:123],a[24:27]
M a[28:31],v[32:35],v[124:127],a[28:31]
R v[32:35],v4 offset:0
T v[68:69],v17 offset:32768
M a[32:35],v[36:39],v[96:99],a[32:35]
SA m0,s40,114688
)"
R"(M a[36:39],v[36:39],v[100:103],a[36:39]
M a[40:43],v[36:39],v[104:107],a[40:43]
T v[70:71],v17 offset:33792
SM s41,2097152
M a[44:47],v[36:39],v[108:111],a[44:47]
M a[48:51],v[36:39],v[112:115],a[48:51]
M a[52:55],v[36:39],v[116:119],a[52:55]
T v[72:73],v18 offset:32768
)"
R"(B %1,s[64:67],s41 offen lds
M a[56:59],v[36:39],v[120:123],a[56:59]
M a[60:63],v[36:39],v[124:127],a[60:63]
R v[36:39],v4 offset:2048
M a[64:67],v[40:43],v[96:99],a[64:67]
T v[74:75],v18 offset:33792
SA m0,s40,102400
M a[68:71],v[40:43],v[100:103],a[68:71]
)"
R"(M a[72:75],v[40:43],v[104:107],a[72:75]
SM s41,2228224
M a[76:79],v[40:43],v[108:111],a[76:79]
T v[76:77],v19 offset:32768
M a[80:83],v[40:43],v[112:115],a[80:83]
M a[84:87],v[40:43],v[116:119],a[84:87]
B %1,s[60:63],s41 offen lds
M a[88:91],v[40:43],v[120:123],a[88:91]
)"
R"(T v[78:79],v19 offset:33792
M a[92:95],v[40:43],v[124:127],a[92:95]
R v[40:43],v4 offset:4096
M a[96:99],v[44:47],v[96:99],a[96:99]
SA m0,s40,118784
M a[100:103],v[44:47],v[100:103],a[100:103]
T v[80:81],v20 offset:32768
M a[104:107],v[44:47],v[104:107],a[104:107]
)"
R"(SM s41,2228224
M a[108:111],v[44:47],v[108:111],a[108:111]
M a[112:115],v[44:47],v[112:115],a[112:115]
T v[82:83],v20 offset:33792
M a[116:119],v[44:47],v[116:119],a[116:119]
B %1,s[64:67],s41 offen lds
M a[120:123],v[44:47],v[120:123],a[120:123]
M a[124:127],v[44:47],v[124:127],a[124:127]
)"
R"(R v[44:47],v4 offset:6144
T v[84:85],v21 offset:32768
M a[128:131],v[48:51],v[96:99],a[128:131]
SA m0,s40,106496
M a[132:135],v[48:51],v[100:103],a[132:135]
M a[136:139],v[48:51],v[104:107],a[136:139]
T v[86:87],v21 offset:33792
SM s41,2359296
)"
R"(M a[140:143],v[48:51],v[108:111],a[140:143]
M a[144:147],v[48:51],v[112:115],a[144:147]
M a[148:151],v[48:51],v[116:119],a[148:151]
T v[88:89],v22 offset:32768
B %1,s[60:63],s41 offen lds
M a[152:155],v[48:51],v[120:123],a[152:155]
M a[156:159],v[48:51],v[124:127],a[156:159]
R v[48:51],v4 offset:8192
)"
R"(M a[160:163],v[52:55],v[96:99],a[160:163]
T v[90:91],v22 offset:33792
SA m0,s40,122880
M a[164:167],v[52:55],v[100:103],a[164:167]
M a[168:171],v[52:55],v[104:107],a[168:171]
SM s41,2359296
M a[172:175],v[52:55],v[108:111],a[172:175]
T v[92:93],v23 offset:32768
)"
R"(M a[176:179],v[52:55],v[112:115],a[176:179]
M a[180:183],v[52:55],v[116:119],a[180:183]
B %1,s[64:67],s41 offen lds
M a[184:187],v[52:55],v[120:123],a[184:187]
T v[94:95],v23 offset:33792
M a[188:191],v[52:55],v[124:127],a[188:191]
R v[52:55],v4 offset:10240
M a[192:195],v[56:59],v[96:99],a[192:195]
)"
R"(SA m0,s40,110592
M a[196:199],v[56:59],v[100:103],a[196:199]
M a[200:203],v[56:59],v[104:107],a[200:203]
SM s41,2490368
M a[204:207],v[56:59],v[108:111],a[204:207]
M a[208:211],v[56:59],v[112:115],a[208:211]
M a[212:215],v[56:59],v[116:119],a[212:215]
B %1,s[60:63],s41 offen lds
)"
R"(M a[216:219],v[56:59],v[120:123],a[216:219]
M a[220:223],v[56:59],v[124:127],a[220:223]
R v[56:59],v4 offset:12288
M a[224:227],v[60:63],v[96:99],a[224:227]
SA m0,s40,126976
M a[228:231],v[60:63],v[100:103],a[228:231]
M a[232:235],v[60:63],v[104:107],a[232:235]
SM s41,2490368
)"
R"(M a[236:239],v[60:63],v[108:111],a[236:239]
M a[240:243],v[60:63],v[112:115],a[240:243]
M a[244:247],v[60:63],v[116:119],a[244:247]
B %1,s[64:67],s41 offen lds
M a[248:251],v[60:63],v[120:123],a[248:251]
M a[252:255],v[60:63],v[124:127],a[252:255]
R v[60:63],v4 offset:14336
W lgkmcnt(14)
)"
R"(M a[0:3],v[32:35],v[64:67],a[0:3]
SA m0,s40,65536
M a[4:7],v[32:35],v[68:71],a[4:7]
T v[96:97],v16 offset:40960
M a[8:11],v[32:35],v[72:75],a[8:11]
SM s41,640
M a[12:15],v[32:35],v[76:79],a[12:15]
W lgkmcnt(4)
)"
R"(M a[16:19],v[32:35],v[80:83],a[16:19]
T v[98:99],v16 offset:41984
B %0,s[48:51],s41 offen lds
M a[20:23],v[32:35],v[84:87],a[20:23]
M a[24:27],v[32:35],v[88:91],a[24:27]
M a[28:31],v[32:35],v[92:95],a[28:31]
R v[32:35],v5 offset:0
T v[100:101],v17 offset:40960
)"
R"(M a[32:35],v[36:39],v[64:67],a[32:35]
SA m0,s40,69632
M a[36:39],v[36:39],v[68:71],a[36:39]
SM s41,33408
M a[40:43],v[36:39],v[72:75],a[40:43]
T v[102:103],v17 offset:41984
M a[44:47],v[36:39],v[76:79],a[44:47]
M a[48:51],v[36:39],v[80:83],a[48:51]
)"
R"(B %0,s[48:51],s41 offen lds
M a[52:55],v[36:39],v[84:87],a[52:55]
T v[104:105],v18 offset:40960
M a[56:59],v[36:39],v[88:91],a[56:59]
SA m0,s40,73728
M a[60:63],v[36:39],v[92:95],a[60:63]
R v[36:39],v5 offset:2048
M a[64:67],v[40:43],v[64:67],a[64:67]
)"
R"(T v[106:107],v18 offset:41984
SM s41,66176
M a[68:71],v[40:43],v[68:71],a[68:71]
M a[72:75],v[40:43],v[72:75],a[72:75]
M a[76:79],v[40:43],v[76:79],a[76:79]
T v[108:109],v19 offset:40960
B %0,s[48:51],s41 offen lds
M a[80:83],v[40:43],v[80:83],a[80:83]
)"
R"(M a[84:87],v[40:43],v[84:87],a[84:87]
SA m0,s40,77824
M a[88:91],v[40:43],v[88:91],a[88:91]
T v[110:111],v19 offset:41984
M a[92:95],v[40:43],v[92:95],a[92:95]
R v[40:43],v5 offset:4096
M a[96:99],v[44:47],v[64:67],a[96:99]
SM s41,98944
)"
R"(M a[100:103],v[44:47],v[68:71],a[100:103]
T v[112:113],v20 offset:40960
M a[104:107],v[44:47],v[72:75],a[104:107]
B %0,s[48:51],s41 offen lds
M a[108:111],v[44:47],v[76:79],a[108:111]
M a[112:115],v[44:47],v[80:83],a[112:115]
T v[114:115],v20 offset:41984
SA m0,s40,81920
)"
R"(M a[116:119],v[44:47],v[84:87],a[116:119]
M a[120:123],v[44:47],v[88:91],a[120:123]
M a[124:127],v[44:47],v[92:95],a[124:127]
R v[44:47],v5 offset:6144
T v[116:117],v21 offset:40960
M a[128:131],v[48:51],v[64:67],a[128:131]
SM s41,131712
M a[132:135],v[48:51],v[68:71],a[132:135]
)"
R"(B %0,s[48:51],s41 offen lds
M a[136:139],v[48:51],v[72:75],a[136:139]
T v[118:119],v21 offset:41984
M a[140:143],v[48:51],v[76:79],a[140:143]
M a[144:147],v[48:51],v[80:83],a[144:147]
SA m0,s40,86016
M a[148:151],v[48:51],v[84:87],a[148:151]
T v[120:121],v22 offset:40960
)"
R"(M a[152:155],v[48:51],v[88:91],a[152:155]
SM s41,164480
M a[156:159],v[48:51],v[92:95],a[156:159]
R v[48:51],v5 offset:8192
W lgkmcnt(15)
M a[160:163],v[52:55],v[64:67],a[160:163]
T v[122:123],v22 offset:41984
B %0,s[48:51],s41 offen lds
)"
R"(M a[164:167],v[52:55],v[68:71],a[164:167]
M a[168:171],v[52:55],v[72:75],a[168:171]
M a[172:175],v[52:55],v[76:79],a[172:175]
T v[124:125],v23 offset:40960
SA m0,s40,90112
M a[176:179],v[52:55],v[80:83],a[176:179]
M a[180:183],v[52:55],v[84:87],a[180:183]
SM s41,197248
)"
R"(M a[184:187],v[52:55],v[88:91],a[184:187]
T v[126:127],v23 offset:41984
M a[188:191],v[52:55],v[92:95],a[188:191]
R v[52:55],v5 offset:10240
M a[192:195],v[56:59],v[64:67],a[192:195]
B %0,s[48:51],s41 offen lds
M a[196:199],v[56:59],v[68:71],a[196:199]
M a[200:203],v[56:59],v[72:75],a[200:203]
)"
R"(SA m0,s40,94208
M a[204:207],v[56:59],v[76:79],a[204:207]
M a[208:211],v[56:59],v[80:83],a[208:211]
SM s41,230016
M a[212:215],v[56:59],v[84:87],a[212:215]
M a[216:219],v[56:59],v[88:91],a[216:219]
M a[220:223],v[56:59],v[92:95],a[220:223]
R v[56:59],v5 offset:12288
)"
R"(B %0,s[48:51],s41 offen lds
M a[224:227],v[60:63],v[64:67],a[224:227]
M a[228:231],v[60:63],v[68:71],a[228:231]
M a[232:235],v[60:63],v[72:75],a[232:235]
M a[236:239],v[60:63],v[76:79],a[236:239]
M a[240:243],v[60:63],v[80:83],a[240:243]
M a[244:247],v[60:63],v[84:87],a[244:247]
M a[248:251],v[60:63],v[88:91],a[248:251]
)"
R"(M a[252:255],v[60:63],v[92:95],a[252:255]
R v[60:63],v5 offset:14336
W vmcnt(8) lgkmcnt(0)
s_barrier
M a[0:3],v[32:35],v[96:99],a[0:3]
SA m0,s40,131072
M a[4:7],v[32:35],v[100:103],a[4:7]
T v[64:65],v16 offset:0
)"
R"(M a[8:11],v[32:35],v[104:107],a[8:11]
SM s41,2621440
M a[12:15],v[32:35],v[108:111],a[12:15]
M a[16:19],v[32:35],v[112:115],a[16:19]
T v[66:67],v16 offset:1024
M a[20:23],v[32:35],v[116:119],a[20:23]
B %1,s[60:63],s41 offen lds
M a[24:27],v[32:35],v[120:123],a[24:27]
)"
R"(M a[28:31],v[32:35],v[124:127],a[28:31]
R v[32:35],v4 offset:32768
T v[68:69],v17 offset:0
M a[32:35],v[36:39],v[96:99],a[32:35]
SA m0,s40,147456
M a[36:39],v[36:39],v[100:103],a[36:39]
M a[40:43],v[36:39],v[104:107],a[40:43]
T v[70:71],v17 offset:1024
)"
R"(SM s41,2621440
M a[44:47],v[36:39],v[108:111],a[44:47]
M a[48:51],v[36:39],v[112:115],a[48:51]
M a[52:55],v[36:39],v[116:119],a[52:55]
T v[72:73],v18 offset:0
B %1,s[64:67],s41 offen lds
M a[56:59],v[36:39],v[120:123],a[56:59]
M a[60:63],v[36:39],v[124:127],a[60:63]
)"
R"(R v[36:39],v4 offset:34816
M a[64:67],v[40:43],v[96:99],a[64:67]
T v[74:75],v18 offset:1024
SA m0,s40,135168
M a[68:71],v[40:43],v[100:103],a[68:71]
M a[72:75],v[40:43],v[104:107],a[72:75]
SM s41,2752512
M a[76:79],v[40:43],v[108:111],a[76:79]
)"
R"(T v[76:77],v19 offset:0
M a[80:83],v[40:43],v[112:115],a[80:83]
M a[84:87],v[40:43],v[116:119],a[84:87]
B %1,s[60:63],s41 offen lds
M a[88:91],v[40:43],v[120:123],a[88:91]
T v[78:79],v19 offset:1024
M a[92:95],v[40:43],v[124:127],a[92:95]
R v[40:43],v4 offset:36864
)"
R"(M a[96:99],v[44:47],v[96:99],a[96:99]
SA m0,s40,151552
M a[100:103],v[44:47],v[100:103],a[100:103]
T v[80:81],v20 offset:0
M a[104:107],v[44:47],v[104:107],a[104:107]
SM s41,2752512
M a[108:111],v[44:47],v[108:111],a[108:111]
M a[112:115],v[44:47],v[112:115],a[112:115]
)"
R"(T v[82:83],v20 offset:1024
M a[116:119],v[44:47],v[116:119],a[116:119]
B %1,s[64:67],s41 offen lds
M a[120:123],v[44:47],v[120:123],a[120:123]
M a[124:127],v[44:47],v[124:127],a[124:127]
R v[44:47],v4 offset:38912
T v[84:85],v21 offset:0
M a[128:131],v[48:51],v[96:99],a[128:131]
)"
R"(SA m0,s40,139264
M a[132:135],v[48:51],v[100:103],a[132:135]
M a[136:139],v[48:51],v[104:107],a[136:139]
T v[86:87],v21 offset:1024
SM s41,2883584
M a[140:143],v[48:51],v[108:111],a[140:143]
M a[144:147],v[48:51],v[112:115],a[144:147]
M a[148:151],v[48:51],v[116:119],a[148:151]
)"
R"(T v[88:89],v22 offset:0
B %1,s[60:63],s41 offen lds
M a[152:155],v[48:51],v[120:123],a[152:155]
M a[156:159],v[48:51],v[124:127],a[156:159]
R v[48:51],v4 offset:40960
M a[160:163],v[52:55],v[96:99],a[160:163]
T v[90:91],v22 offset:1024
SA m0,s40,155648
)"
R"(M a[164:167],v[52:55],v[100:103],a[164:167]
M a[168:171],v[52:55],v[104:107],a[168:171]
SM s41,2883584
M a[172:175],v[52:55],v[108:111],a[172:175]
T v[92:93],v23 offset:0
M a[176:179],v[52:55],v[112:115],a[176:179]
M a[180:183],v[52:55],v[116:119],a[180:183]
B %1,s[64:67],s41 offen lds
)"
R"(M a[184:187],v[52:55],v[120:123],a[184:187]
T v[94:95],v23 offset:1024
M a[188:191],v[52:55],v[124:127],a[188:191]
R v[52:55],v4 offset:43008
M a[192:195],v[56:59],v[96:99],a[192:195]
SA m0,s40,143360
M a[196:199],v[56:59],v[100:103],a[196:199]
M a[200:203],v[56:59],v[104:107],a[200:203]
)"
R"(SM s41,3014656
M a[204:207],v[56:59],v[108:111],a[204:207]
M a[208:211],v[56:59],v[112:115],a[208:211]
M a[212:215],v[56:59],v[116:119],a[212:215]
B %1,s[60:63],s41 offen lds
M a[216:219],v[56:59],v[120:123],a[216:219]
M a[220:223],v[56:59],v[124:127],a[220:223]
R v[56:59],v4 offset:45056
)"
R"(M a[224:227],v[60:63],v[96:99],a[224:227]
SA m0,s40,159744
M a[228:231],v[60:63],v[100:103],a[228:231]
M a[232:235],v[60:63],v[104:107],a[232:235]
SM s41,3014656
M a[236:239],v[60:63],v[108:111],a[236:239]
M a[240:243],v[60:63],v[112:115],a[240:243]
M a[244:247],v[60:63],v[116:119],a[244:247]
)"
R"(B %1,s[64:67],s41 offen lds
M a[248:251],v[60:63],v[120:123],a[248:251]
M a[252:255],v[60:63],v[124:127],a[252:255]
R v[60:63],v4 offset:47104
W lgkmcnt(14)
M a[0:3],v[32:35],v[64:67],a[0:3]
SA m0,s40,0
M a[4:7],v[32:35],v[68:71],a[4:7]
)"
R"(T v[96:97],v16 offset:8192
M a[8:11],v[32:35],v[72:75],a[8:11]
SM s41,768
M a[12:15],v[32:35],v[76:79],a[12:15]
W lgkmcnt(4)
M a[16:19],v[32:35],v[80:83],a[16:19]
T v[98:99],v16 offset:9216
B %0,s[48:51],s41 offen lds
)"
R"(M a[20:23],v[32:35],v[84:87],a[20:23]
M a[24:27],v[32:35],v[88:91],a[24:27]
M a[28:31],v[32:35],v[92:95],a[28:31]
R v[32:35],v5 offset:32768
T v[100:101],v17 offset:8192
M a[32:35],v[36:39],v[64:67],a[32:35]
SA m0,s40,4096
M a[36:39],v[36:39],v[68:71],a[36:39]
)"
R"(SM s41,33536
M a[40:43],v[36:39],v[72:75],a[40:43]
T v[102:103],v17 offset:9216
M a[44:47],v[36:39],v[76:79],a[44:47]
M a[48:51],v[36:39],v[80:83],a[48:51]
B %0,s[48:51],s41 offen lds
M a[52:55],v[36:39],v[84:87],a[52:55]
T v[104:105],v18 offset:8192
)"
R"(M a[56:59],v[36:39],v[88:91],a[56:59]
SA m0,s40,8192
M a[60:63],v[36:39],v[92:95],a[60:63]
R v[36:39],v5 offset:34816
M a[64:67],v[40:43],v[64:67],a[64:67]
T v[106:107],v18 offset:9216
SM s41,66304
M a[68:71],v[40:43],v[68:71],a[68:71]
)"
R"(M a[72:75],v[40:43],v[72:75],a[72:75]
M a[76:79],v[40:43],v[76:79],a[76:79]
T v[108:109],v19 offset:8192
B %0,s[48:51],s41 offen lds
M a[80:83],v[40:43],v[80:83],a[80:83]
M a[84:87],v[40:43],v[84:87],a[84:87]
SA m0,s40,12288
M a[88:91],v[40:43],v[88:91],a[88:91]
)"
R"(T v[110:111],v19 offset:9216
M a[92:95],v[40:43],v[92:95],a[92:95]
R v[40:43],v5 offset:36864
M a[96:99],v[44:47],v[64:67],a[96:99]
SM s41,99072
M a[100:103],v[44:47],v[68:71],a[100:103]
T v[112:113],v20 offset:8192
M a[104:107],v[44:47],v[72:75],a[104:107]
)"
R"(B %0,s[48:51],s41 offen lds
M a[108:111],v[44:47],v[76:79],a[108:111]
M a[112:115],v[44:47],v[80:83],a[112:115]
T v[114:115],v20 offset:9216
SA m0,s40,16384
M a[116:119],v[44:47],v[84:87],a[116:119]
M a[120:123],v[44:47],v[88:91],a[120:123]
M a[124:127],v[44:47],v[92:95],a[124:127]
)"
R"(R v[44:47],v5 offset:38912
T v[116:117],v21 offset:8192
M a[128:131],v[48:51],v[64:67],a[128:131]
SM s41,131840
M a[132:135],v[48:51],v[68:71],a[132:135]
B %0,s[48:51],s41 offen lds
M a[136:139],v[48:51],v[72:75],a[136:139]
T v[118:119],v21 offset:9216
)"
R"(M a[140:143],v[48:51],v[76:79],a[140:143]
M a[144:147],v[48:51],v[80:83],a[144:147]
SA m0,s40,20480
M a[148:151],v[48:51],v[84:87],a[148:151]
T v[120:121],v22 offset:8192
M a[152:155],v[48:51],v[88:91],a[152:155]
SM s41,164608
M a[156:159],v[48:51],v[92:95],a[156:159]
)"
R"(R v[48:51],v5 offset:40960
W lgkmcnt(15)
M a[160:163],v[52:55],v[64:67],a[160:163]
T v[122:123],v22 offset:9216
B %0,s[48:51],s41 offen lds
M a[164:167],v[52:55],v[68:71],a[164:167]
M a[168:171],v[52:55],v[72:75],a[168:171]
M a[172:175],v[52:55],v[76:79],a[172:175]
)"
R"(T v[124:125],v23 offset:8192
SA m0,s40,24576
M a[176:179],v[52:55],v[80:83],a[176:179]
M a[180:183],v[52:55],v[84:87],a[180:183]
SM s41,197376
M a[184:187],v[52:55],v[88:91],a[184:187]
T v[126:127],v23 offset:9216
M a[188:191],v[52:55],v[92:95],a[188:191]
)"
R"(R v[52:55],v5 offset:43008
M a[192:195],v[56:59],v[64:67],a[192:195]
B %0,s[48:51],s41 offen lds
M a[196:199],v[56:59],v[68:71],a[196:199]
M a[200:203],v[56:59],v[72:75],a[200:203]
SA m0,s40,28672
M a[204:207],v[56:59],v[76:79],a[204:207]
M a[208:211],v[56:59],v[80:83],a[208:211]
)"
R"(SM s41,230144
M a[212:215],v[56:59],v[84:87],a[212:215]
M a[216:219],v[56:59],v[88:91],a[216:219]
M a[220:223],v[56:59],v[92:95],a[220:223]
R v[56:59],v5 offset:45056
B %0,s[48:51],s41 offen lds
M a[224:227],v[60:63],v[64:67],a[224:227]
M a[228:231],v[60:63],v[68:71],a[228:231]
)"
R"(M a[232:235],v[60:63],v[72:75],a[232:235]
M a[236:239],v[60:63],v[76:79],a[236:239]
M a[240:243],v[60:63],v[80:83],a[240:243]
M a[244:247],v[60:63],v[84:87],a[244:247]
M a[248:251],v[60:63],v[88:91],a[248:251]
M a[252:255],v[60:63],v[92:95],a[252:255]
R v[60:63],v5 offset:47104
W vmcnt(8) lgkmcnt(0)
)"
R"(s_barrier
M a[0:3],v[32:35],v[96:99],a[0:3]
SA m0,s40,98304
M a[4:7],v[32:35],v[100:103],a[4:7]
T v[64:65],v16 offset:32768
M a[8:11],v[32:35],v[104:107],a[8:11]
SM s41,3145728
M a[12:15],v[32:35],v[108:111],a[12:15]
)"
R"(M a[16:19],v[32:35],v[112:115],a[16:19]
T v[66:67],v16 offset:33792
M a[20:23],v[32:35],v[116:119],a[20:23]
B %1,s[60:63],s41 offen lds
M a[24:27],v[32:35],v[120:123],a[24:27]
M a[28:31],v[32:35],v[124:127],a[28:31]
R v[32:35],v6 offset:0
T v[68:69],v17 offset:32768
)"
R"(M a[32:35],v[36:39],v[96:99],a[32:35]
SA m0,s40,114688
M a[36:39],v[36:39],v[100:103],a[36:39]
M a[40:43],v[36:39],v[104:107],a[40:43]
T v[70:71],v17 offset:33792
SM s41,3145728
M a[44:47],v[36:39],v[108:111],a[44:47]
M a[48:51],v[36:39],v[112:115],a[48:51]
)"
R"(M a[52:55],v[36:39],v[116:119],a[52:55]
T v[72:73],v18 offset:32768
B %1,s[64:67],s41 offen lds
M a[56:59],v[36:39],v[120:123],a[56:59]
M a[60:63],v[36:39],v[124:127],a[60:63]
R v[36:39],v6 offset:2048
M a[64:67],v[40:43],v[96:99],a[64:67]
T v[74:75],v18 offset:33792
)"
R"(SA m0,s40,102400
M a[68:71],v[40:43],v[100:103],a[68:71]
M a[72:75],v[40:43],v[104:107],a[72:75]
SM s41,3276800
M a[76:79],v[40:43],v[108:111],a[76:79]
T v[76:77],v19 offset:32768
M a[80:83],v[40:43],v[112:115],a[80:83]
M a[84:87],v[40:43],v[116:119],a[84:87]
)"
R"(B %1,s[60:63],s41 offen lds
M a[88:91],v[40:43],v[120:123],a[88:91]
T v[78:79],v19 offset:33792
M a[92:95],v[40:43],v[124:127],a[92:95]
R v[40:43],v6 offset:4096
M a[96:99],v[44:47],v[96:99],a[96:99]
SA m0,s40,118784
M a[100:103],v[44:47],v[100:103],a[100:103]
)"
R"(T v[80:81],v20 offset:32768
M a[104:107],v[44:47],v[104:107],a[104:107]
SM s41,3276800
M a[108:111],v[44:47],v[108:111],a[108:111]
M a[112:115],v[44:47],v[112:115],a[112:115]
T v[82:83],v20 offset:33792
M a[116:119],v[44:47],v[116:119],a[116:119]
B %1,s[64:67],s41 offen lds
)"
R"(M a[120:123],v[44:47],v[120:123],a[120:123]
M a[124:127],v[44:47],v[124:127],a[124:127]
R v[44:47],v6 offset:6144
T v[84:85],v21 offset:32768
M a[128:131],v[48:51],v[96:99],a[128:131]
SA m0,s40,106496
M a[132:135],v[48:51],v[100:103],a[132:135]
M a[136:139],v[48:51],v[104:107],a[136:139]
)"
R"(T v[86:87],v21 offset:33792
SM s41,3407872
M a[140:143],v[48:51],v[108:111],a[140:143]
M a[144:147],v[48:51],v[112:115],a[144:147]
M a[148:151],v[48:51],v[116:119],a[148:151]
T v[88:89],v22 offset:32768
B %1,s[60:63],s41 offen lds
M a[152:155],v[48:51],v[120:123],a[152:155]
)"
R"(M a[156:159],v[48:51],v[124:127],a[156:159]
R v[48:51],v6 offset:8192
M a[160:163],v[52:55],v[96:99],a[160:163]
T v[90:91],v22 offset:33792
SA m0,s40,122880
M a[164:167],v[52:55],v[100:103],a[164:167]
M a[168:171],v[52:55],v[104:107],a[168:171]
SM s41,3407872
)"
R"(M a[172:175],v[52:55],v[108:111],a[172:175]
T v[92:93],v23 offset:32768
M a[176:179],v[52:55],v[112:115],a[176:179]
M a[180:183],v[52:55],v[116:119],a[180:183]
B %1,s[64:67],s41 offen lds
M a[184:187],v[52:55],v[120:123],a[184:187]
T v[94:95],v23 offset:33792
M a[188:191],v[52:55],v[124:127],a[188:191]
)"
R"(R v[52:55],v6 offset:10240
M a[192:195],v[56:59],v[96:99],a[192:195]
SA m0,s40,110592
M a[196:199],v[56:59],v[100:103],a[196:199]
M a[200:203],v[56:59],v[104:107],a[200:203]
SM s41,3538944
M a[204:207],v[56:59],v[108:111],a[204:207]
M a[208:211],v[56:59],v[112:115],a[208:211]
)"
R"(M a[212:215],v[56:59],v[116:119],a[212:215]
B %1,s[60:63],s41 offen lds
M a[216:219],v[56:59],v[120:123],a[216:219]
M a[220:223],v[56:59],v[124:127],a[220:223]
R v[56:59],v6 offset:12288
M a[224:227],v[60:63],v[96:99],a[224:227]
SA m0,s40,126976
M a[228:231],v[60:63],v[100:103],a[228:231]
)"
R"(M a[232:235],v[60:63],v[104:107],a[232:235]
SM s41,3538944
M a[236:239],v[60:63],v[108:111],a[236:239]
M a[240:243],v[60:63],v[112:115],a[240:243]
M a[244:247],v[60:63],v[116:119],a[244:247]
B %1,s[64:67],s41 offen lds
M a[248:251],v[60:63],v[120:123],a[248:251]
M a[252:255],v[60:63],v[124:127],a[252:255]
)"
R"(R v[60:63],v6 offset:14336
W lgkmcnt(14)
M a[0:3],v[32:35],v[64:67],a[0:3]
SA m0,s40,32768
M a[4:7],v[32:35],v[68:71],a[4:7]
T v[96:97],v16 offset:40960
M a[8:11],v[32:35],v[72:75],a[8:11]
SM s41,896
)"
R"(M a[12:15],v[32:35],v[76:79],a[12:15]
W lgkmcnt(4)
M a[16:19],v[32:35],v[80:83],a[16:19]
T v[98:99],v16 offset:41984
B %0,s[48:51],s41 offen lds
M a[20:23],v[32:35],v[84:87],a[20:23]
M a[24:27],v[32:35],v[88:91],a[24:27]
M a[28:31],v[32:35],v[92:95],a[28:31]
)"
R"(R v[32:35],v7 offset:0
T v[100:101],v17 offset:40960
M a[32:35],v[36:39],v[64:67],a[32:35]
SA m0,s40,36864
M a[36:39],v[36:39],v[68:71],a[36:39]
SM s41,33664
M a[40:43],v[36:39],v[72:75],a[40:43]
T v[102:103],v17 offset:41984
)"
R"(M a[44:47],v[36:39],v[76:79],a[44:47]
M a[48:51],v[36:39],v[80:83],a[48:51]
B %0,s[48:51],s41 offen lds
M a[52:55],v[36:39],v[84:87],a[52:55]
T v[104:105],v18 offset:40960
M a[56:59],v[36:39],v[88:91],a[56:59]
SA m0,s40,40960
M a[60:63],v[36:39],v[92:95],a[60:63]
)"
R"(R v[36:39],v7 offset:2048
M a[64:67],v[40:43],v[64:67],a[64:67]
T v[106:107],v18 offset:41984
SM s41,66432
M a[68:71],v[40:43],v[68:71],a[68:71]
M a[72:75],v[40:43],v[72:75],a[72:75]
M a[76:79],v[40:43],v[76:79],a[76:79]
T v[108:109],v19 offset:40960
)"
R"(B %0,s[48:51],s41 offen lds
M a[80:83],v[40:43],v[80:83],a[80:83]
M a[84:87],v[40:43],v[84:87],a[84:87]
SA m0,s40,45056
M a[88:91],v[40:43],v[88:91],a[88:91]
T v[110:111],v19 offset:41984
M a[92:95],v[40:43],v[92:95],a[92:95]
R v[40:43],v7 offset:4096
)"
R"(M a[96:99],v[44:47],v[64:67],a[96:99]
SM s41,99200
M a[100:103],v[44:47],v[68:71],a[100:103]
T v[112:113],v20 offset:40960
M a[104:107],v[44:47],v[72:75],a[104:107]
B %0,s[48:51],s41 offen lds
M a[108:111],v[44:47],v[76:79],a[108:111]
M a[112:115],v[44:47],v[80:83],a[112:115]
)"
R"(T v[114:115],v20 offset:41984
SA m0,s40,49152
M a[116:119],v[44:47],v[84:87],a[116:119]
M a[120:123],v[44:47],v[88:91],a[120:123]
M a[124:127],v[44:47],v[92:95],a[124:127]
R v[44:47],v7 offset:6144
T v[116:117],v21 offset:40960
M a[128:131],v[48:51],v[64:67],a[128:131]
)"
R"(SM s41,131968
M a[132:135],v[48:51],v[68:71],a[132:135]
B %0,s[48:51],s41 offen lds
M a[136:139],v[48:51],v[72:75],a[136:139]
T v[118:119],v21 offset:41984
M a[140:143],v[48:51],v[76:79],a[140:143]
M a[144:147],v[48:51],v[80:83],a[144:147]
SA m0,s40,53248
)"
R"(M a[148:151],v[48:51],v[84:87],a[148:151]
T v[120:121],v22 offset:40960
M a[152:155],v[48:51],v[88:91],a[152:155]
SM s41,164736
M a[156:159],v[48:51],v[92:95],a[156:159]
R v[48:51],v7 offset:8192
W lgkmcnt(15)
M a[160:163],v[52:55],v[64:67],a[160:163]
)"
R"(T v[122:123],v22 offset:41984
B %0,s[48:51],s41 offen lds
M a[164:167],v[52:55],v[68:71],a[164:167]
M a[168:171],v[52:55],v[72:75],a[168:171]
M a[172:175],v[52:55],v[76:79],a[172:175]
T v[124:125],v23 offset:40960
SA m0,s40,57344
M a[176:179],v[52:55],v[80:83],a[176:179]
)"
R"(M a[180:183],v[52:55],v[84:87],a[180:183]
SM s41,197504
M a[184:187],v[52:55],v[88:91],a[184:187]
T v[126:127],v23 offset:41984
M a[188:191],v[52:55],v[92:95],a[188:191]
R v[52:55],v7 offset:10240
M a[192:195],v[56:59],v[64:67],a[192:195]
B %0,s[48:51],s41 offen lds
)"
R"(M a[196:199],v[56:59],v[68:71],a[196:199]
M a[200:203],v[56:59],v[72:75],a[200:203]
SA m0,s40,61440
M a[204:207],v[56:59],v[76:79],a[204:207]
M a[208:211],v[56:59],v[80:83],a[208:211]
SM s41,230272
M a[212:215],v[56:59],v[84:87],a[212:215]
M a[216:219],v[56:59],v[88:91],a[216:219]
)"
R"(M a[220:223],v[56:59],v[92:95],a[220:223]
R v[56:59],v7 offset:12288
B %0,s[48:51],s41 offen lds
M a[224:227],v[60:63],v[64:67],a[224:227]
M a[228:231],v[60:63],v[68:71],a[228:231]
M a[232:235],v[60:63],v[72:75],a[232:235]
M a[236:239],v[60:63],v[76:79],a[236:239]
M a[240:243],v[60:63],v[80:83],a[240:243]
)"
R"(M a[244:247],v[60:63],v[84:87],a[244:247]
M a[248:251],v[60:63],v[88:91],a[248:251]
M a[252:255],v[60:63],v[92:95],a[252:255]
R v[60:63],v7 offset:14336
W vmcnt(8) lgkmcnt(0)
s_barrier
M a[0:3],v[32:35],v[96:99],a[0:3]
SA m0,s40,131072
)"
R"(M a[4:7],v[32:35],v[100:103],a[4:7]
T v[64:65],v16 offset:0
M a[8:11],v[32:35],v[104:107],a[8:11]
SM s41,3670016
M a[12:15],v[32:35],v[108:111],a[12:15]
M a[16:19],v[32:35],v[112:115],a[16:19]
T v[66:67],v16 offset:1024
M a[20:23],v[32:35],v[116:119],a[20:23]
)"
R"(B %1,s[60:63],s41 offen lds
M a[24:27],v[32:35],v[120:123],a[24:27]
M a[28:31],v[32:35],v[124:127],a[28:31]
R v[32:35],v4 offset:0
T v[68:69],v17 offset:0
M a[32:35],v[36:39],v[96:99],a[32:35]
SA m0,s40,147456
M a[36:39],v[36:39],v[100:103],a[36:39]
)"
R"(M a[40:43],v[36:39],v[104:107],a[40:43]
T v[70:71],v17 offset:1024
SM s41,3670016
M a[44:47],v[36:39],v[108:111],a[44:47]
M a[48:51],v[36:39],v[112:115],a[48:51]
M a[52:55],v[36:39],v[116:119],a[52:55]
T v[72:73],v18 offset:0
B %1,s[64:67],s41 offen lds
)"
R"(M a[56:59],v[36:39],v[120:123],a[56:59]
M a[60:63],v[36:39],v[124:127],a[60:63]
R v[36:39],v4 offset:2048
M a[64:67],v[40:43],v[96:99],a[64:67]
T v[74:75],v18 offset:1024
SA m0,s40,135168
M a[68:71],v[40:43],v[100:103],a[68:71]
M a[72:75],v[40:43],v[104:107],a[72:75]
)"
R"(SM s41,3801088
M a[76:79],v[40:43],v[108:111],a[76:79]
T v[76:77],v19 offset:0
M a[80:83],v[40:43],v[112:115],a[80:83]
M a[84:87],v[40:43],v[116:119],a[84:87]
B %1,s[60:63],s41 offen lds
M a[88:91],v[40:43],v[120:123],a[88:91]
T v[78:79],v19 offset:1024
)"
R"(M a[92:95],v[40:43],v[124:127],a[92:95]
R v[40:43],v4 offset:4096
M a[96:99],v[44:47],v[96:99],a[96:99]
SA m0,s40,151552
M a[100:103],v[44:47],v[100:103],a[100:103]
T v[80:81],v20 offset:0
M a[104:107],v[44:47],v[104:107],a[104:107]
SM s41,3801088
)"
R"(M a[108:111],v[44:47],v[108:111],a[108:111]
M a[112:115],v[44:47],v[112:115],a[112:115]
T v[82:83],v20 offset:1024
M a[116:119],v[44:47],v[116:119],a[116:119]
B %1,s[64:67],s41 offen lds
M a[120:123],v[44:47],v[120:123],a[120:123]
M a[124:127],v[44:47],v[124:127],a[124:127]
R v[44:47],v4 offset:6144
)"
R"(T v[84:85],v21 offset:0
M a[128:131],v[48:51],v[96:99],a[128:131]
SA m0,s40,139264
M a[132:135],v[48:51],v[100:103],a[132:135]
M a[136:139],v[48:51],v[104:107],a[136:139]
T v[86:87],v21 offset:1024
SM s41,3932160
M a[140:143],v[48:51],v[108:111],a[140:143]
)"
R"(M a[144:147],v[48:51],v[112:115],a[144:147]
M a[148:151],v[48:51],v[116:119],a[148:151]
T v[88:89],v22 offset:0
B %1,s[60:63],s41 offen lds
M a[152:155],v[48:51],v[120:123],a[152:155]
M a[156:159],v[48:51],v[124:127],a[156:159]
R v[48:51],v4 offset:8192
M a[160:163],v[52:55],v[96:99],a[160:163]
)"
R"(T v[90:91],v22 offset:1024
SA m0,s40,155648
M a[164:167],v[52:55],v[100:103],a[164:167]
M a[168:171],v[52:55],v[104:107],a[168:171]
SM s41,3932160
M a[172:175],v[52:55],v[108:111],a[172:175]
T v[92:93],v23 offset:0
M a[176:179],v[52:55],v[112:115],a[176:179]
)"
R"(M a[180:183],v[52:55],v[116:119],a[180:183]
B %1,s[64:67],s41 offen lds
M a[184:187],v[52:55],v[120:123],a[184:187]
T v[94:95],v23 offset:1024
M a[188:191],v[52:55],v[124:127],a[188:191]
R v[52:55],v4 offset:10240
M a[192:195],v[56:59],v[96:99],a[192:195]
SA m0,s40,143360
)"
R"(M a[196:199],v[56:59],v[100:103],a[196:199]
M a[200:203],v[56:59],v[104:107],a[200:203]
SM s41,4063232
M a[204:207],v[56:59],v[108:111],a[204:207]
M a[208:211],v[56:59],v[112:115],a[208:211]
M a[212:215],v[56:59],v[116:119],a[212:215]
B %1,s[60:63],s41 offen lds
M a[216:219],v[56:59],v[120:123],a[216:219]
)"
R"(M a[220:223],v[56:59],v[124:127],a[220:223]
R v[56:59],v4 offset:12288
M a[224:227],v[60:63],v[96:99],a[224:227]
SA m0,s40,159744
M a[228:231],v[60:63],v[100:103],a[228:231]
M a[232:235],v[60:63],v[104:107],a[232:235]
SM s41,4063232
M a[236:239],v[60:63],v[108:111],a[236:239]
)"
R"(M a[240:243],v[60:63],v[112:115],a[240:243]
M a[244:247],v[60:63],v[116:119],a[244:247]
B %1,s[64:67],s41 offen lds
M a[248:251],v[60:63],v[120:123],a[248:251]
M a[252:255],v[60:63],v[124:127],a[252:255]
R v[60:63],v4 offset:14336
W lgkmcnt(14)
M a[0:3],v[32:35],v[64:67],a[0:3]
)"
R"(SA m0,s40,65536
M a[4:7],v[32:35],v[68:71],a[4:7]
T v[96:97],v16 offset:8192
M a[8:11],v[32:35],v[72:75],a[8:11]
SM s41,0
M a[12:15],v[32:35],v[76:79],a[12:15]
W lgkmcnt(4)
M a[16:19],v[32:35],v[80:83],a[16:19]
)"
R"(T v[98:99],v16 offset:9216
B %0,s[52:55],s41 offen lds
M a[20:23],v[32:35],v[84:87],a[20:23]
M a[24:27],v[32:35],v[88:91],a[24:27]
M a[28:31],v[32:35],v[92:95],a[28:31]
R v[32:35],v5 offset:0
T v[100:101],v17 offset:8192
M a[32:35],v[36:39],v[64:67],a[32:35]
)"
R"(SA m0,s40,69632
M a[36:39],v[36:39],v[68:71],a[36:39]
SM s41,32768
M a[40:43],v[36:39],v[72:75],a[40:43]
T v[102:103],v17 offset:9216
M a[44:47],v[36:39],v[76:79],a[44:47]
M a[48:51],v[36:39],v[80:83],a[48:51]
B %0,s[52:55],s41 offen lds
)"
R"(M a[52:55],v[36:39],v[84:87],a[52:55]
T v[104:105],v18 offset:8192
M a[56:59],v[36:39],v[88:91],a[56:59]
SA m0,s40,73728
M a[60:63],v[36:39],v[92:95],a[60:63]
R v[36:39],v5 offset:2048
M a[64:67],v[40:43],v[64:67],a[64:67]
T v[106:107],v18 offset:9216
)"
R"(SM s41,65536
M a[68:71],v[40:43],v[68:71],a[68:71]
M a[72:75],v[40:43],v[72:75],a[72:75]
M a[76:79],v[40:43],v[76:79],a[76:79]
T v[108:109],v19 offset:8192
B %0,s[52:55],s41 offen lds
M a[80:83],v[40:43],v[80:83],a[80:83]
M a[84:87],v[40:43],v[84:87],a[84:87]
)"
R"(SA m0,s40,77824
M a[88:91],v[40:43],v[88:91],a[88:91]
T v[110:111],v19 offset:9216
M a[92:95],v[40:43],v[92:95],a[92:95]
R v[40:43],v5 offset:4096
M a[96:99],v[44:47],v[64:67],a[96:99]
SM s41,98304
M a[100:103],v[44:47],v[68:71],a[100:103]
)"
R"(T v[112:113],v20 offset:8192
M a[104:107],v[44:47],v[72:75],a[104:107]
B %0,s[52:55],s41 offen lds
M a[108:111],v[44:47],v[76:79],a[108:111]
M a[112:115],v[44:47],v[80:83],a[112:115]
T v[114:115],v20 offset:9216
SA m0,s40,81920
M a[116:119],v[44:47],v[84:87],a[116:119]
)"
R"(M a[120:123],v[44:47],v[88:91],a[120:123]
M a[124:127],v[44:47],v[92:95],a[124:127]
R v[44:47],v5 offset:6144
T v[116:117],v21 offset:8192
M a[128:131],v[48:51],v[64:67],a[128:131]
SM s41,131072
M a[132:135],v[48:51],v[68:71],a[132:135]
B %0,s[52:55],s41 offen lds
)"
R"(M a[136:139],v[48:51],v[72:75],a[136:139]
T v[118:119],v21 offset:9216
M a[140:143],v[48:51],v[76:79],a[140:143]
M a[144:147],v[48:51],v[80:83],a[144:147]
SA m0,s40,86016
M a[148:151],v[48:51],v[84:87],a[148:151]
T v[120:121],v22 offset:8192
M a[152:155],v[48:51],v[88:91],a[152:155]
)"
R"(SM s41,163840
M a[156:159],v[48:51],v[92:95],a[156:159]
R v[48:51],v5 offset:8192
W lgkmcnt(15)
M a[160:163],v[52:55],v[64:67],a[160:163]
T v[122:123],v22 offset:9216
B %0,s[52:55],s41 offen lds
M a[164:167],v[52:55],v[68:71],a[164:167]
)"
R"(M a[168:171],v[52:55],v[72:75],a[168:171]
M a[172:175],v[52:55],v[76:79],a[172:175]
T v[124:125],v23 offset:8192
SA m0,s40,90112
M a[176:179],v[52:55],v[80:83],a[176:179]
M a[180:183],v[52:55],v[84:87],a[180:183]
SM s41,196608
M a[184:187],v[52:55],v[88:91],a[184:187]
)"
R"(T v[126:127],v23 offset:9216
M a[188:191],v[52:55],v[92:95],a[188:191]
R v[52:55],v5 offset:10240
M a[192:195],v[56:59],v[64:67],a[192:195]
B %0,s[52:55],s41 offen lds
M a[196:199],v[56:59],v[68:71],a[196:199]
M a[200:203],v[56:59],v[72:75],a[200:203]
SA m0,s40,94208
)"
R"(M a[204:207],v[56:59],v[76:79],a[204:207]
M a[208:211],v[56:59],v[80:83],a[208:211]
SM s41,229376
M a[212:215],v[56:59],v[84:87],a[212:215]
M a[216:219],v[56:59],v[88:91],a[216:219]
M a[220:223],v[56:59],v[92:95],a[220:223]
R v[56:59],v5 offset:12288
B %0,s[52:55],s41 offen lds
)"
R"(M a[224:227],v[60:63],v[64:67],a[224:227]
M a[228:231],v[60:63],v[68:71],a[228:231]
M a[232:235],v[60:63],v[72:75],a[232:235]
M a[236:239],v[60:63],v[76:79],a[236:239]
M a[240:243],v[60:63],v[80:83],a[240:243]
M a[244:247],v[60:63],v[84:87],a[244:247]
M a[248:251],v[60:63],v[88:91],a[248:251]
M a[252:255],v[60:63],v[92:95],a[252:255]
)"
R"(R v[60:63],v5 offset:14336
W vmcnt(8) lgkmcnt(0)
s_barrier
M a[0:3],v[32:35],v[96:99],a[0:3]
SA m0,s40,98304
M a[4:7],v[32:35],v[100:103],a[4:7]
T v[64:65],v16 offset:32768
M a[8:11],v[32:35],v[104:107],a[8:11]
)"
R"(M a[12:15],v[32:35],v[108:111],a[12:15]
SM s41,0
M a[16:19],v[32:35],v[112:115],a[16:19]
T v[66:67],v16 offset:33792
M a[20:23],v[32:35],v[116:119],a[20:23]
M a[24:27],v[32:35],v[120:123],a[24:27]
M a[28:31],v[32:35],v[124:127],a[28:31]
R v[32:35],v4 offset:32768
)"
R"(T v[68:69],v17 offset:32768
M a[32:35],v[36:39],v[96:99],a[32:35]
B %2,s[72:75],s41 offen lds
M a[36:39],v[36:39],v[100:103],a[36:39]
M a[40:43],v[36:39],v[104:107],a[40:43]
T v[70:71],v17 offset:33792
SA m0,s40,102400
M a[44:47],v[36:39],v[108:111],a[44:47]
)"
R"(M a[48:51],v[36:39],v[112:115],a[48:51]
M a[52:55],v[36:39],v[116:119],a[52:55]
T v[72:73],v18 offset:32768
M a[56:59],v[36:39],v[120:123],a[56:59]
SM s41,98304
M a[60:63],v[36:39],v[124:127],a[60:63]
R v[36:39],v4 offset:34816
M a[64:67],v[40:43],v[96:99],a[64:67]
)"
R"(T v[74:75],v18 offset:33792
M a[68:71],v[40:43],v[100:103],a[68:71]
B %2,s[72:75],s41 offen lds
M a[72:75],v[40:43],v[104:107],a[72:75]
M a[76:79],v[40:43],v[108:111],a[76:79]
T v[76:77],v19 offset:32768
M a[80:83],v[40:43],v[112:115],a[80:83]
M a[84:87],v[40:43],v[116:119],a[84:87]
)"
R"(SA m0,s40,106496
M a[88:91],v[40:43],v[120:123],a[88:91]
T v[78:79],v19 offset:33792
M a[92:95],v[40:43],v[124:127],a[92:95]
R v[40:43],v4 offset:36864
M a[96:99],v[44:47],v[96:99],a[96:99]
SM s41,196608
M a[100:103],v[44:47],v[100:103],a[100:103]
)"
R"(T v[80:81],v20 offset:32768
M a[104:107],v[44:47],v[104:107],a[104:107]
M a[108:111],v[44:47],v[108:111],a[108:111]
M a[112:115],v[44:47],v[112:115],a[112:115]
T v[82:83],v20 offset:33792
B %2,s[72:75],s41 offen lds
M a[116:119],v[44:47],v[116:119],a[116:119]
M a[120:123],v[44:47],v[120:123],a[120:123]
)"
R"(M a[124:127],v[44:47],v[124:127],a[124:127]
R v[44:47],v4 offset:38912
T v[84:85],v21 offset:32768
M a[128:131],v[48:51],v[96:99],a[128:131]
SA m0,s40,110592
M a[132:135],v[48:51],v[100:103],a[132:135]
M a[136:139],v[48:51],v[104:107],a[136:139]
T v[86:87],v21 offset:33792
)"
R"(M a[140:143],v[48:51],v[108:111],a[140:143]
SM s41,294912
M a[144:147],v[48:51],v[112:115],a[144:147]
M a[148:151],v[48:51],v[116:119],a[148:151]
T v[88:89],v22 offset:32768
M a[152:155],v[48:51],v[120:123],a[152:155]
M a[156:159],v[48:51],v[124:127],a[156:159]
R v[48:51],v4 offset:40960
)"
R"(B %2,s[72:75],s41 offen lds
M a[160:163],v[52:55],v[96:99],a[160:163]
T v[90:91],v22 offset:33792
M a[164:167],v[52:55],v[100:103],a[164:167]
M a[168:171],v[52:55],v[104:107],a[168:171]
SA m0,s40,0
M a[172:175],v[52:55],v[108:111],a[172:175]
T v[92:93],v23 offset:32768
)"
R"(M a[176:179],v[52:55],v[112:115],a[176:179]
M a[180:183],v[52:55],v[116:119],a[180:183]
M a[184:187],v[52:55],v[120:123],a[184:187]
T v[94:95],v23 offset:33792
SM s41,128
M a[188:191],v[52:55],v[124:127],a[188:191]
R v[52:55],v4 offset:43008
M a[192:195],v[56:59],v[96:99],a[192:195]
)"
R"(M a[196:199],v[56:59],v[100:103],a[196:199]
B %0,s[52:55],s41 offen lds
M a[200:203],v[56:59],v[104:107],a[200:203]
M a[204:207],v[56:59],v[108:111],a[204:207]
M a[208:211],v[56:59],v[112:115],a[208:211]
M a[212:215],v[56:59],v[116:119],a[212:215]
SA m0,s40,4096
M a[216:219],v[56:59],v[120:123],a[216:219]
)"
R"(M a[220:223],v[56:59],v[124:127],a[220:223]
R v[56:59],v4 offset:45056
M a[224:227],v[60:63],v[96:99],a[224:227]
SM s41,32896
M a[228:231],v[60:63],v[100:103],a[228:231]
M a[232:235],v[60:63],v[104:107],a[232:235]
M a[236:239],v[60:63],v[108:111],a[236:239]
M a[240:243],v[60:63],v[112:115],a[240:243]
)"
R"(B %0,s[52:55],s41 offen lds
M a[244:247],v[60:63],v[116:119],a[244:247]
M a[248:251],v[60:63],v[120:123],a[248:251]
M a[252:255],v[60:63],v[124:127],a[252:255]
R v[60:63],v4 offset:47104
W lgkmcnt(14)
M a[0:3],v[32:35],v[64:67],a[0:3]
SA m0,s40,8192
)"
R"(M a[4:7],v[32:35],v[68:71],a[4:7]
T v[96:97],v16 offset:40960
M a[8:11],v[32:35],v[72:75],a[8:11]
M a[12:15],v[32:35],v[76:79],a[12:15]
SM s41,65664
W lgkmcnt(4)
M a[16:19],v[32:35],v[80:83],a[16:19]
T v[98:99],v16 offset:41984
)"
R"(M a[20:23],v[32:35],v[84:87],a[20:23]
M a[24:27],v[32:35],v[88:91],a[24:27]
B %0,s[52:55],s41 offen lds
M a[28:31],v[32:35],v[92:95],a[28:31]
R v[32:35],v5 offset:32768
T v[100:101],v17 offset:40960
M a[32:35],v[36:39],v[64:67],a[32:35]
M a[36:39],v[36:39],v[68:71],a[36:39]
)"
R"(SA m0,s40,12288
M a[40:43],v[36:39],v[72:75],a[40:43]
T v[102:103],v17 offset:41984
M a[44:47],v[36:39],v[76:79],a[44:47]
M a[48:51],v[36:39],v[80:83],a[48:51]
SM s41,98432
M a[52:55],v[36:39],v[84:87],a[52:55]
T v[104:105],v18 offset:40960
)"
R"(M a[56:59],v[36:39],v[88:91],a[56:59]
M a[60:63],v[36:39],v[92:95],a[60:63]
R v[36:39],v5 offset:34816
M a[64:67],v[40:43],v[64:67],a[64:67]
T v[106:107],v18 offset:41984
B %0,s[52:55],s41 offen lds
M a[68:71],v[40:43],v[68:71],a[68:71]
M a[72:75],v[40:43],v[72:75],a[72:75]
)"
R"(M a[76:79],v[40:43],v[76:79],a[76:79]
T v[108:109],v19 offset:40960
SA m0,s40,16384
M a[80:83],v[40:43],v[80:83],a[80:83]
M a[84:87],v[40:43],v[84:87],a[84:87]
M a[88:91],v[40:43],v[88:91],a[88:91]
T v[110:111],v19 offset:41984
SM s41,131200
)"
R"(M a[92:95],v[40:43],v[92:95],a[92:95]
R v[40:43],v5 offset:36864
M a[96:99],v[44:47],v[64:67],a[96:99]
M a[100:103],v[44:47],v[68:71],a[100:103]
T v[112:113],v20 offset:40960
B %0,s[52:55],s41 offen lds
M a[104:107],v[44:47],v[72:75],a[104:107]
M a[108:111],v[44:47],v[76:79],a[108:111]
)"
R"(M a[112:115],v[44:47],v[80:83],a[112:115]
T v[114:115],v20 offset:41984
SA m0,s40,20480
M a[116:119],v[44:47],v[84:87],a[116:119]
M a[120:123],v[44:47],v[88:91],a[120:123]
M a[124:127],v[44:47],v[92:95],a[124:127]
R v[44:47],v5 offset:38912
T v[116:117],v21 offset:40960
)"
R"(M a[128:131],v[48:51],v[64:67],a[128:131]
SM s41,163968
M a[132:135],v[48:51],v[68:71],a[132:135]
M a[136:139],v[48:51],v[72:75],a[136:139]
T v[118:119],v21 offset:41984
M a[140:143],v[48:51],v[76:79],a[140:143]
B %0,s[52:55],s41 offen lds
M a[144:147],v[48:51],v[80:83],a[144:147]
)"
R"(M a[148:151],v[48:51],v[84:87],a[148:151]
T v[120:121],v22 offset:40960
M a[152:155],v[48:51],v[88:91],a[152:155]
SA m0,s40,24576
M a[156:159],v[48:51],v[92:95],a[156:159]
R v[48:51],v5 offset:40960
W lgkmcnt(15)
M a[160:163],v[52:55],v[64:67],a[160:163]
)"
R"(T v[122:123],v22 offset:41984
M a[164:167],v[52:55],v[68:71],a[164:167]
SM s41,196736
M a[168:171],v[52:55],v[72:75],a[168:171]
M a[172:175],v[52:55],v[76:79],a[172:175]
T v[124:125],v23 offset:40960
M a[176:179],v[52:55],v[80:83],a[176:179]
B %0,s[52:55],s41 offen lds
)"
R"(M a[180:183],v[52:55],v[84:87],a[180:183]
M a[184:187],v[52:55],v[88:91],a[184:187]
T v[126:127],v23 offset:41984
M a[188:191],v[52:55],v[92:95],a[188:191]
R v[52:55],v5 offset:43008
M a[192:195],v[56:59],v[64:67],a[192:195]
SA m0,s40,28672
M a[196:199],v[56:59],v[68:71],a[196:199]
)"
R"(M a[200:203],v[56:59],v[72:75],a[200:203]
M a[204:207],v[56:59],v[76:79],a[204:207]
SM s41,229504
M a[208:211],v[56:59],v[80:83],a[208:211]
M a[212:215],v[56:59],v[84:87],a[212:215]
M a[216:219],v[56:59],v[88:91],a[216:219]
B %0,s[52:55],s41 offen lds
M a[220:223],v[56:59],v[92:95],a[220:223]
)"
R"(R v[56:59],v5 offset:45056
M a[224:227],v[60:63],v[64:67],a[224:227]
M a[228:231],v[60:63],v[68:71],a[228:231]
M a[232:235],v[60:63],v[72:75],a[232:235]
M a[236:239],v[60:63],v[76:79],a[236:239]
M a[240:243],v[60:63],v[80:83],a[240:243]
M a[244:247],v[60:63],v[84:87],a[244:247]
M a[248:251],v[60:63],v[88:91],a[248:251]
)"
R"(M a[252:255],v[60:63],v[92:95],a[252:255]
R v[60:63],v5 offset:47104
W vmcnt(10) lgkmcnt(0)
s_barrier
M a[0:3],v[32:35],v[96:99],a[0:3]
SA m0,s40,114688
M a[4:7],v[32:35],v[100:103],a[4:7]
M a[8:11],v[32:35],v[104:107],a[8:11]
)"
R"(T v[64:65],v24 offset:0
M a[12:15],v[32:35],v[108:111],a[12:15]
M a[16:19],v[32:35],v[112:115],a[16:19]
SM s41,393216
M a[20:23],v[32:35],v[116:119],a[20:23]
T v[66:67],v24 offset:512
M a[24:27],v[32:35],v[120:123],a[24:27]
M a[28:31],v[32:35],v[124:127],a[28:31]
)"
R"(R v[32:35],v8 offset:0
M a[32:35],v[36:39],v[96:99],a[32:35]
B %2,s[72:75],s41 offen lds
M a[36:39],v[36:39],v[100:103],a[36:39]
M a[40:43],v[36:39],v[104:107],a[40:43]
T v[68:69],v25 offset:0
M a[44:47],v[36:39],v[108:111],a[44:47]
M a[48:51],v[36:39],v[112:115],a[48:51]
)"
R"(SA m0,s40,118784
M a[52:55],v[36:39],v[116:119],a[52:55]
T v[70:71],v25 offset:512
M a[56:59],v[36:39],v[120:123],a[56:59]
M a[60:63],v[36:39],v[124:127],a[60:63]
R v[36:39],v8 offset:2048
M a[64:67],v[40:43],v[96:99],a[64:67]
M a[68:71],v[40:43],v[100:103],a[68:71]
)"
R"(SM s41,491520
M a[72:75],v[40:43],v[104:107],a[72:75]
T v[72:73],v26 offset:0
M a[76:79],v[40:43],v[108:111],a[76:79]
M a[80:83],v[40:43],v[112:115],a[80:83]
M a[84:87],v[40:43],v[116:119],a[84:87]
T v[74:75],v26 offset:512
B %2,s[72:75],s41 offen lds
)"
R"(M a[88:91],v[40:43],v[120:123],a[88:91]
M a[92:95],v[40:43],v[124:127],a[92:95]
R v[40:43],v8 offset:4096
M a[96:99],v[44:47],v[96:99],a[96:99]
M a[100:103],v[44:47],v[100:103],a[100:103]
SA m0,s40,32768
M a[104:107],v[44:47],v[104:107],a[104:107]
M a[108:111],v[44:47],v[108:111],a[108:111]
)"
R"(M a[112:115],v[44:47],v[112:115],a[112:115]
M a[116:119],v[44:47],v[116:119],a[116:119]
SM s41,256
M a[120:123],v[44:47],v[120:123],a[120:123]
M a[124:127],v[44:47],v[124:127],a[124:127]
R v[44:47],v8 offset:6144
M a[128:131],v[48:51],v[96:99],a[128:131]
M a[132:135],v[48:51],v[100:103],a[132:135]
)"
R"(M a[136:139],v[48:51],v[104:107],a[136:139]
B %0,s[52:55],s41 offen lds
M a[140:143],v[48:51],v[108:111],a[140:143]
M a[144:147],v[48:51],v[112:115],a[144:147]
M a[148:151],v[48:51],v[116:119],a[148:151]
M a[152:155],v[48:51],v[120:123],a[152:155]
SA m0,s40,36864
M a[156:159],v[48:51],v[124:127],a[156:159]
)"
R"(M a[160:163],v[52:55],v[96:99],a[160:163]
M a[164:167],v[52:55],v[100:103],a[164:167]
M a[168:171],v[52:55],v[104:107],a[168:171]
SM s41,33024
M a[172:175],v[52:55],v[108:111],a[172:175]
M a[176:179],v[52:55],v[112:115],a[176:179]
M a[180:183],v[52:55],v[116:119],a[180:183]
M a[184:187],v[52:55],v[120:123],a[184:187]
)"
R"(B %0,s[52:55],s41 offen lds
M a[188:191],v[52:55],v[124:127],a[188:191]
M a[192:195],v[56:59],v[96:99],a[192:195]
M a[196:199],v[56:59],v[100:103],a[196:199]
M a[200:203],v[56:59],v[104:107],a[200:203]
M a[204:207],v[56:59],v[108:111],a[204:207]
SA m0,s40,40960
M a[208:211],v[56:59],v[112:115],a[208:211]
)"
R"(M a[212:215],v[56:59],v[116:119],a[212:215]
M a[216:219],v[56:59],v[120:123],a[216:219]
M a[220:223],v[56:59],v[124:127],a[220:223]
SM s41,65792
M a[224:227],v[60:63],v[96:99],a[224:227]
M a[228:231],v[60:63],v[100:103],a[228:231]
M a[232:235],v[60:63],v[104:107],a[232:235]
M a[236:239],v[60:63],v[108:111],a[236:239]
)"
R"(B %0,s[52:55],s41 offen lds
M a[240:243],v[60:63],v[112:115],a[240:243]
M a[244:247],v[60:63],v[116:119],a[244:247]
M a[248:251],v[60:63],v[120:123],a[248:251]
M a[252:255],v[60:63],v[124:127],a[252:255]
s_lshl_b32 s42,%17,13
v_add_u32 v4,s42,v27
v_xor_b32 v5,64,v4
)"
R"(v_add_u32 v6,0x10000,v4
v_add_u32 v7,0x10000,v5
W lgkmcnt(2)
M v[128:131],v[64:67],v[32:35],0
T v[96:97],v24 offset:4096
SA m0,s40,45056
SM s41,98560
M v[144:147],v[68:71],v[32:35],0
)"
R"(T v[98:99],v24 offset:4608
B %0,s[52:55],s41 offen lds
M v[160:163],v[32:35],v[72:75],0
R v[32:35],v7 offset:0
SA m0,s40,49152
SM s41,131328
M v[132:135],v[64:67],v[36:39],0
T v[100:101],v25 offset:4096
)"
R"(B %0,s[52:55],s41 offen lds
M v[148:151],v[68:71],v[36:39],0
T v[102:103],v25 offset:4608
SA m0,s40,53248
M v[164:167],v[36:39],v[72:75],0
R v[36:39],v7 offset:2048
SM s41,164096
B %0,s[52:55],s41 offen lds
)"
R"(W lgkmcnt(6)
M v[136:139],v[64:67],v[40:43],0
T v[104:105],v26 offset:4096
SA m0,s40,57344
M v[152:155],v[68:71],v[40:43],0
T v[106:107],v26 offset:4608
SM s41,196864
B %0,s[52:55],s41 offen lds
)"
R"(M v[168:171],v[40:43],v[72:75],0
R v[40:43],v7 offset:4096
SA m0,s40,61440
M v[140:143],v[64:67],v[44:47],0
SM s41,229632
M v[156:159],v[68:71],v[44:47],0
B %0,s[52:55],s41 offen lds
M v[172:175],v[44:47],v[72:75],0
)"
R"(R v[44:47],v7 offset:6144
W vmcnt(10) lgkmcnt(0)
s_barrier
M v[128:131],v[96:99],v[32:35],v[128:131]
T v[64:65],v24 offset:8192
SA m0,s40,122880
SM s41,589824
M v[144:147],v[100:103],v[32:35],v[144:147]
)"
R"(T v[66:67],v24 offset:8704
B %2,s[72:75],s41 offen lds
M v[160:163],v[32:35],v[104:107],v[160:163]
R v[32:35],v4 offset:0
SA m0,s40,126976
M v[132:135],v[96:99],v[36:39],v[132:135]
T v[68:69],v25 offset:8192
SM s41,688128
)"
R"(M v[148:151],v[100:103],v[36:39],v[148:151]
T v[70:71],v25 offset:8704
B %2,s[72:75],s41 offen lds
SA m0,s40,65536
M v[164:167],v[36:39],v[104:107],v[164:167]
R v[36:39],v4 offset:2048
SM s41,384
M v[136:139],v[96:99],v[40:43],v[136:139]
)"
R"(T v[72:73],v26 offset:8192
B %0,s[52:55],s41 offen lds
M v[152:155],v[100:103],v[40:43],v[152:155]
T v[74:75],v26 offset:8704
SA m0,s40,69632
M v[168:171],v[40:43],v[104:107],v[168:171]
R v[40:43],v4 offset:4096
SM s41,33152
)"
R"(B %0,s[52:55],s41 offen lds
M v[140:143],v[96:99],v[44:47],v[140:143]
SA m0,s40,73728
M v[156:159],v[100:103],v[44:47],v[156:159]
SM s41,65920
M v[172:175],v[44:47],v[104:107],v[172:175]
R v[44:47],v4 offset:6144
B %0,s[52:55],s41 offen lds
)"
R"(W lgkmcnt(2)
M v[128:131],v[64:67],v[32:35],v[128:131]
T v[96:97],v24 offset:12288
SA m0,s40,77824
SM s41,98688
AR v176,a0
M v[144:147],v[68:71],v[32:35],v[144:147]
T v[98:99],v24 offset:12800
)"
R"(B %0,s[52:55],s41 offen lds
AR v177,a1
M v[160:163],v[32:35],v[72:75],v[160:163]
R v[32:35],v5 offset:0
SA m0,s40,81920
SM s41,131456
AR v178,a2
M v[132:135],v[64:67],v[36:39],v[132:135]
)"
R"(T v[100:101],v25 offset:12288
B %0,s[52:55],s41 offen lds
AR v179,a3
M v[148:151],v[68:71],v[36:39],v[148:151]
T v[102:103],v25 offset:12800
SA m0,s40,86016
AR v180,a32
M v[164:167],v[36:39],v[72:75],v[164:167]
)"
R"(R v[36:39],v5 offset:2048
SM s41,164224
B %0,s[52:55],s41 offen lds
AR v181,a33
W lgkmcnt(6)
M v[136:139],v[64:67],v[40:43],v[136:139]
T v[104:105],v26 offset:12288
SA m0,s40,90112
)"
R"(AR v182,a34
AR v183,a35
M v[152:155],v[68:71],v[40:43],v[152:155]
T v[106:107],v26 offset:12800
SM s41,196992
B %0,s[52:55],s41 offen lds
CV v176,v176,v177
M v[168:171],v[40:43],v[72:75],v[168:171]
)"
R"(R v[40:43],v5 offset:4096
SA m0,s40,94208
CV v177,v178,v179
M v[140:143],v[64:67],v[44:47],v[140:143]
SM s41,229760
CV v178,v180,v181
M v[156:159],v[68:71],v[44:47],v[156:159]
B %0,s[52:55],s41 offen lds
)"
R"(CV v179,v182,v183
M v[172:175],v[44:47],v[72:75],v[172:175]
R v[44:47],v5 offset:6144
GS v3,v[176:179],s[44:45] offset:0 sc1
W vmcnt(11) lgkmcnt(0)
s_barrier
M v[128:131],v[96:99],v[32:35],v[128:131]
T v[64:65],v24 offset:16384
)"
R"(SA m0,s40,98304
SM s41,786432
AR v188,a4
M v[144:147],v[100:103],v[32:35],v[144:147]
T v[66:67],v24 offset:16896
B %2,s[72:75],s41 offen lds
AR v189,a5
M v[160:163],v[32:35],v[104:107],v[160:163]
)"
R"(R v[32:35],v4 offset:32768
SA m0,s40,102400
AR v190,a6
M v[132:135],v[96:99],v[36:39],v[132:135]
T v[68:69],v25 offset:16384
SM s41,884736
AR v191,a7
M v[148:151],v[100:103],v[36:39],v[148:151]
)"
R"(T v[70:71],v25 offset:16896
B %2,s[72:75],s41 offen lds
SA m0,s40,0
AR v192,a36
M v[164:167],v[36:39],v[104:107],v[164:167]
R v[36:39],v4 offset:34816
SM s41,512
AR v193,a37
)"
R"(AR v194,a38
M v[136:139],v[96:99],v[40:43],v[136:139]
T v[72:73],v26 offset:16384
B %0,s[52:55],s41 offen lds
AR v195,a39
M v[152:155],v[100:103],v[40:43],v[152:155]
T v[74:75],v26 offset:16896
SA m0,s40,4096
)"
R"(CV v188,v188,v189
M v[168:171],v[40:43],v[104:107],v[168:171]
R v[40:43],v4 offset:36864
SM s41,33280
B %0,s[52:55],s41 offen lds
CV v189,v190,v191
M v[140:143],v[96:99],v[44:47],v[140:143]
SA m0,s40,8192
)"
R"(CV v190,v192,v193
M v[156:159],v[100:103],v[44:47],v[156:159]
SM s41,66048
CV v191,v194,v195
M v[172:175],v[44:47],v[104:107],v[172:175]
R v[44:47],v4 offset:38912
B %0,s[52:55],s41 offen lds
GS v3,v[188:191],s[44:45] offset:1024 sc1
)"
R"(W lgkmcnt(2)
M v[128:131],v[64:67],v[32:35],v[128:131]
T v[96:97],v24 offset:20480
SA m0,s40,12288
SM s41,98816
M v[144:147],v[68:71],v[32:35],v[144:147]
T v[98:99],v24 offset:20992
B %0,s[52:55],s41 offen lds
)"
R"(M v[160:163],v[32:35],v[72:75],v[160:163]
R v[32:35],v5 offset:32768
SA m0,s40,16384
SM s41,131584
M v[132:135],v[64:67],v[36:39],v[132:135]
T v[100:101],v25 offset:20480
B %0,s[52:55],s41 offen lds
M v[148:151],v[68:71],v[36:39],v[148:151]
)"
R"(T v[102:103],v25 offset:20992
SA m0,s40,20480
M v[164:167],v[36:39],v[72:75],v[164:167]
R v[36:39],v5 offset:34816
SM s41,164352
B %0,s[52:55],s41 offen lds
W lgkmcnt(6)
M v[136:139],v[64:67],v[40:43],v[136:139]
)"
R"(T v[104:105],v26 offset:20480
SA m0,s40,24576
M v[152:155],v[68:71],v[40:43],v[152:155]
T v[106:107],v26 offset:20992
SM s41,197120
B %0,s[52:55],s41 offen lds
M v[168:171],v[40:43],v[72:75],v[168:171]
R v[40:43],v5 offset:36864
)"
R"(SA m0,s40,28672
M v[140:143],v[64:67],v[44:47],v[140:143]
SM s41,229888
M v[156:159],v[68:71],v[44:47],v[156:159]
B %0,s[52:55],s41 offen lds
M v[172:175],v[44:47],v[72:75],v[172:175]
R v[44:47],v5 offset:38912
W vmcnt(12) lgkmcnt(0)
)"
R"(s_barrier
M v[128:131],v[96:99],v[32:35],v[128:131]
T v[64:65],v24 offset:24576
SA m0,s40,106496
SM s41,983040
AR v176,a8
M v[144:147],v[100:103],v[32:35],v[144:147]
T v[66:67],v24 offset:25088
)"
R"(B %2,s[72:75],s41 offen lds
AR v177,a9
M v[160:163],v[32:35],v[104:107],v[160:163]
R v[32:35],v6 offset:0
SA m0,s40,110592
AR v178,a10
M v[132:135],v[96:99],v[36:39],v[132:135]
T v[68:69],v25 offset:24576
)"
R"(SM s41,1081344
AR v179,a11
M v[148:151],v[100:103],v[36:39],v[148:151]
T v[70:71],v25 offset:25088
B %2,s[72:75],s41 offen lds
SA m0,s40,32768
AR v180,a40
M v[164:167],v[36:39],v[104:107],v[164:167]
)"
R"(R v[36:39],v6 offset:2048
SM s41,640
AR v181,a41
AR v182,a42
M v[136:139],v[96:99],v[40:43],v[136:139]
T v[72:73],v26 offset:24576
B %0,s[52:55],s41 offen lds
AR v183,a43
)"
R"(M v[152:155],v[100:103],v[40:43],v[152:155]
T v[74:75],v26 offset:25088
SA m0,s40,36864
CV v176,v176,v177
M v[168:171],v[40:43],v[104:107],v[168:171]
R v[40:43],v6 offset:4096
SM s41,33408
B %0,s[52:55],s41 offen lds
)"
R"(CV v177,v178,v179
M v[140:143],v[96:99],v[44:47],v[140:143]
SA m0,s40,40960
CV v178,v180,v181
M v[156:159],v[100:103],v[44:47],v[156:159]
SM s41,66176
CV v179,v182,v183
M v[172:175],v[44:47],v[104:107],v[172:175]
)"
R"(R v[44:47],v6 offset:6144
B %0,s[52:55],s41 offen lds
GS v3,v[176:179],s[44:45] offset:2048 sc1
W lgkmcnt(2)
M v[128:131],v[64:67],v[32:35],v[128:131]
T v[96:97],v24 offset:28672
SA m0,s40,45056
SM s41,98944
)"
R"(AR v188,a12
M v[144:147],v[68:71],v[32:35],v[144:147]
T v[98:99],v24 offset:29184
B %0,s[52:55],s41 offen lds
AR v189,a13
M v[160:163],v[32:35],v[72:75],v[160:163]
R v[32:35],v7 offset:0
SA m0,s40,49152
)"
R"(SM s41,131712
AR v190,a14
M v[132:135],v[64:67],v[36:39],v[132:135]
T v[100:101],v25 offset:28672
B %0,s[52:55],s41 offen lds
AR v191,a15
M v[148:151],v[68:71],v[36:39],v[148:151]
T v[102:103],v25 offset:29184
)"
R"(SA m0,s40,53248
AR v192,a44
M v[164:167],v[36:39],v[72:75],v[164:167]
R v[36:39],v7 offset:2048
SM s41,164480
B %0,s[52:55],s41 offen lds
AR v193,a45
W lgkmcnt(6)
)"
R"(M v[136:139],v[64:67],v[40:43],v[136:139]
T v[104:105],v26 offset:28672
SA m0,s40,57344
AR v194,a46
AR v195,a47
M v[152:155],v[68:71],v[40:43],v[152:155]
T v[106:107],v26 offset:29184
SM s41,197248
)"
R"(B %0,s[52:55],s41 offen lds
CV v188,v188,v189
M v[168:171],v[40:43],v[72:75],v[168:171]
R v[40:43],v7 offset:4096
SA m0,s40,61440
CV v189,v190,v191
M v[140:143],v[64:67],v[44:47],v[140:143]
SM s41,230016
)"
R"(CV v190,v192,v193
M v[156:159],v[68:71],v[44:47],v[156:159]
B %0,s[52:55],s41 offen lds
CV v191,v194,v195
M v[172:175],v[44:47],v[72:75],v[172:175]
R v[44:47],v7 offset:6144
GS v3,v[188:191],s[44:45] offset:3072 sc1
W vmcnt(12) lgkmcnt(0)
)"
R"(s_barrier
M v[128:131],v[96:99],v[32:35],v[128:131]
T v[64:65],v24 offset:0
SA m0,s40,114688
SM s41,1179648
M v[144:147],v[100:103],v[32:35],v[144:147]
T v[66:67],v24 offset:512
B %2,s[72:75],s41 offen lds
)"
R"(M v[160:163],v[32:35],v[104:107],v[160:163]
R v[32:35],v4 offset:0
SA m0,s40,118784
M v[132:135],v[96:99],v[36:39],v[132:135]
T v[68:69],v25 offset:0
SM s41,1277952
M v[148:151],v[100:103],v[36:39],v[148:151]
T v[70:71],v25 offset:512
)"
R"(B %2,s[72:75],s41 offen lds
SA m0,s40,65536
M v[164:167],v[36:39],v[104:107],v[164:167]
R v[36:39],v4 offset:2048
SM s41,768
M v[136:139],v[96:99],v[40:43],v[136:139]
T v[72:73],v26 offset:0
B %0,s[52:55],s41 offen lds
)"
R"(M v[152:155],v[100:103],v[40:43],v[152:155]
T v[74:75],v26 offset:512
SA m0,s40,69632
M v[168:171],v[40:43],v[104:107],v[168:171]
R v[40:43],v4 offset:4096
SM s41,33536
B %0,s[52:55],s41 offen lds
M v[140:143],v[96:99],v[44:47],v[140:143]
)"
R"(SA m0,s40,73728
M v[156:159],v[100:103],v[44:47],v[156:159]
SM s41,66304
M v[172:175],v[44:47],v[104:107],v[172:175]
R v[44:47],v4 offset:6144
B %0,s[52:55],s41 offen lds
W lgkmcnt(2)
M v[128:131],v[64:67],v[32:35],v[128:131]
)"
R"(T v[96:97],v24 offset:4096
SA m0,s40,77824
SM s41,99072
AR v176,a16
M v[144:147],v[68:71],v[32:35],v[144:147]
T v[98:99],v24 offset:4608
B %0,s[52:55],s41 offen lds
AR v177,a17
)"
R"(AR v178,a18
M v[160:163],v[32:35],v[72:75],v[160:163]
R v[32:35],v5 offset:0
SA m0,s40,81920
SM s41,131840
AR v179,a19
M v[132:135],v[64:67],v[36:39],v[132:135]
T v[100:101],v25 offset:4096
)"
R"(B %0,s[52:55],s41 offen lds
AR v180,a48
M v[148:151],v[68:71],v[36:39],v[148:151]
T v[102:103],v25 offset:4608
SA m0,s40,86016
AR v181,a49
M v[164:167],v[36:39],v[72:75],v[164:167]
R v[36:39],v5 offset:2048
)"
R"(SM s41,164608
B %0,s[52:55],s41 offen lds
AR v182,a50
W lgkmcnt(6)
M v[136:139],v[64:67],v[40:43],v[136:139]
T v[104:105],v26 offset:4096
SA m0,s40,90112
AR v183,a51
)"
R"(CV v176,v176,v177
M v[152:155],v[68:71],v[40:43],v[152:155]
T v[106:107],v26 offset:4608
SM s41,197376
B %0,s[52:55],s41 offen lds
CV v177,v178,v179
M v[168:171],v[40:43],v[72:75],v[168:171]
R v[40:43],v5 offset:4096
)"
R"(SA m0,s40,94208
CV v178,v180,v181
M v[140:143],v[64:67],v[44:47],v[140:143]
SM s41,230144
CV v179,v182,v183
SA s44,s44,0x1000
M v[156:159],v[68:71],v[44:47],v[156:159]
B %0,s[52:55],s41 offen lds
)"
R"(s_addc_u32 s45,s45,0
M v[172:175],v[44:47],v[72:75],v[172:175]
R v[44:47],v5 offset:6144
GS v3,v[176:179],s[44:45] offset:0 sc1
W vmcnt(12) lgkmcnt(0)
s_barrier
M v[128:131],v[96:99],v[32:35],v[128:131]
T v[64:65],v24 offset:8192
)"
R"(SA m0,s40,122880
SM s41,1376256
AR v188,a20
M v[144:147],v[100:103],v[32:35],v[144:147]
T v[66:67],v24 offset:8704
B %2,s[72:75],s41 offen lds
AR v189,a21
M v[160:163],v[32:35],v[104:107],v[160:163]
)"
R"(R v[32:35],v4 offset:32768
SA m0,s40,126976
AR v190,a22
M v[132:135],v[96:99],v[36:39],v[132:135]
T v[68:69],v25 offset:8192
SM s41,1474560
AR v191,a23
M v[148:151],v[100:103],v[36:39],v[148:151]
)"
R"(T v[70:71],v25 offset:8704
B %2,s[72:75],s41 offen lds
SA m0,s40,0
AR v192,a52
M v[164:167],v[36:39],v[104:107],v[164:167]
R v[36:39],v4 offset:34816
SM s41,896
AR v193,a53
)"
R"(AR v194,a54
M v[136:139],v[96:99],v[40:43],v[136:139]
T v[72:73],v26 offset:8192
B %0,s[52:55],s41 offen lds
AR v195,a55
M v[152:155],v[100:103],v[40:43],v[152:155]
T v[74:75],v26 offset:8704
SA m0,s40,4096
)"
R"(CV v188,v188,v189
M v[168:171],v[40:43],v[104:107],v[168:171]
R v[40:43],v4 offset:36864
SM s41,33664
B %0,s[52:55],s41 offen lds
CV v189,v190,v191
M v[140:143],v[96:99],v[44:47],v[140:143]
SA m0,s40,8192
)"
R"(CV v190,v192,v193
M v[156:159],v[100:103],v[44:47],v[156:159]
SM s41,66432
CV v191,v194,v195
M v[172:175],v[44:47],v[104:107],v[172:175]
R v[44:47],v4 offset:38912
B %0,s[52:55],s41 offen lds
GS v3,v[188:191],s[44:45] offset:1024 sc1
)"
R"(W lgkmcnt(2)
M v[128:131],v[64:67],v[32:35],v[128:131]
T v[96:97],v24 offset:12288
SA m0,s40,12288
SM s41,99200
M v[144:147],v[68:71],v[32:35],v[144:147]
T v[98:99],v24 offset:12800
B %0,s[52:55],s41 offen lds
)"
R"(M v[160:163],v[32:35],v[72:75],v[160:163]
R v[32:35],v5 offset:32768
SA m0,s40,16384
SM s41,131968
M v[132:135],v[64:67],v[36:39],v[132:135]
T v[100:101],v25 offset:12288
B %0,s[52:55],s41 offen lds
M v[148:151],v[68:71],v[36:39],v[148:151]
)"
R"(T v[102:103],v25 offset:12800
SA m0,s40,20480
M v[164:167],v[36:39],v[72:75],v[164:167]
R v[36:39],v5 offset:34816
SM s41,164736
B %0,s[52:55],s41 offen lds
W lgkmcnt(6)
M v[136:139],v[64:67],v[40:43],v[136:139]
)"
R"(T v[104:105],v26 offset:12288
SA m0,s40,24576
M v[152:155],v[68:71],v[40:43],v[152:155]
T v[106:107],v26 offset:12800
SM s41,197504
B %0,s[52:55],s41 offen lds
M v[168:171],v[40:43],v[72:75],v[168:171]
R v[40:43],v5 offset:36864
)"
R"(SA m0,s40,28672
M v[140:143],v[64:67],v[44:47],v[140:143]
SM s41,230272
M v[156:159],v[68:71],v[44:47],v[156:159]
B %0,s[52:55],s41 offen lds
M v[172:175],v[44:47],v[72:75],v[172:175]
R v[44:47],v5 offset:38912
W vmcnt(12) lgkmcnt(0)
)"
R"(s_barrier
M v[128:131],v[96:99],v[32:35],v[128:131]
T v[64:65],v24 offset:16384
SA m0,s40,131072
SM s41,0
AR v176,a24
M v[144:147],v[100:103],v[32:35],v[144:147]
T v[66:67],v24 offset:16896
)"
R"(B %1,s[68:71],s41 offen lds
AR v177,a25
M v[160:163],v[32:35],v[104:107],v[160:163]
R v[32:35],v6 offset:0
SA m0,s40,135168
SM s41,131072
AR v178,a26
M v[132:135],v[96:99],v[36:39],v[132:135]
)"
R"(T v[68:69],v25 offset:16384
B %1,s[68:71],s41 offen lds
AR v179,a27
M v[148:151],v[100:103],v[36:39],v[148:151]
T v[70:71],v25 offset:16896
SA m0,s40,139264
SM s41,262144
AR v180,a56
)"
R"(M v[164:167],v[36:39],v[104:107],v[164:167]
R v[36:39],v6 offset:2048
B %1,s[68:71],s41 offen lds
AR v181,a57
AR v182,a58
M v[136:139],v[96:99],v[40:43],v[136:139]
T v[72:73],v26 offset:16384
SA m0,s40,143360
)"
R"(SM s41,393216
AR v183,a59
M v[152:155],v[100:103],v[40:43],v[152:155]
T v[74:75],v26 offset:16896
B %1,s[68:71],s41 offen lds
CV v176,v176,v177
M v[168:171],v[40:43],v[104:107],v[168:171]
R v[40:43],v6 offset:4096
)"
R"(SA m0,s40,32768
SA s41,s76,0
CV v177,v178,v179
M v[140:143],v[96:99],v[44:47],v[140:143]
B %0,s[56:59],s41 offen lds
CV v178,v180,v181
M v[156:159],v[100:103],v[44:47],v[156:159]
SA m0,s40,36864
)"
R"(SA s41,s76,32768
CV v179,v182,v183
M v[172:175],v[44:47],v[104:107],v[172:175]
R v[44:47],v6 offset:6144
B %0,s[56:59],s41 offen lds
GS v3,v[176:179],s[44:45] offset:2048 sc1
W lgkmcnt(2)
M v[128:131],v[64:67],v[32:35],v[128:131]
)"
R"(T v[96:97],v24 offset:20480
SA m0,s40,40960
SA s41,s76,65536
AR v188,a28
M v[144:147],v[68:71],v[32:35],v[144:147]
T v[98:99],v24 offset:20992
B %0,s[56:59],s41 offen lds
SA m0,s40,45056
)"
R"(AR v189,a29
M v[160:163],v[32:35],v[72:75],v[160:163]
R v[32:35],v7 offset:0
SA s41,s76,98304
AR v190,a30
M v[132:135],v[64:67],v[36:39],v[132:135]
T v[100:101],v25 offset:20480
B %0,s[56:59],s41 offen lds
)"
R"(SA m0,s40,49152
AR v191,a31
M v[148:151],v[68:71],v[36:39],v[148:151]
T v[102:103],v25 offset:20992
SA s41,s77,0
B %0,s[56:59],s41 offen lds
AR v192,a60
M v[164:167],v[36:39],v[72:75],v[164:167]
)"
R"(R v[36:39],v7 offset:2048
SA m0,s40,53248
AR v193,a61
AR v194,a62
W lgkmcnt(6)
M v[136:139],v[64:67],v[40:43],v[136:139]
T v[104:105],v26 offset:20480
SA s41,s77,32768
)"
R"(B %0,s[56:59],s41 offen lds
AR v195,a63
M v[152:155],v[68:71],v[40:43],v[152:155]
T v[106:107],v26 offset:20992
SA m0,s40,57344
SA s41,s77,65536
CV v188,v188,v189
M v[168:171],v[40:43],v[72:75],v[168:171]
)"
R"(R v[40:43],v7 offset:4096
B %0,s[56:59],s41 offen lds
CV v189,v190,v191
M v[140:143],v[64:67],v[44:47],v[140:143]
SA m0,s40,61440
SA s41,s77,98304
CV v190,v192,v193
M v[156:159],v[68:71],v[44:47],v[156:159]
)"
R"(B %0,s[56:59],s41 offen lds
CV v191,v194,v195
M v[172:175],v[44:47],v[72:75],v[172:175]
R v[44:47],v7 offset:6144
GS v3,v[188:191],s[44:45] offset:3072 sc1
W vmcnt(14) lgkmcnt(0)
s_barrier
M v[128:131],v[96:99],v[32:35],v[128:131]
)"
R"(T v[64:65],v24 offset:24576
SA m0,s40,147456
SM s41,524288
M v[144:147],v[100:103],v[32:35],v[144:147]
T v[66:67],v24 offset:25088
B %1,s[68:71],s41 offen lds
M v[160:163],v[32:35],v[104:107],v[160:163]
R v[32:35],v4 offset:0
)"
R"(SA m0,s40,151552
SM s41,655360
M v[132:135],v[96:99],v[36:39],v[132:135]
T v[68:69],v25 offset:24576
B %1,s[68:71],s41 offen lds
M v[148:151],v[100:103],v[36:39],v[148:151]
T v[70:71],v25 offset:25088
SA m0,s40,155648
)"
R"(SM s41,786432
M v[164:167],v[36:39],v[104:107],v[164:167]
R v[36:39],v4 offset:2048
B %1,s[68:71],s41 offen lds
M v[136:139],v[96:99],v[40:43],v[136:139]
T v[72:73],v26 offset:24576
SA m0,s40,159744
SM s41,917504
)"
R"(M v[152:155],v[100:103],v[40:43],v[152:155]
T v[74:75],v26 offset:25088
B %1,s[68:71],s41 offen lds
M v[168:171],v[40:43],v[104:107],v[168:171]
R v[40:43],v4 offset:4096
SA m0,s40,65536
SA s41,s76,128
M v[140:143],v[96:99],v[44:47],v[140:143]
)"
R"(B %0,s[56:59],s41 offen lds
M v[156:159],v[100:103],v[44:47],v[156:159]
SA m0,s40,69632
SA s41,s76,32896
M v[172:175],v[44:47],v[104:107],v[172:175]
R v[44:47],v4 offset:6144
B %0,s[56:59],s41 offen lds
W lgkmcnt(2)
)"
R"(M v[128:131],v[64:67],v[32:35],v[128:131]
T v[96:97],v24 offset:28672
SA m0,s40,73728
SA s41,s76,65664
AR v176,a64
M v[144:147],v[68:71],v[32:35],v[144:147]
T v[98:99],v24 offset:29184
B %0,s[56:59],s41 offen lds
)"
R"(SA m0,s40,77824
AR v177,a65
M v[160:163],v[32:35],v[72:75],v[160:163]
R v[32:35],v5 offset:0
SA s41,s76,98432
AR v178,a66
AR v179,a67
M v[132:135],v[64:67],v[36:39],v[132:135]
)"
R"(T v[100:101],v25 offset:28672
B %0,s[56:59],s41 offen lds
SA m0,s40,81920
AR v180,a96
M v[148:151],v[68:71],v[36:39],v[148:151]
T v[102:103],v25 offset:29184
SA s41,s77,128
B %0,s[56:59],s41 offen lds
)"
R"(AR v181,a97
M v[164:167],v[36:39],v[72:75],v[164:167]
R v[36:39],v5 offset:2048
SA m0,s40,86016
AR v182,a98
AR v183,a99
W lgkmcnt(6)
M v[136:139],v[64:67],v[40:43],v[136:139]
)"
R"(T v[104:105],v26 offset:28672
SA s41,s77,32896
B %0,s[56:59],s41 offen lds
CV v176,v176,v177
M v[152:155],v[68:71],v[40:43],v[152:155]
T v[106:107],v26 offset:29184
SA m0,s40,90112
SA s41,s77,65664
)"
R"(CV v177,v178,v179
M v[168:171],v[40:43],v[72:75],v[168:171]
R v[40:43],v5 offset:4096
B %0,s[56:59],s41 offen lds
CV v178,v180,v181
M v[140:143],v[64:67],v[44:47],v[140:143]
SA m0,s40,94208
SA s41,s77,98432
)"
R"(CV v179,v182,v183
SA s44,s44,0x1000
M v[156:159],v[68:71],v[44:47],v[156:159]
B %0,s[56:59],s41 offen lds
s_addc_u32 s45,s45,0
M v[172:175],v[44:47],v[72:75],v[172:175]
R v[44:47],v5 offset:6144
GS v3,v[176:179],s[44:45] offset:0 sc1
)"
R"(W lgkmcnt(2)
M v[128:131],v[96:99],v[32:35],v[128:131]
AR v188,a68
M v[144:147],v[100:103],v[32:35],v[144:147]
AR v189,a69
M v[160:163],v[32:35],v[104:107],v[160:163]
AR v190,a70
M v[132:135],v[96:99],v[36:39],v[132:135]
)"
R"(AR v191,a71
M v[148:151],v[100:103],v[36:39],v[148:151]
AR v192,a100
M v[164:167],v[36:39],v[104:107],v[164:167]
AR v193,a101
AR v194,a102
W lgkmcnt(0)
M v[136:139],v[96:99],v[40:43],v[136:139]
)"
R"(AR v195,a103
M v[152:155],v[100:103],v[40:43],v[152:155]
CV v188,v188,v189
M v[168:171],v[40:43],v[104:107],v[168:171]
CV v189,v190,v191
M v[140:143],v[96:99],v[44:47],v[140:143]
CV v190,v192,v193
M v[156:159],v[100:103],v[44:47],v[156:159]
)"
R"(CV v191,v194,v195
M v[172:175],v[44:47],v[104:107],v[172:175]
GS v3,v[188:191],s[44:45] offset:1024 sc1
v_lshl_or_b32 v216,v209,16,v208
v_lshl_or_b32 v217,v211,16,v210
v_mov_b32 v218,0
v_mov_b32 v219,0
v_mov_b32 v220,0
)"
R"(v_mov_b32 v221,0
v_mov_b32 v222,v216
v_mov_b32 v223,v217
s_nop 7
s_nop 1
CV v32,v128,v129
AR v232,a72
CV v33,v130,v131
)"
R"(CV v34,v144,v145
CV v35,v146,v147
CV v36,v132,v133
AR v233,a73
CV v37,v134,v135
CV v38,v148,v149
CV v39,v150,v151
CV v40,v136,v137
)"
R"(CV v41,v138,v139
AR v234,a74
CV v42,v152,v153
CV v43,v154,v155
CV v44,v140,v141
CV v45,v142,v143
AR v235,a75
CV v46,v156,v157
)"
R"(CV v47,v158,v159
CV v48,v160,v161
CV v49,v162,v163
CV v50,v164,v165
AR v236,a104
CV v51,v166,v167
CV v52,v168,v169
CV v53,v170,v171
)"
R"(CV v54,v172,v173
CV v55,v174,v175
AR v237,a105
M v[64:67],v[216:219],v[32:35],0
M v[68:71],v[220:223],v[32:35],0
M v[72:75],v[216:219],v[36:39],0
M v[76:79],v[220:223],v[36:39],0
AR v238,a106
)"
R"(M v[80:83],v[216:219],v[40:43],0
M v[84:87],v[220:223],v[40:43],0
M v[88:91],v[216:219],v[44:47],0
M v[92:95],v[220:223],v[44:47],0
MX v208,v64,v65,v66
AR v239,a107
MX v209,v68,v69,v70
MX v210,v72,v73,v74
)"
R"(MX v211,v76,v77,v78
MX v212,v80,v81,v82
MX v213,v84,v85,v86
CV v232,v232,v233
MX v214,v88,v89,v90
MX v215,v92,v93,v94
v_min3_f32 v216,v64,v65,v66
v_min3_f32 v217,v68,v69,v70
)"
R"(CV v233,v234,v235
v_min3_f32 v218,v72,v73,v74
v_min3_f32 v219,v76,v77,v78
v_min3_f32 v220,v80,v81,v82
v_min3_f32 v221,v84,v85,v86
v_min3_f32 v222,v88,v89,v90
CV v234,v236,v237
v_min3_f32 v223,v92,v93,v94
)"
R"(v_max_f32 v208,v208,v67
v_max_f32 v209,v209,v71
v_max_f32 v210,v210,v75
v_max_f32 v211,v211,v79
CV v235,v238,v239
v_max_f32 v212,v212,v83
v_max_f32 v213,v213,v87
v_max_f32 v214,v214,v91
)"
R"(v_max_f32 v215,v215,v95
GS v3,v[232:235],s[44:45] offset:2048 sc1
v_min_f32 v216,v216,v67
v_min_f32 v217,v217,v71
v_min_f32 v218,v218,v75
v_min_f32 v219,v219,v79
v_min_f32 v220,v220,v83
AR v232,a76
)"
R"(v_min_f32 v221,v221,v87
v_min_f32 v222,v222,v91
v_min_f32 v223,v223,v95
v_mov_b32 v224,v208
v_mov_b32 v225,v209
AR v233,a77
v_mov_b32 v226,v210
v_mov_b32 v227,v211
)"
R"(v_mov_b32 v228,v212
v_mov_b32 v229,v213
AR v234,a78
v_mov_b32 v230,v214
v_mov_b32 v231,v215
v_permlane32_swap_b32 v208,v224
v_permlane32_swap_b32 v209,v225
v_permlane32_swap_b32 v210,v226
)"
R"(AR v235,a79
v_permlane32_swap_b32 v211,v227
v_permlane32_swap_b32 v212,v228
v_permlane32_swap_b32 v213,v229
v_permlane32_swap_b32 v214,v230
v_permlane32_swap_b32 v215,v231
AR v236,a108
v_max_f32 v208,v208,v224
)"
R"(v_max_f32 v209,v209,v225
v_max_f32 v210,v210,v226
v_max_f32 v211,v211,v227
AR v237,a109
v_max_f32 v212,v212,v228
v_max_f32 v213,v213,v229
v_max_f32 v214,v214,v230
v_max_f32 v215,v215,v231
)"
R"(v_mov_b32 v224,v208
AR v238,a110
v_mov_b32 v225,v209
v_mov_b32 v226,v210
v_mov_b32 v227,v211
v_mov_b32 v228,v212
v_mov_b32 v229,v213
AR v239,a111
)"
R"(v_mov_b32 v230,v214
v_mov_b32 v231,v215
v_permlane16_swap_b32 v208,v224
v_permlane16_swap_b32 v209,v225
CV v232,v232,v233
v_permlane16_swap_b32 v210,v226
v_permlane16_swap_b32 v211,v227
v_permlane16_swap_b32 v212,v228
)"
R"(v_permlane16_swap_b32 v213,v229
v_permlane16_swap_b32 v214,v230
CV v233,v234,v235
v_permlane16_swap_b32 v215,v231
v_max_f32 v208,v208,v224
v_max_f32 v209,v209,v225
v_max_f32 v210,v210,v226
v_max_f32 v211,v211,v227
)"
R"(CV v234,v236,v237
v_max_f32 v212,v212,v228
v_max_f32 v213,v213,v229
v_max_f32 v214,v214,v230
v_max_f32 v215,v215,v231
CV v235,v238,v239
v_mov_b32 v224,v216
v_mov_b32 v225,v217
)"
R"(v_mov_b32 v226,v218
v_mov_b32 v227,v219
v_mov_b32 v228,v220
GS v3,v[232:235],s[44:45] offset:3072 sc1
v_mov_b32 v229,v221
v_mov_b32 v230,v222
v_mov_b32 v231,v223
v_permlane32_swap_b32 v216,v224
)"
R"(v_permlane32_swap_b32 v217,v225
AR v232,a80
v_permlane32_swap_b32 v218,v226
v_permlane32_swap_b32 v219,v227
v_permlane32_swap_b32 v220,v228
v_permlane32_swap_b32 v221,v229
AR v233,a81
v_permlane32_swap_b32 v222,v230
)"
R"(v_permlane32_swap_b32 v223,v231
v_min_f32 v216,v216,v224
v_min_f32 v217,v217,v225
v_min_f32 v218,v218,v226
AR v234,a82
v_min_f32 v219,v219,v227
v_min_f32 v220,v220,v228
v_min_f32 v221,v221,v229
)"
R"(v_min_f32 v222,v222,v230
v_min_f32 v223,v223,v231
AR v235,a83
v_mov_b32 v224,v216
v_mov_b32 v225,v217
v_mov_b32 v226,v218
v_mov_b32 v227,v219
AR v236,a112
)"
R"(v_mov_b32 v228,v220
v_mov_b32 v229,v221
v_mov_b32 v230,v222
v_mov_b32 v231,v223
v_permlane16_swap_b32 v216,v224
AR v237,a113
v_permlane16_swap_b32 v217,v225
v_permlane16_swap_b32 v218,v226
)"
R"(v_permlane16_swap_b32 v219,v227
v_permlane16_swap_b32 v220,v228
v_permlane16_swap_b32 v221,v229
AR v238,a114
v_permlane16_swap_b32 v222,v230
v_permlane16_swap_b32 v223,v231
v_min_f32 v216,v216,v224
v_min_f32 v217,v217,v225
)"
R"(AR v239,a115
v_min_f32 v218,v218,v226
v_min_f32 v219,v219,v227
v_min_f32 v220,v220,v228
v_min_f32 v221,v221,v229
v_min_f32 v222,v222,v230
CV v232,v232,v233
v_min_f32 v223,v223,v231
)"
R"(MU v208,0xbfb8aa3b,v208
MU v209,0xbfb8aa3b,v209
MU v210,0xbfb8aa3b,v210
MU v211,0xbfb8aa3b,v211
CV v233,v234,v235
MU v212,0xbfb8aa3b,v212
MU v213,0xbfb8aa3b,v213
MU v214,0xbfb8aa3b,v214
)"
R"(MU v215,0xbfb8aa3b,v215
CV v234,v236,v237
MU v216,0x3fb8aa3b,v216
MU v217,0x3fb8aa3b,v217
MU v218,0x3fb8aa3b,v218
MU v219,0x3fb8aa3b,v219
MU v220,0x3fb8aa3b,v220
CV v235,v238,v239
)"
R"(MU v221,0x3fb8aa3b,v221
MU v222,0x3fb8aa3b,v222
MU v223,0x3fb8aa3b,v223
v_fma_f32 v96,v64,v30,v208
v_fma_f32 v100,v68,v30,v209
SA s44,s44,0x1000
v_fma_f32 v104,v72,v30,v210
v_fma_f32 v108,v76,v30,v211
)"
R"(v_fma_f32 v112,v80,v30,v212
v_fma_f32 v116,v84,v30,v213
s_addc_u32 s45,s45,0
v_fma_f32 v120,v88,v30,v214
v_fma_f32 v124,v92,v30,v215
v_fma_f32 v176,-v64,v30,v216
v_fma_f32 v180,-v68,v30,v217
v_fma_f32 v184,-v72,v30,v218
)"
R"(GS v3,v[232:235],s[44:45] offset:0 sc1
v_fma_f32 v188,-v76,v30,v219
v_fma_f32 v192,-v80,v30,v220
v_fma_f32 v196,-v84,v30,v221
v_fma_f32 v200,-v88,v30,v222
v_fma_f32 v204,-v92,v30,v223
AR v232,a84
v_fma_f32 v97,v65,v30,v208
)"
R"(v_fma_f32 v101,v69,v30,v209
v_fma_f32 v105,v73,v30,v210
v_fma_f32 v109,v77,v30,v211
AR v233,a85
v_fma_f32 v113,v81,v30,v212
v_fma_f32 v117,v85,v30,v213
v_fma_f32 v121,v89,v30,v214
v_fma_f32 v125,v93,v30,v215
)"
R"(v_fma_f32 v177,-v65,v30,v216
AR v234,a86
v_fma_f32 v181,-v69,v30,v217
v_fma_f32 v185,-v73,v30,v218
v_fma_f32 v189,-v77,v30,v219
v_fma_f32 v193,-v81,v30,v220
v_fma_f32 v197,-v85,v30,v221
AR v235,a87
)"
R"(v_fma_f32 v201,-v89,v30,v222
v_fma_f32 v205,-v93,v30,v223
v_fma_f32 v98,v66,v30,v208
v_fma_f32 v102,v70,v30,v209
AR v236,a116
v_fma_f32 v106,v74,v30,v210
v_fma_f32 v110,v78,v30,v211
v_fma_f32 v114,v82,v30,v212
)"
R"(v_fma_f32 v118,v86,v30,v213
v_fma_f32 v122,v90,v30,v214
AR v237,a117
v_fma_f32 v126,v94,v30,v215
v_fma_f32 v178,-v66,v30,v216
v_fma_f32 v182,-v70,v30,v217
v_fma_f32 v186,-v74,v30,v218
v_fma_f32 v190,-v78,v30,v219
)"
R"(AR v238,a118
v_fma_f32 v194,-v82,v30,v220
v_fma_f32 v198,-v86,v30,v221
v_fma_f32 v202,-v90,v30,v222
v_fma_f32 v206,-v94,v30,v223
AR v239,a119
v_fma_f32 v99,v67,v30,v208
v_fma_f32 v103,v71,v30,v209
)"
R"(v_fma_f32 v107,v75,v30,v210
v_fma_f32 v111,v79,v30,v211
v_fma_f32 v115,v83,v30,v212
CV v232,v232,v233
v_fma_f32 v119,v87,v30,v213
v_fma_f32 v123,v91,v30,v214
v_fma_f32 v127,v95,v30,v215
v_fma_f32 v179,-v67,v30,v216
)"
R"(v_fma_f32 v183,-v71,v30,v217
CV v233,v234,v235
v_fma_f32 v187,-v75,v30,v218
v_fma_f32 v191,-v79,v30,v219
v_fma_f32 v195,-v83,v30,v220
v_fma_f32 v199,-v87,v30,v221
CV v234,v236,v237
v_fma_f32 v203,-v91,v30,v222
)"
R"(v_fma_f32 v207,-v95,v30,v223
EX v96,v96
EX v100,v100
EX v104,v104
CV v235,v238,v239
EX v108,v108
EX v112,v112
EX v116,v116
)"
R"(EX v120,v120
EX v124,v124
GS v3,v[232:235],s[44:45] offset:1024 sc1
EX v176,v176
EX v180,v180
EX v184,v184
EX v188,v188
AR v232,a88
)"
R"(EX v192,v192
EX v196,v196
EX v200,v200
EX v204,v204
EX v97,v97
AR v233,a89
EX v101,v101
EX v105,v105
)"
R"(EX v109,v109
EX v113,v113
EX v117,v117
AR v234,a90
EX v121,v121
EX v125,v125
EX v177,v177
EX v181,v181
)"
R"(AR v235,a91
EX v185,v185
EX v189,v189
EX v193,v193
EX v197,v197
EX v201,v201
AR v236,a120
EX v205,v205
)"
R"(EX v98,v98
EX v102,v102
EX v106,v106
EX v110,v110
AR v237,a121
EX v114,v114
EX v118,v118
EX v122,v122
)"
R"(EX v126,v126
AR v238,a122
EX v178,v178
EX v182,v182
EX v186,v186
EX v190,v190
EX v194,v194
AR v239,a123
)"
R"(EX v198,v198
EX v202,v202
EX v206,v206
EX v99,v99
CV v232,v232,v233
EX v103,v103
EX v107,v107
EX v111,v111
)"
R"(EX v115,v115
EX v119,v119
CV v233,v234,v235
EX v123,v123
EX v127,v127
EX v179,v179
EX v183,v183
EX v187,v187
)"
R"(CV v234,v236,v237
EX v191,v191
EX v195,v195
EX v199,v199
EX v203,v203
CV v235,v238,v239
EX v207,v207
AD v208,v96,v97
)"
R"(AD v209,v100,v101
AD v210,v104,v105
AD v211,v108,v109
GS v3,v[232:235],s[44:45] offset:2048 sc1
AD v212,v112,v113
AD v213,v116,v117
AD v214,v120,v121
AD v215,v124,v125
)"
R"(AD v216,v176,v177
AR v232,a92
AD v217,v180,v181
AD v218,v184,v185
AD v219,v188,v189
AD v220,v192,v193
AR v233,a93
AD v221,v196,v197
)"
R"(AD v222,v200,v201
AD v223,v204,v205
AD v208,v208,v98
AD v209,v209,v102
AR v234,a94
AD v210,v210,v106
AD v211,v211,v110
AD v212,v212,v114
)"
R"(AD v213,v213,v118
AD v214,v214,v122
AR v235,a95
AD v215,v215,v126
AD v216,v216,v178
AD v217,v217,v182
AD v218,v218,v186
AR v236,a124
)"
R"(AD v219,v219,v190
AD v220,v220,v194
AD v221,v221,v198
AD v222,v222,v202
AD v223,v223,v206
AR v237,a125
AD v208,v208,v99
AD v209,v209,v103
)"
R"(AD v210,v210,v107
AD v211,v211,v111
AD v212,v212,v115
AR v238,a126
AD v213,v213,v119
AD v214,v214,v123
AD v215,v215,v127
AD v216,v216,v179
)"
R"(AR v239,a127
AD v217,v217,v183
AD v218,v218,v187
AD v219,v219,v191
AD v220,v220,v195
AD v221,v221,v199
CV v232,v232,v233
AD v222,v222,v203
)"
R"(AD v223,v223,v207
v_mov_b32 v224,v208
v_mov_b32 v225,v209
v_mov_b32 v226,v210
CV v233,v234,v235
v_mov_b32 v227,v211
v_mov_b32 v228,v212
v_mov_b32 v229,v213
)"
R"(v_mov_b32 v230,v214
CV v234,v236,v237
v_mov_b32 v231,v215
v_permlane32_swap_b32 v208,v224
v_permlane32_swap_b32 v209,v225
v_permlane32_swap_b32 v210,v226
v_permlane32_swap_b32 v211,v227
CV v235,v238,v239
)"
R"(v_permlane32_swap_b32 v212,v228
v_permlane32_swap_b32 v213,v229
v_permlane32_swap_b32 v214,v230
v_permlane32_swap_b32 v215,v231
AD v208,v208,v224
GS v3,v[232:235],s[44:45] offset:3072 sc1
AD v209,v209,v225
AD v210,v210,v226
)"
R"(AD v211,v211,v227
AD v212,v212,v228
AR v232,a128
AD v213,v213,v229
AD v214,v214,v230
AD v215,v215,v231
v_mov_b32 v224,v208
v_mov_b32 v225,v209
)"
R"(AR v233,a129
v_mov_b32 v226,v210
v_mov_b32 v227,v211
v_mov_b32 v228,v212
v_mov_b32 v229,v213
v_mov_b32 v230,v214
AR v234,a130
v_mov_b32 v231,v215
)"
R"(v_permlane16_swap_b32 v208,v224
v_permlane16_swap_b32 v209,v225
v_permlane16_swap_b32 v210,v226
AR v235,a131
v_permlane16_swap_b32 v211,v227
v_permlane16_swap_b32 v212,v228
v_permlane16_swap_b32 v213,v229
v_permlane16_swap_b32 v214,v230
)"
R"(v_permlane16_swap_b32 v215,v231
AR v236,a160
AD v208,v208,v224
AD v209,v209,v225
AD v210,v210,v226
AD v211,v211,v227
AD v212,v212,v228
AR v237,a161
)"
R"(AD v213,v213,v229
AD v214,v214,v230
AD v215,v215,v231
v_mov_b32 v224,v216
AR v238,a162
v_mov_b32 v225,v217
v_mov_b32 v226,v218
v_mov_b32 v227,v219
)"
R"(v_mov_b32 v228,v220
v_mov_b32 v229,v221
AR v239,a163
v_mov_b32 v230,v222
v_mov_b32 v231,v223
v_permlane32_swap_b32 v216,v224
v_permlane32_swap_b32 v217,v225
v_permlane32_swap_b32 v218,v226
)"
R"(CV v232,v232,v233
v_permlane32_swap_b32 v219,v227
v_permlane32_swap_b32 v220,v228
v_permlane32_swap_b32 v221,v229
v_permlane32_swap_b32 v222,v230
CV v233,v234,v235
v_permlane32_swap_b32 v223,v231
AD v216,v216,v224
)"
R"(AD v217,v217,v225
AD v218,v218,v226
AD v219,v219,v227
CV v234,v236,v237
AD v220,v220,v228
AD v221,v221,v229
AD v222,v222,v230
AD v223,v223,v231
)"
R"(v_mov_b32 v224,v216
CV v235,v238,v239
v_mov_b32 v225,v217
v_mov_b32 v226,v218
v_mov_b32 v227,v219
v_mov_b32 v228,v220
SA s44,s44,0x1000
v_mov_b32 v229,v221
)"
R"(v_mov_b32 v230,v222
v_mov_b32 v231,v223
v_permlane16_swap_b32 v216,v224
v_permlane16_swap_b32 v217,v225
s_addc_u32 s45,s45,0
v_permlane16_swap_b32 v218,v226
v_permlane16_swap_b32 v219,v227
v_permlane16_swap_b32 v220,v228
)"
R"(v_permlane16_swap_b32 v221,v229
v_permlane16_swap_b32 v222,v230
GS v3,v[232:235],s[44:45] offset:0 sc1
v_permlane16_swap_b32 v223,v231
AD v216,v216,v224
AD v217,v217,v225
AD v218,v218,v226
AR v232,a132
)"
R"(AD v219,v219,v227
AD v220,v220,v228
AD v221,v221,v229
AD v222,v222,v230
AD v223,v223,v231
AR v233,a133
v_rcp_f32 v208,v208
v_rcp_f32 v209,v209
)"
R"(v_rcp_f32 v210,v210
v_rcp_f32 v211,v211
v_rcp_f32 v212,v212
AR v234,a134
v_rcp_f32 v213,v213
v_rcp_f32 v214,v214
v_rcp_f32 v215,v215
v_rcp_f32 v216,v216
)"
R"(AR v235,a135
v_rcp_f32 v217,v217
v_rcp_f32 v218,v218
v_rcp_f32 v219,v219
v_rcp_f32 v220,v220
v_rcp_f32 v221,v221
AR v236,a164
v_rcp_f32 v222,v222
)"
R"(v_rcp_f32 v223,v223
MU v96,v96,v208
MU v100,v100,v209
MU v104,v104,v210
AR v237,a165
MU v108,v108,v211
MU v112,v112,v212
MU v116,v116,v213
)"
R"(MU v120,v120,v214
AR v238,a166
MU v124,v124,v215
MU v176,v176,v216
MU v180,v180,v217
MU v184,v184,v218
MU v188,v188,v219
AR v239,a167
)"
R"(MU v192,v192,v220
MU v196,v196,v221
MU v200,v200,v222
MU v204,v204,v223
MU v97,v97,v208
CV v232,v232,v233
MU v101,v101,v209
MU v105,v105,v210
)"
R"(MU v109,v109,v211
MU v113,v113,v212
CV v233,v234,v235
MU v117,v117,v213
MU v121,v121,v214
MU v125,v125,v215
MU v177,v177,v216
MU v181,v181,v217
)"
R"(CV v234,v236,v237
MU v185,v185,v218
MU v189,v189,v219
MU v193,v193,v220
MU v197,v197,v221
MU v201,v201,v222
CV v235,v238,v239
MU v205,v205,v223
)"
R"(MU v98,v98,v208
MU v102,v102,v209
MU v106,v106,v210
GS v3,v[232:235],s[44:45] offset:1024 sc1
MU v110,v110,v211
MU v114,v114,v212
MU v118,v118,v213
MU v122,v122,v214
)"
R"(MU v126,v126,v215
AR v232,a136
MU v178,v178,v216
MU v182,v182,v217
MU v186,v186,v218
MU v190,v190,v219
MU v194,v194,v220
AR v233,a137
)"
R"(MU v198,v198,v221
MU v202,v202,v222
MU v206,v206,v223
MU v99,v99,v208
AR v234,a138
MU v103,v103,v209
MU v107,v107,v210
MU v111,v111,v211
)"
R"(MU v115,v115,v212
MU v119,v119,v213
AR v235,a139
MU v123,v123,v214
MU v127,v127,v215
MU v179,v179,v216
MU v183,v183,v217
MU v187,v187,v218
)"
R"(AR v236,a168
MU v191,v191,v219
MU v195,v195,v220
MU v199,v199,v221
MU v203,v203,v222
AR v237,a169
MU v207,v207,v223
CV v64,v96,v97
)"
R"(CV v68,v100,v101
CV v72,v104,v105
CV v76,v108,v109
AR v238,a170
CV v80,v112,v113
CV v84,v116,v117
CV v88,v120,v121
CV v92,v124,v125
)"
R"(CV v66,v176,v177
AR v239,a171
CV v70,v180,v181
CV v74,v184,v185
CV v78,v188,v189
CV v82,v192,v193
CV v232,v232,v233
CV v86,v196,v197
)"
R"(CV v90,v200,v201
CV v94,v204,v205
CV v65,v98,v99
CV v69,v102,v103
CV v233,v234,v235
CV v73,v106,v107
CV v77,v110,v111
CV v81,v114,v115
)"
R"(CV v85,v118,v119
CV v89,v122,v123
CV v234,v236,v237
CV v93,v126,v127
CV v67,v178,v179
CV v71,v182,v183
CV v75,v186,v187
CV v235,v238,v239
)"
R"(CV v79,v190,v191
CV v83,v194,v195
CV v87,v198,v199
CV v91,v202,v203
CV v95,v206,v207
GS v3,v[232:235],s[44:45] offset:2048 sc1
GS v3,v[64:67],s[78:79] offset:0
GS v3,v[72:75],s[78:79] offset:1024
)"
R"(GS v3,v[80:83],s[78:79] offset:2048
GS v3,v[88:91],s[78:79] offset:3072
ds_write_b64 v10,v[68:69] offset:0
ds_write_b64 v10,v[70:71] offset:2048
ds_write_b64 v10,v[76:77] offset:512
ds_write_b64 v10,v[78:79] offset:2560
ds_write_b64 v10,v[84:85] offset:1024
ds_write_b64 v10,v[86:87] offset:3072
)"
R"(ds_write_b64 v10,v[92:93] offset:1536
ds_write_b64 v10,v[94:95] offset:3584
W lgkmcnt(0)
T v[96:97],v11 offset:0
T v[98:99],v11 offset:512
T v[100:101],v11 offset:2048
T v[102:103],v11 offset:2560
T v[104:105],v11 offset:1024
)"
R"(T v[106:107],v11 offset:1536
T v[108:109],v11 offset:3072
T v[110:111],v11 offset:3584
v_mov_b32 v112,0x3f803f80
v_mov_b32 v113,0x3f803f80
v_mov_b32 v114,0x3f803f80
v_mov_b32 v115,0x3f803f80
W lgkmcnt(6)
)"
R"(M v[116:119],v[96:99],v[48:51],0
W lgkmcnt(4)
M v[120:123],v[100:103],v[48:51],0
M v[124:127],v[96:99],v[112:115],0
M v[128:131],v[100:103],v[112:115],0
W lgkmcnt(2)
M v[116:119],v[104:107],v[52:55],v[116:119]
W lgkmcnt(0)
)"
R"(M v[120:123],v[108:111],v[52:55],v[120:123]
M v[124:127],v[104:107],v[112:115],v[124:127]
M v[128:131],v[108:111],v[112:115],v[128:131]
s_nop 3
ds_write_b128 v12,v[116:119] offset:0
s_nop 0
ds_write_b128 v12,v[120:123] offset:1024
ds_write_b128 v12,v[124:127] offset:2048
)"
R"(ds_write_b128 v12,v[128:131] offset:3072
W lgkmcnt(0)
s_barrier
R v[132:135],v13 offset:0
R v[136:139],v13 offset:4096
R v[140:143],v13 offset:8192
R v[144:147],v13 offset:12288
W lgkmcnt(2)
)"
R"(AD v132,v132,v136
AD v133,v133,v137
AD v134,v134,v138
AD v135,v135,v139
W lgkmcnt(1)
AD v132,v132,v140
AD v133,v133,v141
AD v134,v134,v142
)"
R"(AD v135,v135,v143
W lgkmcnt(0)
AD v132,v132,v144
AD v133,v133,v145
AD v134,v134,v146
AD v135,v135,v147
GS v3,v[132:135],s[80:81] sc1
W lgkmcnt(0)
)"
R"(s_barrier
v_xor_b32 v16,0,v28
v_xor_b32 v17,32,v28
v_xor_b32 v18,64,v28
v_xor_b32 v19,96,v28
v_xor_b32 v20,128,v28
v_xor_b32 v21,160,v28
v_xor_b32 v22,192,v28
)"
R"(v_xor_b32 v23,224,v28
s_lshl_b32 s42,%17,12
v_add_u32 v4,s42,v27
v_xor_b32 v5,64,v4
v_add_u32 v6,0x10000,v4
v_add_u32 v7,0x10000,v5
SA m0,s40,98304
SM s41,1048576
)"
R"(B %1,s[68:71],s41 offen lds
SA m0,s40,102400
SM s41,1179648
B %1,s[68:71],s41 offen lds
SA m0,s40,106496
SM s41,1310720
B %1,s[68:71],s41 offen lds
SA m0,s40,110592
)"
R"(SM s41,1441792
B %1,s[68:71],s41 offen lds
SA m0,s40,0
SA s41,s76,256
B %0,s[56:59],s41 offen lds
SA m0,s40,4096
SA s41,s76,33024
B %0,s[56:59],s41 offen lds
)"
R"(SA m0,s40,8192
SA s41,s76,65792
B %0,s[56:59],s41 offen lds
SA m0,s40,12288
SA s41,s76,98560
B %0,s[56:59],s41 offen lds
SA m0,s40,16384
SA s41,s77,256
)"
R"(B %0,s[56:59],s41 offen lds
SA m0,s40,20480
SA s41,s77,33024
B %0,s[56:59],s41 offen lds
SA m0,s40,24576
SA s41,s77,65792
B %0,s[56:59],s41 offen lds
SA m0,s40,28672
)"
R"(SA s41,s77,98560
B %0,s[56:59],s41 offen lds
W vmcnt(41) lgkmcnt(0)
s_barrier
T v[64:65],v16 offset:32768
T v[66:67],v16 offset:33792
T v[68:69],v17 offset:32768
T v[70:71],v17 offset:33792
)"
R"(T v[72:73],v18 offset:32768
T v[74:75],v18 offset:33792
T v[76:77],v19 offset:32768
T v[78:79],v19 offset:33792
T v[80:81],v20 offset:32768
T v[82:83],v20 offset:33792
T v[84:85],v21 offset:32768
T v[86:87],v21 offset:33792
)"
R"(T v[88:89],v22 offset:32768
T v[90:91],v22 offset:33792
T v[92:93],v23 offset:32768
T v[94:95],v23 offset:33792
R v[32:35],v4 offset:32768
R v[36:39],v4 offset:34816
R v[40:43],v4 offset:49152
R v[44:47],v4 offset:51200
)"
R"(W lgkmcnt(3)
M v[48:51],v[64:67],v[32:35],0
T v[96:97],v16 offset:40960
M v[52:55],v[68:71],v[32:35],0
T v[98:99],v16 offset:41984
AR v8,a140
M v[56:59],v[72:75],v[32:35],0
M v[60:63],v[76:79],v[32:35],0
)"
R"(T v[100:101],v17 offset:40960
AR v9,a141
M v[128:131],v[80:83],v[32:35],0
T v[102:103],v17 offset:41984
M v[132:135],v[84:87],v[32:35],0
M v[136:139],v[88:91],v[32:35],0
T v[104:105],v18 offset:40960
AR v10,a142
)"
R"(M v[140:143],v[92:95],v[32:35],0
R v[32:35],v5 offset:32768
T v[106:107],v18 offset:41984
W lgkmcnt(9)
M v[144:147],v[64:67],v[36:39],0
AR v11,a143
M v[148:151],v[68:71],v[36:39],0
T v[108:109],v19 offset:40960
)"
R"(M v[152:155],v[72:75],v[36:39],0
T v[110:111],v19 offset:41984
M v[156:159],v[76:79],v[36:39],0
AR v12,a172
M v[160:163],v[80:83],v[36:39],0
T v[112:113],v20 offset:40960
M v[164:167],v[84:87],v[36:39],0
T v[114:115],v20 offset:41984
)"
R"(AR v13,a173
M v[168:171],v[88:91],v[36:39],0
M v[172:175],v[92:95],v[36:39],0
R v[36:39],v5 offset:34816
T v[116:117],v21 offset:40960
W lgkmcnt(14)
M v[176:179],v[64:67],v[40:43],0
T v[118:119],v21 offset:41984
)"
R"(AR v14,a174
M v[180:183],v[68:71],v[40:43],0
M v[184:187],v[72:75],v[40:43],0
T v[120:121],v22 offset:40960
AR v15,a175
M v[188:191],v[76:79],v[40:43],0
T v[122:123],v22 offset:41984
M v[192:195],v[80:83],v[40:43],0
)"
R"(CV v8,v8,v9
M v[196:199],v[84:87],v[40:43],0
T v[124:125],v23 offset:40960
M v[200:203],v[88:91],v[40:43],0
T v[126:127],v23 offset:41984
M v[204:207],v[92:95],v[40:43],0
R v[40:43],v5 offset:49152
CV v9,v10,v11
)"
R"(W lgkmcnt(15)
M v[208:211],v[64:67],v[44:47],0
M v[212:215],v[68:71],v[44:47],0
CV v10,v12,v13
M v[216:219],v[72:75],v[44:47],0
M v[220:223],v[76:79],v[44:47],0
M v[224:227],v[80:83],v[44:47],0
CV v11,v14,v15
)"
R"(M v[228:231],v[84:87],v[44:47],0
M v[232:235],v[88:91],v[44:47],0
GS v3,v[8:11],s[44:45] offset:3072 sc1
M v[236:239],v[92:95],v[44:47],0
R v[44:47],v5 offset:51200
W vmcnt(29) lgkmcnt(0)
s_barrier
M v[48:51],v[96:99],v[32:35],v[48:51]
)"
R"(T v[64:65],v16 offset:49152
SA m0,s40,114688
M v[52:55],v[100:103],v[32:35],v[52:55]
T v[66:67],v16 offset:50176
SM s41,1572864
M v[56:59],v[104:107],v[32:35],v[56:59]
AR v8,a144
AR v9,a145
)"
R"(M v[60:63],v[108:111],v[32:35],v[60:63]
T v[68:69],v17 offset:49152
B %1,s[68:71],s41 offen lds
M v[128:131],v[112:115],v[32:35],v[128:131]
T v[70:71],v17 offset:50176
AR v10,a146
M v[132:135],v[116:119],v[32:35],v[132:135]
SA m0,s40,118784
)"
R"(AR v11,a147
M v[136:139],v[120:123],v[32:35],v[136:139]
T v[72:73],v18 offset:49152
AR v12,a176
M v[140:143],v[124:127],v[32:35],v[140:143]
R v[32:35],v6 offset:0
T v[74:75],v18 offset:50176
M v[144:147],v[96:99],v[36:39],v[144:147]
)"
R"(SM s41,1703936
B %1,s[68:71],s41 offen lds
M v[148:151],v[100:103],v[36:39],v[148:151]
T v[76:77],v19 offset:49152
AR v13,a177
M v[152:155],v[104:107],v[36:39],v[152:155]
T v[78:79],v19 offset:50176
SA m0,s40,122880
)"
R"(M v[156:159],v[108:111],v[36:39],v[156:159]
AR v14,a178
AR v15,a179
M v[160:163],v[112:115],v[36:39],v[160:163]
T v[80:81],v20 offset:49152
SM s41,1835008
M v[164:167],v[116:119],v[36:39],v[164:167]
T v[82:83],v20 offset:50176
)"
R"(CV v8,v8,v9
M v[168:171],v[120:123],v[36:39],v[168:171]
B %1,s[68:71],s41 offen lds
CV v9,v10,v11
M v[172:175],v[124:127],v[36:39],v[172:175]
R v[36:39],v6 offset:2048
T v[84:85],v21 offset:49152
M v[176:179],v[96:99],v[40:43],v[176:179]
)"
R"(T v[86:87],v21 offset:50176
SA m0,s40,126976
CV v10,v12,v13
M v[180:183],v[100:103],v[40:43],v[180:183]
SM s41,1966080
CV v11,v14,v15
M v[184:187],v[104:107],v[40:43],v[184:187]
T v[88:89],v22 offset:49152
)"
R"(SA s44,s44,0x1000
M v[188:191],v[108:111],v[40:43],v[188:191]
T v[90:91],v22 offset:50176
B %1,s[68:71],s41 offen lds
s_addc_u32 s45,s45,0
M v[192:195],v[112:115],v[40:43],v[192:195]
GS v3,v[8:11],s[44:45] offset:0 sc1
s_nop 1
)"
R"(AR v8,a148
M v[196:199],v[116:119],v[40:43],v[196:199]
T v[92:93],v23 offset:49152
SA m0,s40,32768
M v[200:203],v[120:123],v[40:43],v[200:203]
T v[94:95],v23 offset:50176
AR v9,a149
M v[204:207],v[124:127],v[40:43],v[204:207]
)"
R"(R v[40:43],v6 offset:16384
SA s41,s76,384
M v[208:211],v[96:99],v[44:47],v[208:211]
B %0,s[56:59],s41 offen lds
AR v10,a150
M v[212:215],v[100:103],v[44:47],v[212:215]
AR v11,a151
AR v12,a180
)"
R"(M v[216:219],v[104:107],v[44:47],v[216:219]
SA m0,s40,36864
AR v13,a181
M v[220:223],v[108:111],v[44:47],v[220:223]
AR v14,a182
AR v15,a183
M v[224:227],v[112:115],v[44:47],v[224:227]
SA s41,s76,33152
)"
R"(CV v8,v8,v9
M v[228:231],v[116:119],v[44:47],v[228:231]
CV v9,v10,v11
CV v10,v12,v13
M v[232:235],v[120:123],v[44:47],v[232:235]
B %0,s[56:59],s41 offen lds
CV v11,v14,v15
M v[236:239],v[124:127],v[44:47],v[236:239]
)"
R"(R v[44:47],v6 offset:18432
GS v3,v[8:11],s[44:45] offset:1024 sc1
W lgkmcnt(11)
M v[48:51],v[64:67],v[32:35],v[48:51]
T v[96:97],v16 offset:57344
SA m0,s40,40960
M v[52:55],v[68:71],v[32:35],v[52:55]
T v[98:99],v16 offset:58368
)"
R"(SA s41,s76,65920
M v[56:59],v[72:75],v[32:35],v[56:59]
B %0,s[56:59],s41 offen lds
SA m0,s40,45056
M v[60:63],v[76:79],v[32:35],v[60:63]
T v[100:101],v17 offset:57344
SA s41,s76,98688
W lgkmcnt(5)
)"
R"(M v[128:131],v[80:83],v[32:35],v[128:131]
T v[102:103],v17 offset:58368
B %0,s[56:59],s41 offen lds
M v[132:135],v[84:87],v[32:35],v[132:135]
SA m0,s40,49152
SA s41,s77,384
M v[136:139],v[88:91],v[32:35],v[136:139]
T v[104:105],v18 offset:57344
)"
R"(B %0,s[56:59],s41 offen lds
M v[140:143],v[92:95],v[32:35],v[140:143]
R v[32:35],v7 offset:0
T v[106:107],v18 offset:58368
M v[144:147],v[64:67],v[36:39],v[144:147]
SA m0,s40,53248
SA s41,s77,33152
M v[148:151],v[68:71],v[36:39],v[148:151]
)"
R"(T v[108:109],v19 offset:57344
B %0,s[56:59],s41 offen lds
AR v8,a152
M v[152:155],v[72:75],v[36:39],v[152:155]
T v[110:111],v19 offset:58368
SA m0,s40,57344
M v[156:159],v[76:79],v[36:39],v[156:159]
SA s41,s77,65920
)"
R"(B %0,s[56:59],s41 offen lds
M v[160:163],v[80:83],v[36:39],v[160:163]
T v[112:113],v20 offset:57344
SA m0,s40,61440
AR v9,a153
M v[164:167],v[84:87],v[36:39],v[164:167]
T v[114:115],v20 offset:58368
SA s41,s77,98688
)"
R"(M v[168:171],v[88:91],v[36:39],v[168:171]
B %0,s[56:59],s41 offen lds
s_sub_u32 s92,15,%20
AR v10,a154
M v[172:175],v[92:95],v[36:39],v[172:175]
R v[36:39],v7 offset:2048
T v[116:117],v21 offset:57344
s_lshl_b32 s92,s92,1
)"
R"(W lgkmcnt(14)
M v[176:179],v[64:67],v[40:43],v[176:179]
T v[118:119],v21 offset:58368
SA s92,s92,2
s_lshl_b32 s92,s92,14
M v[180:183],v[68:71],v[40:43],v[180:183]
s_lshl_b32 s42,%19,19
s_mov_b64 s[60:61],%16
)"
R"(AR v11,a155
M v[184:187],v[72:75],v[40:43],v[184:187]
T v[120:121],v22 offset:57344
SA s60,s60,s42
M v[188:191],v[76:79],v[40:43],v[188:191]
T v[122:123],v22 offset:58368
s_addc_u32 s61,s61,0
AR v12,a184
)"
R"(M v[192:195],v[80:83],v[40:43],v[192:195]
SA s60,s60,0x1000000
s_addc_u32 s61,s61,0
M v[196:199],v[84:87],v[40:43],v[196:199]
T v[124:125],v23 offset:57344
s_and_b32 s61,s61,0xffff
M v[200:203],v[88:91],v[40:43],v[200:203]
T v[126:127],v23 offset:58368
)"
R"(SM s62,s92
AR v13,a185
M v[204:207],v[92:95],v[40:43],v[204:207]
R v[40:43],v7 offset:16384
SM s63,0x20000
W lgkmcnt(15)
M v[208:211],v[64:67],v[44:47],v[208:211]
s_mov_b64 s[64:65],%16
)"
R"(SA s64,s64,s42
AR v14,a186
M v[212:215],v[68:71],v[44:47],v[212:215]
s_addc_u32 s65,s65,0
SA s64,s64,0x2000000
M v[216:219],v[72:75],v[44:47],v[216:219]
s_addc_u32 s65,s65,0
s_and_b32 s65,s65,0xffff
)"
R"(M v[220:223],v[76:79],v[44:47],v[220:223]
SM s66,s92
AR v15,a187
M v[224:227],v[80:83],v[44:47],v[224:227]
SM s67,0x20000
CV v8,v8,v9
M v[228:231],v[84:87],v[44:47],v[228:231]
CV v9,v10,v11
)"
R"(CV v10,v12,v13
M v[232:235],v[88:91],v[44:47],v[232:235]
CV v11,v14,v15
GS v3,v[8:11],s[44:45] offset:2048 sc1
M v[236:239],v[92:95],v[44:47],v[236:239]
R v[44:47],v7 offset:18432
W vmcnt(16) lgkmcnt(0)
s_barrier
)"
R"(M v[48:51],v[96:99],v[32:35],v[48:51]
T v[64:65],v16 offset:0
SA m0,s40,131072
M v[52:55],v[100:103],v[32:35],v[52:55]
T v[66:67],v16 offset:1024
SM s41,2097152
M v[56:59],v[104:107],v[32:35],v[56:59]
AR v8,a156
)"
R"(AR v9,a157
M v[60:63],v[108:111],v[32:35],v[60:63]
T v[68:69],v17 offset:0
B %1,s[68:71],s41 offen lds
M v[128:131],v[112:115],v[32:35],v[128:131]
T v[70:71],v17 offset:1024
AR v10,a158
M v[132:135],v[116:119],v[32:35],v[132:135]
)"
R"(SA m0,s40,135168
AR v11,a159
M v[136:139],v[120:123],v[32:35],v[136:139]
T v[72:73],v18 offset:0
AR v12,a188
M v[140:143],v[124:127],v[32:35],v[140:143]
R v[32:35],v4 offset:0
T v[74:75],v18 offset:1024
)"
R"(M v[144:147],v[96:99],v[36:39],v[144:147]
SM s41,2228224
B %1,s[68:71],s41 offen lds
M v[148:151],v[100:103],v[36:39],v[148:151]
T v[76:77],v19 offset:0
AR v13,a189
M v[152:155],v[104:107],v[36:39],v[152:155]
T v[78:79],v19 offset:1024
)"
R"(SA m0,s40,139264
M v[156:159],v[108:111],v[36:39],v[156:159]
AR v14,a190
AR v15,a191
M v[160:163],v[112:115],v[36:39],v[160:163]
T v[80:81],v20 offset:0
SM s41,2359296
M v[164:167],v[116:119],v[36:39],v[164:167]
)"
R"(T v[82:83],v20 offset:1024
CV v8,v8,v9
M v[168:171],v[120:123],v[36:39],v[168:171]
B %1,s[68:71],s41 offen lds
CV v9,v10,v11
M v[172:175],v[124:127],v[36:39],v[172:175]
R v[36:39],v4 offset:2048
T v[84:85],v21 offset:0
)"
R"(M v[176:179],v[96:99],v[40:43],v[176:179]
T v[86:87],v21 offset:1024
SA m0,s40,143360
CV v10,v12,v13
M v[180:183],v[100:103],v[40:43],v[180:183]
SM s41,2490368
CV v11,v14,v15
M v[184:187],v[104:107],v[40:43],v[184:187]
)"
R"(T v[88:89],v22 offset:0
GS v3,v[8:11],s[44:45] offset:3072 sc1
M v[188:191],v[108:111],v[40:43],v[188:191]
T v[90:91],v22 offset:1024
B %1,s[68:71],s41 offen lds
AR v8,a192
M v[192:195],v[112:115],v[40:43],v[192:195]
AR v9,a193
)"
R"(AR v10,a194
M v[196:199],v[116:119],v[40:43],v[196:199]
T v[92:93],v23 offset:0
SA m0,s40,65536
M v[200:203],v[120:123],v[40:43],v[200:203]
T v[94:95],v23 offset:1024
AR v11,a195
M v[204:207],v[124:127],v[40:43],v[204:207]
)"
R"(R v[40:43],v4 offset:16384
SA s41,s76,512
M v[208:211],v[96:99],v[44:47],v[208:211]
B %0,s[56:59],s41 offen lds
AR v12,a224
M v[212:215],v[100:103],v[44:47],v[212:215]
AR v13,a225
AR v14,a226
)"
R"(M v[216:219],v[104:107],v[44:47],v[216:219]
SA m0,s40,69632
AR v15,a227
M v[220:223],v[108:111],v[44:47],v[220:223]
CV v8,v8,v9
CV v9,v10,v11
M v[224:227],v[112:115],v[44:47],v[224:227]
SA s41,s76,33280
)"
R"(CV v10,v12,v13
M v[228:231],v[116:119],v[44:47],v[228:231]
CV v11,v14,v15
SA s44,s44,0x1000
M v[232:235],v[120:123],v[44:47],v[232:235]
B %0,s[56:59],s41 offen lds
s_addc_u32 s45,s45,0
M v[236:239],v[124:127],v[44:47],v[236:239]
)"
R"(R v[44:47],v4 offset:18432
GS v3,v[8:11],s[44:45] offset:0 sc1
W lgkmcnt(11)
M v[48:51],v[64:67],v[32:35],v[48:51]
T v[96:97],v16 offset:8192
SA m0,s40,73728
M v[52:55],v[68:71],v[32:35],v[52:55]
T v[98:99],v16 offset:9216
)"
R"(SA s41,s76,66048
M v[56:59],v[72:75],v[32:35],v[56:59]
B %0,s[56:59],s41 offen lds
AR v8,a196
M v[60:63],v[76:79],v[32:35],v[60:63]
T v[100:101],v17 offset:8192
AR v9,a197
W lgkmcnt(5)
)"
R"(M v[128:131],v[80:83],v[32:35],v[128:131]
T v[102:103],v17 offset:9216
SA m0,s40,77824
M v[132:135],v[84:87],v[32:35],v[132:135]
SA s41,s76,98816
AR v10,a198
M v[136:139],v[88:91],v[32:35],v[136:139]
T v[104:105],v18 offset:8192
)"
R"(B %0,s[56:59],s41 offen lds
M v[140:143],v[92:95],v[32:35],v[140:143]
R v[32:35],v5 offset:0
T v[106:107],v18 offset:9216
M v[144:147],v[64:67],v[36:39],v[144:147]
SA m0,s40,81920
AR v11,a199
M v[148:151],v[68:71],v[36:39],v[148:151]
)"
R"(T v[108:109],v19 offset:8192
SA s41,s77,512
AR v12,a228
M v[152:155],v[72:75],v[36:39],v[152:155]
T v[110:111],v19 offset:9216
B %0,s[56:59],s41 offen lds
M v[156:159],v[76:79],v[36:39],v[156:159]
AR v13,a229
)"
R"(AR v14,a230
M v[160:163],v[80:83],v[36:39],v[160:163]
T v[112:113],v20 offset:8192
SA m0,s40,86016
M v[164:167],v[84:87],v[36:39],v[164:167]
T v[114:115],v20 offset:9216
SA s41,s77,33280
AR v15,a231
)"
R"(M v[168:171],v[88:91],v[36:39],v[168:171]
CV v8,v8,v9
CV v9,v10,v11
M v[172:175],v[92:95],v[36:39],v[172:175]
R v[36:39],v5 offset:2048
T v[116:117],v21 offset:8192
W lgkmcnt(14)
M v[176:179],v[64:67],v[40:43],v[176:179]
)"
R"(T v[118:119],v21 offset:9216
B %0,s[56:59],s41 offen lds
M v[180:183],v[68:71],v[40:43],v[180:183]
SA m0,s40,90112
SA s41,s77,66048
CV v10,v12,v13
M v[184:187],v[72:75],v[40:43],v[184:187]
T v[120:121],v22 offset:8192
)"
R"(CV v11,v14,v15
M v[188:191],v[76:79],v[40:43],v[188:191]
T v[122:123],v22 offset:9216
B %0,s[56:59],s41 offen lds
GS v3,v[8:11],s[44:45] offset:1024 sc1
M v[192:195],v[80:83],v[40:43],v[192:195]
SA m0,s40,94208
AR v8,a200
)"
R"(M v[196:199],v[84:87],v[40:43],v[196:199]
T v[124:125],v23 offset:8192
SA s41,s77,98816
M v[200:203],v[88:91],v[40:43],v[200:203]
T v[126:127],v23 offset:9216
AR v9,a201
M v[204:207],v[92:95],v[40:43],v[204:207]
R v[40:43],v5 offset:16384
)"
R"(B %0,s[56:59],s41 offen lds
AR v10,a202
W lgkmcnt(15)
M v[208:211],v[64:67],v[44:47],v[208:211]
s_lshl_b32 s92,%20,12
AR v11,a203
M v[212:215],v[68:71],v[44:47],v[212:215]
AR v12,a232
)"
R"(AR v13,a233
M v[216:219],v[72:75],v[44:47],v[216:219]
s_sub_u32 s80,s80,s92
AR v14,a234
M v[220:223],v[76:79],v[44:47],v[220:223]
s_subb_u32 s81,s81,0
AR v15,a235
M v[224:227],v[80:83],v[44:47],v[224:227]
)"
R"(CV v8,v8,v9
CV v9,v10,v11
M v[228:231],v[84:87],v[44:47],v[228:231]
CV v10,v12,v13
M v[232:235],v[88:91],v[44:47],v[232:235]
CV v11,v14,v15
M v[236:239],v[92:95],v[44:47],v[236:239]
R v[44:47],v5 offset:18432
)"
R"(GS v3,v[8:11],s[44:45] offset:2048 sc1
W vmcnt(17) lgkmcnt(0)
s_barrier
M v[48:51],v[96:99],v[32:35],v[48:51]
T v[64:65],v16 offset:16384
SA m0,s40,147456
M v[52:55],v[100:103],v[32:35],v[52:55]
T v[66:67],v16 offset:17408
)"
R"(SM s41,2621440
M v[56:59],v[104:107],v[32:35],v[56:59]
AR v8,a204
M v[60:63],v[108:111],v[32:35],v[60:63]
T v[68:69],v17 offset:16384
B %1,s[68:71],s41 offen lds
M v[128:131],v[112:115],v[32:35],v[128:131]
T v[70:71],v17 offset:17408
)"
R"(AR v9,a205
M v[132:135],v[116:119],v[32:35],v[132:135]
SA m0,s40,151552
M v[136:139],v[120:123],v[32:35],v[136:139]
T v[72:73],v18 offset:16384
AR v10,a206
M v[140:143],v[124:127],v[32:35],v[140:143]
R v[32:35],v4 offset:32768
)"
R"(T v[74:75],v18 offset:17408
M v[144:147],v[96:99],v[36:39],v[144:147]
SM s41,2752512
B %1,s[68:71],s41 offen lds
M v[148:151],v[100:103],v[36:39],v[148:151]
T v[76:77],v19 offset:16384
AR v11,a207
M v[152:155],v[104:107],v[36:39],v[152:155]
)"
R"(T v[78:79],v19 offset:17408
SA m0,s40,155648
M v[156:159],v[108:111],v[36:39],v[156:159]
AR v12,a236
M v[160:163],v[112:115],v[36:39],v[160:163]
T v[80:81],v20 offset:16384
SM s41,2883584
M v[164:167],v[116:119],v[36:39],v[164:167]
)"
R"(T v[82:83],v20 offset:17408
AR v13,a237
M v[168:171],v[120:123],v[36:39],v[168:171]
B %1,s[68:71],s41 offen lds
M v[172:175],v[124:127],v[36:39],v[172:175]
R v[36:39],v4 offset:34816
T v[84:85],v21 offset:16384
M v[176:179],v[96:99],v[40:43],v[176:179]
)"
R"(T v[86:87],v21 offset:17408
SA m0,s40,159744
M v[180:183],v[100:103],v[40:43],v[180:183]
SM s41,3014656
AR v14,a238
M v[184:187],v[104:107],v[40:43],v[184:187]
T v[88:89],v22 offset:16384
AR v15,a239
)"
R"(M v[188:191],v[108:111],v[40:43],v[188:191]
T v[90:91],v22 offset:17408
B %1,s[68:71],s41 offen lds
M v[192:195],v[112:115],v[40:43],v[192:195]
CV v8,v8,v9
M v[196:199],v[116:119],v[40:43],v[196:199]
T v[92:93],v23 offset:16384
SA m0,s40,0
)"
R"(M v[200:203],v[120:123],v[40:43],v[200:203]
T v[94:95],v23 offset:17408
M v[204:207],v[124:127],v[40:43],v[204:207]
R v[40:43],v4 offset:49152
SA s41,s76,640
M v[208:211],v[96:99],v[44:47],v[208:211]
B %0,s[56:59],s41 offen lds
CV v9,v10,v11
)"
R"(M v[212:215],v[100:103],v[44:47],v[212:215]
CV v10,v12,v13
M v[216:219],v[104:107],v[44:47],v[216:219]
SA m0,s40,4096
M v[220:223],v[108:111],v[44:47],v[220:223]
M v[224:227],v[112:115],v[44:47],v[224:227]
SA s41,s76,33408
CV v11,v14,v15
)"
R"(M v[228:231],v[116:119],v[44:47],v[228:231]
M v[232:235],v[120:123],v[44:47],v[232:235]
B %0,s[56:59],s41 offen lds
GS v3,v[8:11],s[44:45] offset:3072 sc1
M v[236:239],v[124:127],v[44:47],v[236:239]
R v[44:47],v4 offset:51200
W lgkmcnt(11)
M v[48:51],v[64:67],v[32:35],v[48:51]
)"
R"(T v[96:97],v16 offset:24576
SA m0,s40,8192
M v[52:55],v[68:71],v[32:35],v[52:55]
T v[98:99],v16 offset:25600
SA s41,s76,66176
M v[56:59],v[72:75],v[32:35],v[56:59]
AR v8,a208
AR v9,a209
)"
R"(M v[60:63],v[76:79],v[32:35],v[60:63]
T v[100:101],v17 offset:24576
B %0,s[56:59],s41 offen lds
W lgkmcnt(5)
M v[128:131],v[80:83],v[32:35],v[128:131]
T v[102:103],v17 offset:25600
SA m0,s40,12288
M v[132:135],v[84:87],v[32:35],v[132:135]
)"
R"(AR v10,a210
AR v11,a211
M v[136:139],v[88:91],v[32:35],v[136:139]
T v[104:105],v18 offset:24576
SA s41,s76,98944
M v[140:143],v[92:95],v[32:35],v[140:143]
R v[32:35],v5 offset:32768
T v[106:107],v18 offset:25600
)"
R"(M v[144:147],v[64:67],v[36:39],v[144:147]
B %0,s[56:59],s41 offen lds
AR v12,a240
M v[148:151],v[68:71],v[36:39],v[148:151]
T v[108:109],v19 offset:24576
SA m0,s40,16384
M v[152:155],v[72:75],v[36:39],v[152:155]
T v[110:111],v19 offset:25600
)"
R"(AR v13,a241
M v[156:159],v[76:79],v[36:39],v[156:159]
SA s41,s77,640
AR v14,a242
M v[160:163],v[80:83],v[36:39],v[160:163]
T v[112:113],v20 offset:24576
B %0,s[56:59],s41 offen lds
AR v15,a243
)"
R"(M v[164:167],v[84:87],v[36:39],v[164:167]
T v[114:115],v20 offset:25600
CV v8,v8,v9
M v[168:171],v[88:91],v[36:39],v[168:171]
SA m0,s40,20480
CV v9,v10,v11
M v[172:175],v[92:95],v[36:39],v[172:175]
R v[36:39],v5 offset:34816
)"
R"(T v[116:117],v21 offset:24576
W lgkmcnt(14)
M v[176:179],v[64:67],v[40:43],v[176:179]
T v[118:119],v21 offset:25600
SA s41,s77,33408
CV v10,v12,v13
M v[180:183],v[68:71],v[40:43],v[180:183]
B %0,s[56:59],s41 offen lds
)"
R"(CV v11,v14,v15
M v[184:187],v[72:75],v[40:43],v[184:187]
T v[120:121],v22 offset:24576
SA s44,s44,0x1000
M v[188:191],v[76:79],v[40:43],v[188:191]
T v[122:123],v22 offset:25600
SA m0,s40,24576
s_addc_u32 s45,s45,0
)"
R"(M v[192:195],v[80:83],v[40:43],v[192:195]
SA s41,s77,66176
GS v3,v[8:11],s[44:45] offset:0 sc1
M v[196:199],v[84:87],v[40:43],v[196:199]
T v[124:125],v23 offset:24576
AR v8,a212
M v[200:203],v[88:91],v[40:43],v[200:203]
T v[126:127],v23 offset:25600
)"
R"(B %0,s[56:59],s41 offen lds
AR v9,a213
M v[204:207],v[92:95],v[40:43],v[204:207]
R v[40:43],v5 offset:49152
AR v10,a214
W lgkmcnt(15)
M v[208:211],v[64:67],v[44:47],v[208:211]
SA m0,s40,28672
)"
R"(AR v11,a215
M v[212:215],v[68:71],v[44:47],v[212:215]
SA s41,s77,98944
AR v12,a244
M v[216:219],v[72:75],v[44:47],v[216:219]
AR v13,a245
AR v14,a246
M v[220:223],v[76:79],v[44:47],v[220:223]
)"
R"(B %0,s[56:59],s41 offen lds
AR v15,a247
M v[224:227],v[80:83],v[44:47],v[224:227]
CV v8,v8,v9
CV v9,v10,v11
M v[228:231],v[84:87],v[44:47],v[228:231]
CV v10,v12,v13
M v[232:235],v[88:91],v[44:47],v[232:235]
)"
R"(CV v11,v14,v15
M v[236:239],v[92:95],v[44:47],v[236:239]
R v[44:47],v5 offset:51200
GS v3,v[8:11],s[44:45] offset:1024 sc1
W vmcnt(16) lgkmcnt(0)
s_barrier
M v[48:51],v[96:99],v[32:35],v[48:51]
T v[64:65],v16 offset:32768
)"
R"(SA m0,s40,98304
M v[52:55],v[100:103],v[32:35],v[52:55]
T v[66:67],v16 offset:33792
SM s41,3145728
M v[56:59],v[104:107],v[32:35],v[56:59]
AR v8,a216
AR v9,a217
M v[60:63],v[108:111],v[32:35],v[60:63]
)"
R"(T v[68:69],v17 offset:32768
B %1,s[68:71],s41 offen lds
M v[128:131],v[112:115],v[32:35],v[128:131]
T v[70:71],v17 offset:33792
AR v10,a218
M v[132:135],v[116:119],v[32:35],v[132:135]
SA m0,s40,102400
AR v11,a219
)"
R"(M v[136:139],v[120:123],v[32:35],v[136:139]
T v[72:73],v18 offset:32768
AR v12,a248
M v[140:143],v[124:127],v[32:35],v[140:143]
R v[32:35],v6 offset:0
T v[74:75],v18 offset:33792
M v[144:147],v[96:99],v[36:39],v[144:147]
SM s41,3276800
)"
R"(B %1,s[68:71],s41 offen lds
M v[148:151],v[100:103],v[36:39],v[148:151]
T v[76:77],v19 offset:32768
AR v13,a249
M v[152:155],v[104:107],v[36:39],v[152:155]
T v[78:79],v19 offset:33792
SA m0,s40,106496
M v[156:159],v[108:111],v[36:39],v[156:159]
)"
R"(AR v14,a250
AR v15,a251
M v[160:163],v[112:115],v[36:39],v[160:163]
T v[80:81],v20 offset:32768
SM s41,3407872
M v[164:167],v[116:119],v[36:39],v[164:167]
T v[82:83],v20 offset:33792
CV v8,v8,v9
)"
R"(M v[168:171],v[120:123],v[36:39],v[168:171]
B %1,s[68:71],s41 offen lds
CV v9,v10,v11
M v[172:175],v[124:127],v[36:39],v[172:175]
R v[36:39],v6 offset:2048
T v[84:85],v21 offset:32768
M v[176:179],v[96:99],v[40:43],v[176:179]
T v[86:87],v21 offset:33792
)"
R"(SA m0,s40,110592
M v[180:183],v[100:103],v[40:43],v[180:183]
SM s41,3538944
CV v10,v12,v13
M v[184:187],v[104:107],v[40:43],v[184:187]
T v[88:89],v22 offset:32768
CV v11,v14,v15
M v[188:191],v[108:111],v[40:43],v[188:191]
)"
R"(T v[90:91],v22 offset:33792
B %1,s[68:71],s41 offen lds
GS v3,v[8:11],s[44:45] offset:2048 sc1
M v[192:195],v[112:115],v[40:43],v[192:195]
s_nop 0
AR v8,a220
AR v9,a221
M v[196:199],v[116:119],v[40:43],v[196:199]
)"
R"(T v[92:93],v23 offset:32768
SA m0,s40,32768
M v[200:203],v[120:123],v[40:43],v[200:203]
T v[94:95],v23 offset:33792
AR v10,a222
M v[204:207],v[124:127],v[40:43],v[204:207]
R v[40:43],v6 offset:16384
SA s41,s76,768
)"
R"(M v[208:211],v[96:99],v[44:47],v[208:211]
B %0,s[56:59],s41 offen lds
AR v11,a223
M v[212:215],v[100:103],v[44:47],v[212:215]
AR v12,a252
AR v13,a253
M v[216:219],v[104:107],v[44:47],v[216:219]
SA m0,s40,36864
)"
R"(AR v14,a254
M v[220:223],v[108:111],v[44:47],v[220:223]
AR v15,a255
CV v8,v8,v9
M v[224:227],v[112:115],v[44:47],v[224:227]
SA s41,s76,33536
CV v9,v10,v11
M v[228:231],v[116:119],v[44:47],v[228:231]
)"
R"(CV v10,v12,v13
M v[232:235],v[120:123],v[44:47],v[232:235]
B %0,s[56:59],s41 offen lds
CV v11,v14,v15
M v[236:239],v[124:127],v[44:47],v[236:239]
R v[44:47],v6 offset:18432
GS v3,v[8:11],s[44:45] offset:3072 sc1
W lgkmcnt(11)
)"
R"(M v[48:51],v[64:67],v[32:35],v[48:51]
T v[96:97],v16 offset:40960
SA m0,s40,40960
M v[52:55],v[68:71],v[32:35],v[52:55]
T v[98:99],v16 offset:41984
SA s41,s76,66304
M v[56:59],v[72:75],v[32:35],v[56:59]
AW a0,0
)"
R"(AW a1,0
M v[60:63],v[76:79],v[32:35],v[60:63]
T v[100:101],v17 offset:40960
B %0,s[56:59],s41 offen lds
W lgkmcnt(5)
M v[128:131],v[80:83],v[32:35],v[128:131]
T v[102:103],v17 offset:41984
SA m0,s40,45056
)"
R"(M v[132:135],v[84:87],v[32:35],v[132:135]
AW a2,0
M v[136:139],v[88:91],v[32:35],v[136:139]
T v[104:105],v18 offset:40960
SA s41,s76,99072
M v[140:143],v[92:95],v[32:35],v[140:143]
R v[32:35],v7 offset:0
T v[106:107],v18 offset:41984
)"
R"(M v[144:147],v[64:67],v[36:39],v[144:147]
B %0,s[56:59],s41 offen lds
AW a3,0
M v[148:151],v[68:71],v[36:39],v[148:151]
T v[108:109],v19 offset:40960
SA m0,s40,49152
M v[152:155],v[72:75],v[36:39],v[152:155]
T v[110:111],v19 offset:41984
)"
R"(AW a4,0
M v[156:159],v[76:79],v[36:39],v[156:159]
SA s41,s77,768
AW a5,0
M v[160:163],v[80:83],v[36:39],v[160:163]
T v[112:113],v20 offset:40960
B %0,s[56:59],s41 offen lds
M v[164:167],v[84:87],v[36:39],v[164:167]
)"
R"(T v[114:115],v20 offset:41984
AW a6,0
M v[168:171],v[88:91],v[36:39],v[168:171]
SA m0,s40,53248
AW a7,0
M v[172:175],v[92:95],v[36:39],v[172:175]
R v[36:39],v7 offset:2048
T v[116:117],v21 offset:40960
)"
R"(W lgkmcnt(14)
M v[176:179],v[64:67],v[40:43],v[176:179]
T v[118:119],v21 offset:41984
SA s41,s77,33536
M v[180:183],v[68:71],v[40:43],v[180:183]
B %0,s[56:59],s41 offen lds
AW a8,0
M v[184:187],v[72:75],v[40:43],v[184:187]
)"
R"(T v[120:121],v22 offset:40960
AW a9,0
M v[188:191],v[76:79],v[40:43],v[188:191]
T v[122:123],v22 offset:41984
SA m0,s40,57344
M v[192:195],v[80:83],v[40:43],v[192:195]
SA s41,s77,66304
AW a10,0
)"
R"(M v[196:199],v[84:87],v[40:43],v[196:199]
T v[124:125],v23 offset:40960
M v[200:203],v[88:91],v[40:43],v[200:203]
T v[126:127],v23 offset:41984
B %0,s[56:59],s41 offen lds
M v[204:207],v[92:95],v[40:43],v[204:207]
R v[40:43],v7 offset:16384
AW a11,0
)"
R"(W lgkmcnt(15)
M v[208:211],v[64:67],v[44:47],v[208:211]
SA m0,s40,61440
AW a12,0
M v[212:215],v[68:71],v[44:47],v[212:215]
SA s41,s77,99072
M v[216:219],v[72:75],v[44:47],v[216:219]
AW a13,0
)"
R"(M v[220:223],v[76:79],v[44:47],v[220:223]
B %0,s[56:59],s41 offen lds
M v[224:227],v[80:83],v[44:47],v[224:227]
AW a14,0
M v[228:231],v[84:87],v[44:47],v[228:231]
M v[232:235],v[88:91],v[44:47],v[232:235]
AW a15,0
M v[236:239],v[92:95],v[44:47],v[236:239]
)"
R"(R v[44:47],v7 offset:18432
W vmcnt(6) lgkmcnt(0)
s_barrier
s_cmp_lg_u32 %17,0
s_cbranch_scc1 NP5_%=
s_lshl_b32 s92,s95,6
s_mov_b64 s[90:91],%16
SA s90,s90,s92
)"
R"(s_addc_u32 s91,s91,0
SA s90,s90,0x5001000
s_addc_u32 s91,s91,0
v_mov_b32 v8,s88
v_mov_b32 v9,0
s_mov_b64 exec,1
s_nop 1
global_store_dword v9,v8,s[90:91] sc1
)"
R"(s_mov_b64 exec,-1
s_nop 1
NP5_%=:
M v[48:51],v[96:99],v[32:35],v[48:51]
T v[64:65],v16 offset:49152
SA m0,s40,114688
M v[52:55],v[100:103],v[32:35],v[52:55]
T v[66:67],v16 offset:50176
)"
R"(SM s41,3670016
M v[56:59],v[104:107],v[32:35],v[56:59]
AW a16,0
AW a17,0
M v[60:63],v[108:111],v[32:35],v[60:63]
T v[68:69],v17 offset:49152
B %1,s[68:71],s41 offen lds
M v[128:131],v[112:115],v[32:35],v[128:131]
)"
R"(T v[70:71],v17 offset:50176
AW a18,0
M v[132:135],v[116:119],v[32:35],v[132:135]
SA m0,s40,118784
M v[136:139],v[120:123],v[32:35],v[136:139]
T v[72:73],v18 offset:49152
AW a19,0
M v[140:143],v[124:127],v[32:35],v[140:143]
)"
R"(R v[32:35],v4 offset:0
T v[74:75],v18 offset:50176
M v[144:147],v[96:99],v[36:39],v[144:147]
SM s41,3801088
B %1,s[68:71],s41 offen lds
M v[148:151],v[100:103],v[36:39],v[148:151]
T v[76:77],v19 offset:49152
AW a20,0
)"
R"(M v[152:155],v[104:107],v[36:39],v[152:155]
T v[78:79],v19 offset:50176
SA m0,s40,122880
M v[156:159],v[108:111],v[36:39],v[156:159]
AW a21,0
M v[160:163],v[112:115],v[36:39],v[160:163]
T v[80:81],v20 offset:49152
SM s41,3932160
)"
R"(M v[164:167],v[116:119],v[36:39],v[164:167]
T v[82:83],v20 offset:50176
AW a22,0
M v[168:171],v[120:123],v[36:39],v[168:171]
B %1,s[68:71],s41 offen lds
AW a23,0
M v[172:175],v[124:127],v[36:39],v[172:175]
R v[36:39],v4 offset:2048
)"
R"(T v[84:85],v21 offset:49152
M v[176:179],v[96:99],v[40:43],v[176:179]
T v[86:87],v21 offset:50176
SA m0,s40,126976
M v[180:183],v[100:103],v[40:43],v[180:183]
SM s41,4063232
AW a24,0
M v[184:187],v[104:107],v[40:43],v[184:187]
)"
R"(T v[88:89],v22 offset:49152
AW a25,0
M v[188:191],v[108:111],v[40:43],v[188:191]
T v[90:91],v22 offset:50176
B %1,s[68:71],s41 offen lds
M v[192:195],v[112:115],v[40:43],v[192:195]
AW a26,0
M v[196:199],v[116:119],v[40:43],v[196:199]
)"
R"(T v[92:93],v23 offset:49152
SA m0,s40,65536
M v[200:203],v[120:123],v[40:43],v[200:203]
T v[94:95],v23 offset:50176
AW a27,0
M v[204:207],v[124:127],v[40:43],v[204:207]
R v[40:43],v4 offset:16384
SA s41,s76,896
)"
R"(M v[208:211],v[96:99],v[44:47],v[208:211]
B %0,s[56:59],s41 offen lds
AW a28,0
M v[212:215],v[100:103],v[44:47],v[212:215]
M v[216:219],v[104:107],v[44:47],v[216:219]
SA m0,s40,69632
AW a29,0
M v[220:223],v[108:111],v[44:47],v[220:223]
)"
R"(M v[224:227],v[112:115],v[44:47],v[224:227]
SA s41,s76,33664
AW a30,0
M v[228:231],v[116:119],v[44:47],v[228:231]
M v[232:235],v[120:123],v[44:47],v[232:235]
B %0,s[56:59],s41 offen lds
AW a31,0
M v[236:239],v[124:127],v[44:47],v[236:239]
)"
R"(R v[44:47],v4 offset:18432
W lgkmcnt(11)
M v[48:51],v[64:67],v[32:35],v[48:51]
T v[96:97],v16 offset:57344
SA m0,s40,73728
M v[52:55],v[68:71],v[32:35],v[52:55]
T v[98:99],v16 offset:58368
SA s41,s76,66432
)"
R"(M v[56:59],v[72:75],v[32:35],v[56:59]
AW a32,0
AW a33,0
M v[60:63],v[76:79],v[32:35],v[60:63]
T v[100:101],v17 offset:57344
B %0,s[56:59],s41 offen lds
W lgkmcnt(5)
M v[128:131],v[80:83],v[32:35],v[128:131]
)"
R"(T v[102:103],v17 offset:58368
SA m0,s40,77824
M v[132:135],v[84:87],v[32:35],v[132:135]
AW a34,0
M v[136:139],v[88:91],v[32:35],v[136:139]
T v[104:105],v18 offset:57344
SA s41,s76,99200
M v[140:143],v[92:95],v[32:35],v[140:143]
)"
R"(R v[32:35],v5 offset:0
T v[106:107],v18 offset:58368
M v[144:147],v[64:67],v[36:39],v[144:147]
B %0,s[56:59],s41 offen lds
AW a35,0
M v[148:151],v[68:71],v[36:39],v[148:151]
T v[108:109],v19 offset:57344
SA m0,s40,81920
)"
R"(M v[152:155],v[72:75],v[36:39],v[152:155]
T v[110:111],v19 offset:58368
AW a36,0
M v[156:159],v[76:79],v[36:39],v[156:159]
SA s41,s77,896
AW a37,0
M v[160:163],v[80:83],v[36:39],v[160:163]
T v[112:113],v20 offset:57344
)"
R"(B %0,s[56:59],s41 offen lds
M v[164:167],v[84:87],v[36:39],v[164:167]
T v[114:115],v20 offset:58368
AW a38,0
M v[168:171],v[88:91],v[36:39],v[168:171]
SA m0,s40,86016
AW a39,0
M v[172:175],v[92:95],v[36:39],v[172:175]
)"
R"(R v[36:39],v5 offset:2048
T v[116:117],v21 offset:57344
W lgkmcnt(14)
M v[176:179],v[64:67],v[40:43],v[176:179]
T v[118:119],v21 offset:58368
SA s41,s77,33664
M v[180:183],v[68:71],v[40:43],v[180:183]
B %0,s[56:59],s41 offen lds
)"
R"(AW a40,0
M v[184:187],v[72:75],v[40:43],v[184:187]
T v[120:121],v22 offset:57344
AW a41,0
M v[188:191],v[76:79],v[40:43],v[188:191]
T v[122:123],v22 offset:58368
SA m0,s40,90112
M v[192:195],v[80:83],v[40:43],v[192:195]
)"
R"(SA s41,s77,66432
AW a42,0
M v[196:199],v[84:87],v[40:43],v[196:199]
T v[124:125],v23 offset:57344
M v[200:203],v[88:91],v[40:43],v[200:203]
T v[126:127],v23 offset:58368
B %0,s[56:59],s41 offen lds
M v[204:207],v[92:95],v[40:43],v[204:207]
)"
R"(R v[40:43],v5 offset:16384
AW a43,0
W lgkmcnt(15)
M v[208:211],v[64:67],v[44:47],v[208:211]
SA m0,s40,94208
AW a44,0
M v[212:215],v[68:71],v[44:47],v[212:215]
SA s41,s77,99200
)"
R"(M v[216:219],v[72:75],v[44:47],v[216:219]
AW a45,0
M v[220:223],v[76:79],v[44:47],v[220:223]
B %0,s[56:59],s41 offen lds
M v[224:227],v[80:83],v[44:47],v[224:227]
AW a46,0
M v[228:231],v[84:87],v[44:47],v[228:231]
M v[232:235],v[88:91],v[44:47],v[232:235]
)"
R"(AW a47,0
M v[236:239],v[92:95],v[44:47],v[236:239]
R v[44:47],v5 offset:18432
W vmcnt(12) lgkmcnt(0)
s_barrier
M v[48:51],v[96:99],v[32:35],v[48:51]
T v[64:65],v16 offset:0
s_mov_b64 s[90:91],%16
)"
R"(M v[52:55],v[100:103],v[32:35],v[52:55]
T v[66:67],v16 offset:1024
AW a48,0
M v[56:59],v[104:107],v[32:35],v[56:59]
AW a49,0
M v[60:63],v[108:111],v[32:35],v[60:63]
T v[68:69],v17 offset:0
M v[128:131],v[112:115],v[32:35],v[128:131]
)"
R"(T v[70:71],v17 offset:1024
AW a50,0
M v[132:135],v[116:119],v[32:35],v[132:135]
M v[136:139],v[120:123],v[32:35],v[136:139]
T v[72:73],v18 offset:0
AW a51,0
M v[140:143],v[124:127],v[32:35],v[140:143]
R v[32:35],v4 offset:32768
)"
R"(T v[74:75],v18 offset:1024
M v[144:147],v[96:99],v[36:39],v[144:147]
SA s90,s90,0x5000000
AW a52,0
M v[148:151],v[100:103],v[36:39],v[148:151]
T v[76:77],v19 offset:0
M v[152:155],v[104:107],v[36:39],v[152:155]
T v[78:79],v19 offset:1024
)"
R"(AW a53,0
M v[156:159],v[108:111],v[36:39],v[156:159]
M v[160:163],v[112:115],v[36:39],v[160:163]
T v[80:81],v20 offset:0
AW a54,0
M v[164:167],v[116:119],v[36:39],v[164:167]
T v[82:83],v20 offset:1024
M v[168:171],v[120:123],v[36:39],v[168:171]
)"
R"(AW a55,0
M v[172:175],v[124:127],v[36:39],v[172:175]
R v[36:39],v4 offset:34816
T v[84:85],v21 offset:0
M v[176:179],v[96:99],v[40:43],v[176:179]
T v[86:87],v21 offset:1024
s_addc_u32 s91,s91,0
M v[180:183],v[100:103],v[40:43],v[180:183]
)"
R"(AW a56,0
M v[184:187],v[104:107],v[40:43],v[184:187]
T v[88:89],v22 offset:0
AW a57,0
M v[188:191],v[108:111],v[40:43],v[188:191]
T v[90:91],v22 offset:1024
M v[192:195],v[112:115],v[40:43],v[192:195]
AW a58,0
)"
R"(M v[196:199],v[116:119],v[40:43],v[196:199]
T v[92:93],v23 offset:0
M v[200:203],v[120:123],v[40:43],v[200:203]
T v[94:95],v23 offset:1024
AW a59,0
M v[204:207],v[124:127],v[40:43],v[204:207]
R v[40:43],v4 offset:49152
M v[208:211],v[96:99],v[44:47],v[208:211]
)"
R"(global_load_dword v2,v1,s[90:91] sc1
AW a60,0
M v[212:215],v[100:103],v[44:47],v[212:215]
M v[216:219],v[104:107],v[44:47],v[216:219]
AW a61,0
M v[220:223],v[108:111],v[44:47],v[220:223]
M v[224:227],v[112:115],v[44:47],v[224:227]
AW a62,0
)"
R"(M v[228:231],v[116:119],v[44:47],v[228:231]
M v[232:235],v[120:123],v[44:47],v[232:235]
AW a63,0
M v[236:239],v[124:127],v[44:47],v[236:239]
R v[44:47],v4 offset:51200
W lgkmcnt(11)
M v[48:51],v[64:67],v[32:35],v[48:51]
T v[96:97],v16 offset:8192
)"
R"(AW a64,0
M v[52:55],v[68:71],v[32:35],v[52:55]
T v[98:99],v16 offset:9216
M v[56:59],v[72:75],v[32:35],v[56:59]
AW a65,0
M v[60:63],v[76:79],v[32:35],v[60:63]
T v[100:101],v17 offset:8192
W lgkmcnt(5)
)"
R"(M v[128:131],v[80:83],v[32:35],v[128:131]
T v[102:103],v17 offset:9216
AW a66,0
M v[132:135],v[84:87],v[32:35],v[132:135]
M v[136:139],v[88:91],v[32:35],v[136:139]
T v[104:105],v18 offset:8192
AW a67,0
M v[140:143],v[92:95],v[32:35],v[140:143]
)"
R"(R v[32:35],v5 offset:32768
T v[106:107],v18 offset:9216
M v[144:147],v[64:67],v[36:39],v[144:147]
AW a68,0
M v[148:151],v[68:71],v[36:39],v[148:151]
T v[108:109],v19 offset:8192
M v[152:155],v[72:75],v[36:39],v[152:155]
T v[110:111],v19 offset:9216
)"
R"(AW a69,0
M v[156:159],v[76:79],v[36:39],v[156:159]
M v[160:163],v[80:83],v[36:39],v[160:163]
T v[112:113],v20 offset:8192
AW a70,0
M v[164:167],v[84:87],v[36:39],v[164:167]
T v[114:115],v20 offset:9216
M v[168:171],v[88:91],v[36:39],v[168:171]
)"
R"(AW a71,0
M v[172:175],v[92:95],v[36:39],v[172:175]
R v[36:39],v5 offset:34816
T v[116:117],v21 offset:8192
W lgkmcnt(14)
M v[176:179],v[64:67],v[40:43],v[176:179]
T v[118:119],v21 offset:9216
AW a72,0
)"
R"(M v[180:183],v[68:71],v[40:43],v[180:183]
M v[184:187],v[72:75],v[40:43],v[184:187]
T v[120:121],v22 offset:8192
AW a73,0
M v[188:191],v[76:79],v[40:43],v[188:191]
T v[122:123],v22 offset:9216
M v[192:195],v[80:83],v[40:43],v[192:195]
AW a74,0
)"
R"(M v[196:199],v[84:87],v[40:43],v[196:199]
T v[124:125],v23 offset:8192
M v[200:203],v[88:91],v[40:43],v[200:203]
T v[126:127],v23 offset:9216
AW a75,0
M v[204:207],v[92:95],v[40:43],v[204:207]
R v[40:43],v5 offset:49152
W lgkmcnt(15)
)"
R"(M v[208:211],v[64:67],v[44:47],v[208:211]
AW a76,0
M v[212:215],v[68:71],v[44:47],v[212:215]
M v[216:219],v[72:75],v[44:47],v[216:219]
AW a77,0
M v[220:223],v[76:79],v[44:47],v[220:223]
M v[224:227],v[80:83],v[44:47],v[224:227]
AW a78,0
)"
R"(M v[228:231],v[84:87],v[44:47],v[228:231]
M v[232:235],v[88:91],v[44:47],v[232:235]
AW a79,0
M v[236:239],v[92:95],v[44:47],v[236:239]
R v[44:47],v5 offset:51200
W vmcnt(0) lgkmcnt(0)
s_barrier
v_cmp_ne_u32 vcc,s88,v2
)"
R"(s_nop 4
s_cbranch_vccnz CK6_%=
CB6_%=:
M v[48:51],v[96:99],v[32:35],v[48:51]
T v[64:65],v16 offset:16384
SA m0,s46,0
M v[52:55],v[100:103],v[32:35],v[52:55]
T v[66:67],v16 offset:17408
)"
R"(AW a80,0
M v[56:59],v[104:107],v[32:35],v[56:59]
SM s41,0
AW a81,0
M v[60:63],v[108:111],v[32:35],v[60:63]
T v[68:69],v17 offset:16384
M v[128:131],v[112:115],v[32:35],v[128:131]
T v[70:71],v17 offset:17408
)"
R"(B v0,s[60:63],s41 offen offset:0 sc1 lds
M v[132:135],v[116:119],v[32:35],v[132:135]
AW a82,0
M v[136:139],v[120:123],v[32:35],v[136:139]
T v[72:73],v18 offset:16384
B v0,s[60:63],s41 offen offset:1024 sc1 lds
M v[140:143],v[124:127],v[32:35],v[140:143]
R v[32:35],v6 offset:0
)"
R"(T v[74:75],v18 offset:17408
M v[144:147],v[96:99],v[36:39],v[144:147]
B v0,s[60:63],s41 offen offset:2048 sc1 lds
AW a83,0
M v[148:151],v[100:103],v[36:39],v[148:151]
T v[76:77],v19 offset:16384
AW a84,0
M v[152:155],v[104:107],v[36:39],v[152:155]
)"
R"(T v[78:79],v19 offset:17408
B v0,s[60:63],s41 offen offset:3072 sc1 lds
M v[156:159],v[108:111],v[36:39],v[156:159]
AW a85,0
M v[160:163],v[112:115],v[36:39],v[160:163]
T v[80:81],v20 offset:16384
SA m0,s46,131072
M v[164:167],v[116:119],v[36:39],v[164:167]
)"
R"(T v[82:83],v20 offset:17408
AW a86,0
M v[168:171],v[120:123],v[36:39],v[168:171]
SM s41,0
AW a87,0
M v[172:175],v[124:127],v[36:39],v[172:175]
R v[36:39],v6 offset:2048
T v[84:85],v21 offset:16384
)"
R"(M v[176:179],v[96:99],v[40:43],v[176:179]
T v[86:87],v21 offset:17408
B v0,s[64:67],s41 offen offset:0 sc1 lds
M v[180:183],v[100:103],v[40:43],v[180:183]
AW a88,0
M v[184:187],v[104:107],v[40:43],v[184:187]
T v[88:89],v22 offset:16384
B v0,s[64:67],s41 offen offset:1024 sc1 lds
)"
R"(M v[188:191],v[108:111],v[40:43],v[188:191]
T v[90:91],v22 offset:17408
AW a89,0
M v[192:195],v[112:115],v[40:43],v[192:195]
B v0,s[64:67],s41 offen offset:2048 sc1 lds
AW a90,0
M v[196:199],v[116:119],v[40:43],v[196:199]
T v[92:93],v23 offset:16384
)"
R"(M v[200:203],v[120:123],v[40:43],v[200:203]
T v[94:95],v23 offset:17408
B v0,s[64:67],s41 offen offset:3072 sc1 lds
M v[204:207],v[124:127],v[40:43],v[204:207]
R v[40:43],v6 offset:16384
AW a91,0
M v[208:211],v[96:99],v[44:47],v[208:211]
SA m0,s46,16384
)"
R"(AW a92,0
M v[212:215],v[100:103],v[44:47],v[212:215]
M v[216:219],v[104:107],v[44:47],v[216:219]
SM s41,16384
AW a93,0
M v[220:223],v[108:111],v[44:47],v[220:223]
M v[224:227],v[112:115],v[44:47],v[224:227]
B v0,s[60:63],s41 offen offset:0 sc1 lds
)"
R"(AW a94,0
M v[228:231],v[116:119],v[44:47],v[228:231]
M v[232:235],v[120:123],v[44:47],v[232:235]
B v0,s[60:63],s41 offen offset:1024 sc1 lds
AW a95,0
M v[236:239],v[124:127],v[44:47],v[236:239]
R v[44:47],v6 offset:18432
W lgkmcnt(11)
)"
R"(M v[48:51],v[64:67],v[32:35],v[48:51]
T v[96:97],v16 offset:24576
B v0,s[60:63],s41 offen offset:2048 sc1 lds
M v[52:55],v[68:71],v[32:35],v[52:55]
T v[98:99],v16 offset:25600
AW a96,0
M v[56:59],v[72:75],v[32:35],v[56:59]
B v0,s[60:63],s41 offen offset:3072 sc1 lds
)"
R"(AW a97,0
M v[60:63],v[76:79],v[32:35],v[60:63]
T v[100:101],v17 offset:24576
W lgkmcnt(5)
M v[128:131],v[80:83],v[32:35],v[128:131]
T v[102:103],v17 offset:25600
SA m0,s46,147456
M v[132:135],v[84:87],v[32:35],v[132:135]
)"
R"(AW a98,0
M v[136:139],v[88:91],v[32:35],v[136:139]
T v[104:105],v18 offset:24576
SM s41,16384
M v[140:143],v[92:95],v[32:35],v[140:143]
R v[32:35],v7 offset:0
T v[106:107],v18 offset:25600
M v[144:147],v[64:67],v[36:39],v[144:147]
)"
R"(B v0,s[64:67],s41 offen offset:0 sc1 lds
AW a99,0
M v[148:151],v[68:71],v[36:39],v[148:151]
T v[108:109],v19 offset:24576
AW a100,0
M v[152:155],v[72:75],v[36:39],v[152:155]
T v[110:111],v19 offset:25600
B v0,s[64:67],s41 offen offset:1024 sc1 lds
)"
R"(M v[156:159],v[76:79],v[36:39],v[156:159]
AW a101,0
M v[160:163],v[80:83],v[36:39],v[160:163]
T v[112:113],v20 offset:24576
B v0,s[64:67],s41 offen offset:2048 sc1 lds
M v[164:167],v[84:87],v[36:39],v[164:167]
T v[114:115],v20 offset:25600
AW a102,0
)"
R"(M v[168:171],v[88:91],v[36:39],v[168:171]
B v0,s[64:67],s41 offen offset:3072 sc1 lds
AW a103,0
M v[172:175],v[92:95],v[36:39],v[172:175]
R v[36:39],v7 offset:2048
T v[116:117],v21 offset:24576
W lgkmcnt(14)
M v[176:179],v[64:67],v[40:43],v[176:179]
)"
R"(T v[118:119],v21 offset:25600
AW a104,0
M v[180:183],v[68:71],v[40:43],v[180:183]
GL a[192:195],v3,s[80:81] sc1
M v[184:187],v[72:75],v[40:43],v[184:187]
T v[120:121],v22 offset:24576
AW a105,0
M v[188:191],v[76:79],v[40:43],v[188:191]
)"
R"(T v[122:123],v22 offset:25600
SA s80,s80,0x1000
M v[192:195],v[80:83],v[40:43],v[192:195]
AW a106,0
M v[196:199],v[84:87],v[40:43],v[196:199]
T v[124:125],v23 offset:24576
s_addc_u32 s81,s81,0
M v[200:203],v[88:91],v[40:43],v[200:203]
)"
R"(T v[126:127],v23 offset:25600
AW a107,0
M v[204:207],v[92:95],v[40:43],v[204:207]
R v[40:43],v7 offset:16384
GL a[196:199],v3,s[80:81] sc1
W lgkmcnt(15)
M v[208:211],v[64:67],v[44:47],v[208:211]
AW a108,0
)"
R"(M v[212:215],v[68:71],v[44:47],v[212:215]
SA s80,s80,0x1000
M v[216:219],v[72:75],v[44:47],v[216:219]
AW a109,0
M v[220:223],v[76:79],v[44:47],v[220:223]
s_addc_u32 s81,s81,0
M v[224:227],v[80:83],v[44:47],v[224:227]
AW a110,0
)"
R"(M v[228:231],v[84:87],v[44:47],v[228:231]
GL a[200:203],v3,s[80:81] sc1
M v[232:235],v[88:91],v[44:47],v[232:235]
AW a111,0
M v[236:239],v[92:95],v[44:47],v[236:239]
R v[44:47],v7 offset:18432
W lgkmcnt(11)
M v[48:51],v[96:99],v[32:35],v[48:51]
)"
R"(SA s80,s80,0x1000
AW a112,0
M v[52:55],v[100:103],v[32:35],v[52:55]
s_addc_u32 s81,s81,0
M v[56:59],v[104:107],v[32:35],v[56:59]
AW a113,0
M v[60:63],v[108:111],v[32:35],v[60:63]
GL a[204:207],v3,s[80:81] sc1
)"
R"(W lgkmcnt(2)
M v[128:131],v[112:115],v[32:35],v[128:131]
SA s80,s80,0x1000
AW a114,0
M v[132:135],v[116:119],v[32:35],v[132:135]
M v[136:139],v[120:123],v[32:35],v[136:139]
s_addc_u32 s81,s81,0
AW a115,0
)"
R"(M v[140:143],v[124:127],v[32:35],v[140:143]
GL a[208:211],v3,s[80:81] sc1
M v[144:147],v[96:99],v[36:39],v[144:147]
AW a116,0
M v[148:151],v[100:103],v[36:39],v[148:151]
SA s80,s80,0x1000
M v[152:155],v[104:107],v[36:39],v[152:155]
s_addc_u32 s81,s81,0
)"
R"(AW a117,0
M v[156:159],v[108:111],v[36:39],v[156:159]
M v[160:163],v[112:115],v[36:39],v[160:163]
GL a[212:215],v3,s[80:81] sc1
AW a118,0
M v[164:167],v[116:119],v[36:39],v[164:167]
SA s80,s80,0x1000
M v[168:171],v[120:123],v[36:39],v[168:171]
)"
R"(AW a119,0
M v[172:175],v[124:127],v[36:39],v[172:175]
s_addc_u32 s81,s81,0
W lgkmcnt(1)
M v[176:179],v[96:99],v[40:43],v[176:179]
GL a[216:219],v3,s[80:81] sc1
AW a120,0
M v[180:183],v[100:103],v[40:43],v[180:183]
)"
R"(M v[184:187],v[104:107],v[40:43],v[184:187]
SA s80,s80,0x1000
AW a121,0
M v[188:191],v[108:111],v[40:43],v[188:191]
s_addc_u32 s81,s81,0
M v[192:195],v[112:115],v[40:43],v[192:195]
AW a122,0
M v[196:199],v[116:119],v[40:43],v[196:199]
)"
R"(GL a[220:223],v3,s[80:81] sc1
M v[200:203],v[120:123],v[40:43],v[200:203]
GL a[224:227],v3,s[78:79] offset:0
AW a123,0
M v[204:207],v[124:127],v[40:43],v[204:207]
W lgkmcnt(0)
M v[208:211],v[96:99],v[44:47],v[208:211]
GL a[228:231],v3,s[78:79] offset:1024
)"
R"(AW a124,0
M v[212:215],v[100:103],v[44:47],v[212:215]
GL a[232:235],v3,s[78:79] offset:2048
M v[216:219],v[104:107],v[44:47],v[216:219]
AW a125,0
M v[220:223],v[108:111],v[44:47],v[220:223]
GL a[236:239],v3,s[78:79] offset:3072
M v[224:227],v[112:115],v[44:47],v[224:227]
)"
R"(AW a126,0
M v[228:231],v[116:119],v[44:47],v[228:231]
M v[232:235],v[120:123],v[44:47],v[232:235]
AW a127,0
M v[236:239],v[124:127],v[44:47],v[236:239]
s_branch KE1_%=
CK6_%=:
s_mov_b64 s[90:91],%16
)"
R"(SA s90,s90,0x5000000
s_addc_u32 s91,s91,0
SM s89,60000
s_mov_b64 exec,-1
s_nop 1
PL7_%=:
global_load_dword v2,v1,s[90:91] sc1
W vmcnt(0)
)"
R"(v_cmp_ne_u32 vcc,s88,v2
s_sub_u32 s89,s89,1
s_cbranch_scc1 PD8_%=
s_cbranch_vccz PD8_%=
s_sleep 1
s_branch PL7_%=
PD8_%=:
s_mov_b64 exec,-1
)"
R"(s_nop 1
s_branch CB6_%=
KE1_%=:
KVJ_%=:
s_nop 7
s_nop 1
MU v48,v31,v48
MU v49,v31,v49
)"
R"(MU v50,v31,v50
MU v51,v31,v51
MU v52,v31,v52
MU v53,v31,v53
MU v54,v31,v54
MU v55,v31,v55
CV v8,v48,v49
CV v9,v50,v51
)"
R"(CV v10,v52,v53
CV v11,v54,v55
AW a128,v8
AW a129,v9
AW a130,v10
AW a131,v11
MU v56,v31,v56
MU v57,v31,v57
)"
R"(MU v58,v31,v58
MU v59,v31,v59
MU v60,v31,v60
MU v61,v31,v61
MU v62,v31,v62
MU v63,v31,v63
CV v12,v56,v57
CV v13,v58,v59
)"
R"(CV v14,v60,v61
CV v15,v62,v63
AW a132,v12
AW a133,v13
AW a134,v14
AW a135,v15
MU v128,v31,v128
MU v129,v31,v129
)"
R"(MU v130,v31,v130
MU v131,v31,v131
MU v132,v31,v132
MU v133,v31,v133
MU v134,v31,v134
MU v135,v31,v135
CV v8,v128,v129
CV v9,v130,v131
)"
R"(CV v10,v132,v133
CV v11,v134,v135
AW a136,v8
AW a137,v9
AW a138,v10
AW a139,v11
MU v136,v31,v136
MU v137,v31,v137
)"
R"(MU v138,v31,v138
MU v139,v31,v139
MU v140,v31,v140
MU v141,v31,v141
MU v142,v31,v142
MU v143,v31,v143
CV v12,v136,v137
CV v13,v138,v139
)"
R"(CV v14,v140,v141
CV v15,v142,v143
AW a140,v12
AW a141,v13
AW a142,v14
AW a143,v15
MU v144,v31,v144
MU v145,v31,v145
)"
R"(MU v146,v31,v146
MU v147,v31,v147
MU v148,v31,v148
MU v149,v31,v149
MU v150,v31,v150
MU v151,v31,v151
CV v8,v144,v145
CV v9,v146,v147
)"
R"(CV v10,v148,v149
CV v11,v150,v151
AW a144,v8
AW a145,v9
AW a146,v10
AW a147,v11
MU v152,v31,v152
MU v153,v31,v153
)"
R"(MU v154,v31,v154
MU v155,v31,v155
MU v156,v31,v156
MU v157,v31,v157
MU v158,v31,v158
MU v159,v31,v159
CV v12,v152,v153
CV v13,v154,v155
)"
R"(CV v14,v156,v157
CV v15,v158,v159
AW a148,v12
AW a149,v13
AW a150,v14
AW a151,v15
MU v160,v31,v160
MU v161,v31,v161
)"
R"(MU v162,v31,v162
MU v163,v31,v163
MU v164,v31,v164
MU v165,v31,v165
MU v166,v31,v166
MU v167,v31,v167
CV v8,v160,v161
CV v9,v162,v163
)"
R"(CV v10,v164,v165
CV v11,v166,v167
AW a152,v8
AW a153,v9
AW a154,v10
AW a155,v11
MU v168,v31,v168
MU v169,v31,v169
)"
R"(MU v170,v31,v170
MU v171,v31,v171
MU v172,v31,v172
MU v173,v31,v173
MU v174,v31,v174
MU v175,v31,v175
CV v12,v168,v169
CV v13,v170,v171
)"
R"(CV v14,v172,v173
CV v15,v174,v175
AW a156,v12
AW a157,v13
AW a158,v14
AW a159,v15
MU v176,v31,v176
MU v177,v31,v177
)"
R"(MU v178,v31,v178
MU v179,v31,v179
MU v180,v31,v180
MU v181,v31,v181
MU v182,v31,v182
MU v183,v31,v183
CV v8,v176,v177
CV v9,v178,v179
)"
R"(CV v10,v180,v181
CV v11,v182,v183
AW a160,v8
AW a161,v9
AW a162,v10
AW a163,v11
MU v184,v31,v184
MU v185,v31,v185
)"
R"(MU v186,v31,v186
MU v187,v31,v187
MU v188,v31,v188
MU v189,v31,v189
MU v190,v31,v190
MU v191,v31,v191
CV v12,v184,v185
CV v13,v186,v187
)"
R"(CV v14,v188,v189
CV v15,v190,v191
AW a164,v12
AW a165,v13
AW a166,v14
AW a167,v15
MU v192,v31,v192
MU v193,v31,v193
)"
R"(MU v194,v31,v194
MU v195,v31,v195
MU v196,v31,v196
MU v197,v31,v197
MU v198,v31,v198
MU v199,v31,v199
CV v8,v192,v193
CV v9,v194,v195
)"
R"(CV v10,v196,v197
CV v11,v198,v199
AW a168,v8
AW a169,v9
AW a170,v10
AW a171,v11
MU v200,v31,v200
MU v201,v31,v201
)"
R"(MU v202,v31,v202
MU v203,v31,v203
MU v204,v31,v204
MU v205,v31,v205
MU v206,v31,v206
MU v207,v31,v207
CV v12,v200,v201
CV v13,v202,v203
)"
R"(CV v14,v204,v205
CV v15,v206,v207
AW a172,v12
AW a173,v13
AW a174,v14
AW a175,v15
MU v208,v31,v208
MU v209,v31,v209
)"
R"(MU v210,v31,v210
MU v211,v31,v211
MU v212,v31,v212
MU v213,v31,v213
MU v214,v31,v214
MU v215,v31,v215
CV v8,v208,v209
CV v9,v210,v211
)"
R"(CV v10,v212,v213
CV v11,v214,v215
AW a176,v8
AW a177,v9
AW a178,v10
AW a179,v11
MU v216,v31,v216
MU v217,v31,v217
)"
R"(MU v218,v31,v218
MU v219,v31,v219
MU v220,v31,v220
MU v221,v31,v221
MU v222,v31,v222
MU v223,v31,v223
CV v12,v216,v217
CV v13,v218,v219
)"
R"(CV v14,v220,v221
CV v15,v222,v223
AW a180,v12
AW a181,v13
AW a182,v14
AW a183,v15
MU v224,v31,v224
MU v225,v31,v225
)"
R"(MU v226,v31,v226
MU v227,v31,v227
MU v228,v31,v228
MU v229,v31,v229
MU v230,v31,v230
MU v231,v31,v231
CV v8,v224,v225
CV v9,v226,v227
)"
R"(CV v10,v228,v229
CV v11,v230,v231
AW a184,v8
AW a185,v9
AW a186,v10
AW a187,v11
MU v232,v31,v232
MU v233,v31,v233
)"
R"(MU v234,v31,v234
MU v235,v31,v235
MU v236,v31,v236
MU v237,v31,v237
MU v238,v31,v238
MU v239,v31,v239
CV v12,v232,v233
CV v13,v234,v235
)"
R"(CV v14,v236,v237
CV v15,v238,v239
AW a188,v12
AW a189,v13
AW a190,v14
AW a191,v15
W lgkmcnt(0)
s_barrier
)"
R"(s_sub_u32 s58,15,%20
s_lshr_b32 s46,%17,1
s_lshl_b32 s41,%20,1
SA s41,s41,s46
s_lshl_b32 s42,s58,1
SA s42,s42,s46
s_lshl_b32 s44,%17,12
SA s44,s44,%18
)"
R"(SA s45,s44,98304
v_mov_b32 v6,%6
v_add_u32 v3,%18,%6
SM s43,32768
SA s54,s44,32768
SM s47,32768
SM s55,s45
SM m0,s54
)"
R"(s_nop 0
B v0,s[60:63],s43 offen offset:0 sc1 lds
B v0,s[60:63],s43 offen offset:1024 sc1 lds
B v0,s[60:63],s43 offen offset:2048 sc1 lds
B v0,s[60:63],s43 offen offset:3072 sc1 lds
SM m0,s55
s_nop 0
B v0,s[64:67],s47 offen offset:0 sc1 lds
)"
R"(B v0,s[64:67],s47 offen offset:1024 sc1 lds
B v0,s[64:67],s47 offen offset:2048 sc1 lds
B v0,s[64:67],s47 offen offset:3072 sc1 lds
SM s43,49152
SA s54,s44,49152
SM s47,49152
SA s55,s45,16384
SM s52,0
)"
R"(SM s53,0x8000
v_add_u32 v4,98304,v3
v_mov_b32 v1,v3
v_add_u32 v2,0x8000,v4
W vmcnt(8)
AR v88,a192
AR v89,a193
AR v90,a194
)"
R"(AR v91,a195
AR v92,a196
AR v93,a197
AR v94,a198
AR v95,a199
AR v96,a200
AR v97,a201
AR v98,a202
)"
R"(AR v99,a203
AR v100,a204
AR v101,a205
AR v102,a206
AR v103,a207
AR v104,a208
AR v105,a209
AR v106,a210
)"
R"(AR v107,a211
AR v108,a212
AR v109,a213
AR v110,a214
AR v111,a215
AR v112,a216
AR v113,a217
AR v114,a218
)"
R"(AR v115,a219
AR v116,a220
AR v117,a221
AR v118,a222
AR v119,a223
AD v88,v88,v92
AD v89,v89,v93
AD v90,v90,v94
)"
R"(AD v91,v91,v95
AD v96,v96,v100
AD v97,v97,v101
AD v98,v98,v102
AD v99,v99,v103
AD v104,v104,v108
AD v105,v105,v109
AD v106,v106,v110
)"
R"(AD v107,v107,v111
AD v112,v112,v116
AD v113,v113,v117
AD v114,v114,v118
AD v115,v115,v119
AD v88,v88,v96
AD v89,v89,v97
AD v90,v90,v98
)"
R"(AD v91,v91,v99
AD v104,v104,v112
AD v105,v105,v113
AD v106,v106,v114
AD v107,v107,v115
AD v88,v88,v104
AD v89,v89,v105
AD v90,v90,v106
)"
R"(AD v91,v91,v107
s_lshl_b32 s46,%17,10
SA s46,s46,65536
v_add_u32 v9,s46,v3
v_add_u32 v10,65536,v3
ds_write_b128 v9,v[88:91] offset:0
v_mov_b32 v7,0xff800000
v_lshrrev_b32 v8,4,%6
)"
R"(v_and_b32 v5,15,v8
v_lshrrev_b32 v11,4,v8
v_lshlrev_b32 v11,2,v11
v_sub_u32 v5,v5,v11
s_and_b32 s46,%17,1
s_lshl_b32 s46,s46,5
v_add_u32 v5,s46,v5
W lgkmcnt(0)
)"
R"(s_barrier
R v[92:95],v10 offset:0
R v[96:99],v10 offset:1024
R v[100:103],v10 offset:2048
R v[104:107],v10 offset:3072
v_cmp_gt_i32 vcc,0,v5
s_nop 1
v_cndmask_b32 v152,0,v7,vcc
)"
R"(v_cmp_gt_i32 vcc,1,v5
s_nop 1
v_cndmask_b32 v153,0,v7,vcc
v_cmp_gt_i32 vcc,2,v5
s_nop 1
v_cndmask_b32 v154,0,v7,vcc
v_cmp_gt_i32 vcc,3,v5
s_nop 1
)"
R"(v_cndmask_b32 v155,0,v7,vcc
v_cmp_gt_i32 vcc,16,v5
s_nop 1
v_cndmask_b32 v156,0,v7,vcc
v_cmp_gt_i32 vcc,17,v5
s_nop 1
v_cndmask_b32 v157,0,v7,vcc
v_cmp_gt_i32 vcc,18,v5
)"
R"(s_nop 1
v_cndmask_b32 v158,0,v7,vcc
v_cmp_gt_i32 vcc,19,v5
s_nop 1
v_cndmask_b32 v159,0,v7,vcc
v_cmp_gt_i32 vcc,32,v5
s_nop 1
v_cndmask_b32 v160,0,v7,vcc
)"
R"(v_cmp_gt_i32 vcc,33,v5
s_nop 1
v_cndmask_b32 v161,0,v7,vcc
v_cmp_gt_i32 vcc,34,v5
s_nop 1
v_cndmask_b32 v162,0,v7,vcc
v_cmp_gt_i32 vcc,35,v5
s_nop 1
)"
R"(v_cndmask_b32 v163,0,v7,vcc
v_cmp_gt_i32 vcc,48,v5
s_nop 1
v_cndmask_b32 v164,0,v7,vcc
v_cmp_gt_i32 vcc,49,v5
s_nop 1
v_cndmask_b32 v165,0,v7,vcc
v_cmp_gt_i32 vcc,50,v5
)"
R"(s_nop 1
v_cndmask_b32 v166,0,v7,vcc
v_cmp_gt_i32 vcc,51,v5
s_nop 1
v_cndmask_b32 v167,0,v7,vcc
v_cmp_gt_i32 vcc,-16,v5
s_nop 1
v_cndmask_b32 v168,0,v7,vcc
)"
R"(v_cmp_gt_i32 vcc,-15,v5
s_nop 1
v_cndmask_b32 v169,0,v7,vcc
v_cmp_gt_i32 vcc,-14,v5
s_nop 1
v_cndmask_b32 v170,0,v7,vcc
v_cmp_gt_i32 vcc,-13,v5
s_nop 1
)"
R"(v_cndmask_b32 v171,0,v7,vcc
v_cmp_gt_i32 vcc,0,v5
s_nop 1
v_cndmask_b32 v172,0,v7,vcc
v_cmp_gt_i32 vcc,1,v5
s_nop 1
v_cndmask_b32 v173,0,v7,vcc
v_cmp_gt_i32 vcc,2,v5
)"
R"(s_nop 1
v_cndmask_b32 v174,0,v7,vcc
v_cmp_gt_i32 vcc,3,v5
s_nop 1
v_cndmask_b32 v175,0,v7,vcc
v_cmp_gt_i32 vcc,16,v5
s_nop 1
v_cndmask_b32 v176,0,v7,vcc
)"
R"(v_cmp_gt_i32 vcc,17,v5
s_nop 1
v_cndmask_b32 v177,0,v7,vcc
v_cmp_gt_i32 vcc,18,v5
s_nop 1
v_cndmask_b32 v178,0,v7,vcc
v_cmp_gt_i32 vcc,19,v5
s_nop 1
)"
R"(v_cndmask_b32 v179,0,v7,vcc
v_cmp_gt_i32 vcc,32,v5
s_nop 1
v_cndmask_b32 v180,0,v7,vcc
v_cmp_gt_i32 vcc,33,v5
s_nop 1
v_cndmask_b32 v181,0,v7,vcc
v_cmp_gt_i32 vcc,34,v5
)"
R"(s_nop 1
v_cndmask_b32 v182,0,v7,vcc
v_cmp_gt_i32 vcc,35,v5
s_nop 1
v_cndmask_b32 v183,0,v7,vcc
v_mov_b32 v208,0
v_mov_b32 v209,0
v_mov_b32 v210,0
)"
R"(v_mov_b32 v211,0
v_mov_b32 v24,0
v_mov_b32 v25,0
v_mov_b32 v26,0
v_mov_b32 v27,0
SM s48,0xff800000
v_mov_b32 v212,0
v_mov_b32 v213,0
)"
R"(v_mov_b32 v214,0
v_mov_b32 v215,0
v_mov_b32 v28,0
v_mov_b32 v29,0
v_mov_b32 v30,0
v_mov_b32 v31,0
SM s49,0xff800000
v_mov_b32 v216,0
)"
R"(v_mov_b32 v217,0
v_mov_b32 v218,0
v_mov_b32 v219,0
v_mov_b32 v32,0
v_mov_b32 v33,0
v_mov_b32 v34,0
v_mov_b32 v35,0
SM s50,0xff800000
)"
R"(v_mov_b32 v220,0
v_mov_b32 v221,0
v_mov_b32 v222,0
v_mov_b32 v223,0
v_mov_b32 v36,0
v_mov_b32 v37,0
v_mov_b32 v38,0
v_mov_b32 v39,0
)"
R"(SM s51,0xff800000
v_mov_b32 v224,0x3f803f80
v_mov_b32 v225,0x3f803f80
v_mov_b32 v226,0x3f803f80
v_mov_b32 v227,0x3f803f80
v_add_u32 v229,0,v6
v_add_u32 v230,8192,v6
v_add_u32 v231,16384,v6
)"
R"(v_add_u32 v232,24576,v6
s_getpc_b64 s[68:69]
PC0_%=:
SA s68,s68,IN0_%=-PC0_%=
s_addc_u32 s69,s69,0
s_getpc_b64 s[70:71]
PC1_%=:
SA s70,s70,IN1_%=-PC1_%=
)"
R"(s_addc_u32 s71,s71,0
s_getpc_b64 s[72:73]
PC2_%=:
SA s72,s72,IN2_%=-PC2_%=
s_addc_u32 s73,s73,0
s_getpc_b64 s[74:75]
PC3_%=:
SA s74,s74,IN3_%=-PC3_%=
)"
R"(s_addc_u32 s75,s75,0
s_lshl_b32 s46,%20,7
s_lshl_b32 s56,%19,2
SA s46,s46,s56
s_lshl_b32 s46,s46,3
s_lshl_b32 s56,%17,1
SA s46,s46,s56
s_lshl_b32 s46,s46,10
)"
R"(s_mov_b64 s[78:79],%16
SA s78,s78,s46
s_addc_u32 s79,s79,0
SA s78,s78,0x3000000
s_addc_u32 s79,s79,0
s_lshl_b32 s46,s58,7
s_lshl_b32 s56,%19,2
SA s46,s46,s56
)"
R"(s_lshl_b32 s46,s46,3
s_lshl_b32 s56,%17,1
SA s46,s46,s56
s_lshl_b32 s46,s46,10
s_mov_b64 s[80:81],%16
SA s80,s80,s46
s_addc_u32 s81,s81,0
SA s80,s80,0x3000000
)"
R"(s_addc_u32 s81,s81,0
s_lshl_b32 s94,%20,1
SA s94,s94,3
s_cmp_eq_u32 %17,0
s_cselect_b32 s94,s94,-1
SM s40,0
W lgkmcnt(0)
CV v108,v92,v93
)"
R"(CV v110,v96,v97
CV v112,v100,v101
CV v114,v104,v105
CV v109,v94,v95
CV v111,v98,v99
CV v113,v102,v103
CV v115,v106,v107
M v[40:43],v[108:111],a[224:227],0
)"
R"(s_nop 0
M v[56:59],v[112:115],a[224:227],0
M v[44:47],v[108:111],a[228:231],0
M v[60:63],v[112:115],a[228:231],0
M v[48:51],v[108:111],a[232:235],0
M v[64:67],v[112:115],a[232:235],0
M v[52:55],v[108:111],a[236:239],0
M v[68:71],v[112:115],a[236:239],0
)"
R"(R v[88:91],v1 offset:0
R v[92:95],v1 offset:1024
R v[96:99],v1 offset:2048
R v[100:103],v1 offset:3072
R v[104:107],v1 offset:4096
R v[108:111],v1 offset:5120
R v[112:115],v1 offset:6144
R v[116:119],v1 offset:7168
)"
R"(R v[120:123],v1 offset:8192
R v[124:127],v1 offset:9216
R v[128:131],v1 offset:10240
R v[132:135],v1 offset:11264
R v[136:139],v1 offset:12288
R v[140:143],v1 offset:13312
R v[144:147],v1 offset:14336
R v[148:151],v1 offset:15360
)"
R"(s_lshl_b32 s46,%20,4
s_lshl_b32 s56,%17,2
SA s46,s46,s56
s_lshl_b32 s46,s46,4
s_lshr_b32 s56,%19,1
SA s46,s46,s56
s_lshl_b32 s46,s46,10
s_mov_b64 s[90:91],%16
)"
R"(SA s90,s90,s46
s_addc_u32 s91,s91,0
SA s90,s90,0x4500000
s_addc_u32 s91,s91,0
s_and_b32 s46,%19,1
s_lshl_b32 s46,s46,3
v_add_u32 v9,s46,v6
AD v56,0x358637bd,v56
)"
R"(v_rcp_f32 v56,v56
s_nop 0
MU v40,v40,v56
MU v41,v41,v56
MU v42,v42,v56
MU v43,v43,v56
CV v40,v40,v41
CV v41,v42,v43
)"
R"(global_store_dwordx2 v9,v[40:41],s[90:91] sc1
AD v60,0x358637bd,v60
v_rcp_f32 v60,v60
s_nop 0
MU v44,v44,v60
MU v45,v45,v60
MU v46,v46,v60
MU v47,v47,v60
)"
R"(CV v44,v44,v45
CV v45,v46,v47
SA s90,s90,0x4000
s_addc_u32 s91,s91,0
global_store_dwordx2 v9,v[44:45],s[90:91] sc1
AD v64,0x358637bd,v64
v_rcp_f32 v64,v64
s_nop 0
)"
R"(MU v48,v48,v64
MU v49,v49,v64
MU v50,v50,v64
MU v51,v51,v64
CV v48,v48,v49
CV v49,v50,v51
SA s90,s90,0x4000
s_addc_u32 s91,s91,0
)"
R"(global_store_dwordx2 v9,v[48:49],s[90:91] sc1
AD v68,0x358637bd,v68
v_rcp_f32 v68,v68
s_nop 0
MU v52,v52,v68
MU v53,v53,v68
MU v54,v54,v68
MU v55,v55,v68
)"
R"(CV v52,v52,v53
CV v53,v54,v55
SA s90,s90,0x4000
s_addc_u32 s91,s91,0
global_store_dwordx2 v9,v[52:53],s[90:91] sc1
v_mov_b32 v228,0x5f800000
s_mov_b64 s[84:85],0
s_cmp_eq_u32 s41,0
)"
R"(s_cbranch_scc1 T0MF_%=
W lgkmcnt(12)
M v[40:43],v[88:91],a[128:131],v[24:27]
M v[40:43],v[92:95],a[132:135],v[40:43]
M v[40:43],v[96:99],a[136:139],v[40:43]
M v[40:43],v[100:103],a[140:143],v[40:43]
W lgkmcnt(8)
M v[44:47],v[104:107],a[128:131],v[24:27]
)"
R"(M v[44:47],v[108:111],a[132:135],v[44:47]
M v[44:47],v[112:115],a[136:139],v[44:47]
M v[44:47],v[116:119],a[140:143],v[44:47]
W lgkmcnt(4)
M v[48:51],v[120:123],a[128:131],v[24:27]
M v[48:51],v[124:127],a[132:135],v[48:51]
M v[48:51],v[128:131],a[136:139],v[48:51]
M v[48:51],v[132:135],a[140:143],v[48:51]
)"
R"(W lgkmcnt(0)
M v[52:55],v[136:139],a[128:131],v[24:27]
M v[52:55],v[140:143],a[132:135],v[52:55]
M v[52:55],v[144:147],a[136:139],v[52:55]
M v[52:55],v[148:151],a[140:143],v[52:55]
W lgkmcnt(0)
s_nop 7
M v[56:59],v[88:91],a[144:147],v[28:31]
)"
R"(R a[192:195],v2 offset:0
MX v12,v40,v41,v42
MX v12,v12,v43,v44
M v[56:59],v[92:95],a[148:151],v[56:59]
R a[196:199],v2 offset:1024
MX v12,v12,v45,v46
MX v12,v12,v47,v48
M v[56:59],v[96:99],a[152:155],v[56:59]
)"
R"(R a[200:203],v2 offset:2048
MX v12,v12,v49,v50
MX v12,v12,v51,v52
M v[56:59],v[100:103],a[156:159],v[56:59]
R a[204:207],v2 offset:3072
MX v12,v12,v53,v54
v_max_f32 v12,v12,v55
M v[60:63],v[104:107],a[144:147],v[28:31]
)"
R"(R a[208:211],v2 offset:4096
v_mov_b32 v15,v12
s_nop 1
v_permlane32_swap_b32 v12,v15
M v[60:63],v[108:111],a[148:151],v[60:63]
R a[212:215],v2 offset:5120
v_max_f32 v12,v12,v15
v_mov_b32 v15,v12
)"
R"(M v[60:63],v[112:115],a[152:155],v[60:63]
R a[216:219],v2 offset:6144
v_permlane16_swap_b32 v12,v15
v_max_f32 v12,v12,v15
M v[60:63],v[116:119],a[156:159],v[60:63]
R a[220:223],v2 offset:7168
AD v12,0x42ac0000,v12
v_sub_f32 v24,0,v12
)"
R"(M v[64:67],v[120:123],a[144:147],v[28:31]
R a[224:227],v2 offset:8192
v_sub_f32 v25,0,v12
v_sub_f32 v26,0,v12
M v[64:67],v[124:127],a[148:151],v[64:67]
R a[228:231],v2 offset:9216
v_sub_f32 v27,0,v12
v_sub_f32 v40,v40,v12
)"
R"(M v[64:67],v[128:131],a[152:155],v[64:67]
R a[232:235],v2 offset:10240
v_sub_f32 v41,v41,v12
v_sub_f32 v42,v42,v12
M v[64:67],v[132:135],a[156:159],v[64:67]
R a[236:239],v2 offset:11264
v_sub_f32 v43,v43,v12
v_sub_f32 v44,v44,v12
)"
R"(M v[68:71],v[136:139],a[144:147],v[28:31]
R a[240:243],v2 offset:12288
v_sub_f32 v45,v45,v12
v_sub_f32 v46,v46,v12
M v[68:71],v[140:143],a[148:151],v[68:71]
R a[244:247],v2 offset:13312
v_sub_f32 v47,v47,v12
v_sub_f32 v48,v48,v12
)"
R"(M v[68:71],v[144:147],a[152:155],v[68:71]
R a[248:251],v2 offset:14336
v_sub_f32 v49,v49,v12
v_sub_f32 v50,v50,v12
M v[68:71],v[148:151],a[156:159],v[68:71]
R a[252:255],v2 offset:15360
v_sub_f32 v51,v51,v12
v_sub_f32 v52,v52,v12
)"
R"(v_sub_f32 v53,v53,v12
v_sub_f32 v54,v54,v12
v_sub_f32 v55,v55,v12
EX v40,v40
EX v41,v41
EX v42,v42
CV v72,v40,v41
EX v43,v43
)"
R"(EX v44,v44
CV v73,v42,v43
EX v45,v45
EX v46,v46
CV v74,v44,v45
EX v47,v47
EX v48,v48
CV v75,v46,v47
)"
R"(EX v49,v49
EX v50,v50
CV v76,v48,v49
EX v51,v51
EX v52,v52
CV v77,v50,v51
EX v53,v53
EX v54,v54
)"
R"(CV v78,v52,v53
EX v55,v55
s_nop 0
CV v79,v54,v55
M v[40:43],v[88:91],a[160:163],v[32:35]
MX v12,v56,v57,v58
MX v12,v12,v59,v60
MX v12,v12,v61,v62
)"
R"(W lgkmcnt(14)
M a[0:3],a[192:195],v[72:75],a[0:3]
MX v12,v12,v63,v64
MX v12,v12,v65,v66
MX v12,v12,v67,v68
M v[40:43],v[92:95],a[164:167],v[40:43]
MX v12,v12,v69,v70
v_max_f32 v12,v12,v71
)"
R"(v_mov_b32 v15,v12
M a[4:7],a[196:199],v[72:75],a[4:7]
s_nop 0
v_permlane32_swap_b32 v12,v15
v_max_f32 v12,v12,v15
M v[40:43],v[96:99],a[168:171],v[40:43]
SA s53,s53,0x4000
s_and_b32 s53,s53,0xc000
)"
R"(v_add_u32 v2,s53,v4
W lgkmcnt(12)
M a[8:11],a[200:203],v[72:75],a[8:11]
v_mov_b32 v15,v12
s_nop 1
v_permlane16_swap_b32 v12,v15
v_max_f32 v12,v12,v15
M v[40:43],v[100:103],a[172:175],v[40:43]
)"
R"(AD v12,0x42ac0000,v12
v_sub_f32 v28,0,v12
v_sub_f32 v29,0,v12
M a[12:15],a[204:207],v[72:75],a[12:15]
v_sub_f32 v30,0,v12
v_sub_f32 v31,0,v12
v_sub_f32 v56,v56,v12
M v[44:47],v[104:107],a[160:163],v[32:35]
)"
R"(v_sub_f32 v57,v57,v12
v_sub_f32 v58,v58,v12
W lgkmcnt(10)
M a[16:19],a[208:211],v[72:75],a[16:19]
v_sub_f32 v59,v59,v12
v_sub_f32 v60,v60,v12
M v[44:47],v[108:111],a[164:167],v[44:47]
v_sub_f32 v61,v61,v12
)"
R"(v_sub_f32 v62,v62,v12
M a[20:23],a[212:215],v[72:75],a[20:23]
v_sub_f32 v63,v63,v12
v_sub_f32 v64,v64,v12
M v[44:47],v[112:115],a[168:171],v[44:47]
v_sub_f32 v65,v65,v12
W lgkmcnt(8)
M a[24:27],a[216:219],v[72:75],a[24:27]
)"
R"(v_sub_f32 v66,v66,v12
v_sub_f32 v67,v67,v12
M v[44:47],v[116:119],a[172:175],v[44:47]
v_sub_f32 v68,v68,v12
v_sub_f32 v69,v69,v12
M a[28:31],a[220:223],v[72:75],a[28:31]
v_sub_f32 v70,v70,v12
v_sub_f32 v71,v71,v12
)"
R"(M v[48:51],v[120:123],a[160:163],v[32:35]
EX v56,v56
M v[208:211],v[224:227],v[72:75],v[208:211]
EX v57,v57
M v[48:51],v[124:127],a[164:167],v[48:51]
EX v58,v58
CV v80,v56,v57
W lgkmcnt(6)
)"
R"(M a[0:3],a[224:227],v[76:79],a[0:3]
EX v59,v59
M v[48:51],v[128:131],a[168:171],v[48:51]
SA s52,s52,0x4000
s_and_b32 s52,s52,0xc000
v_add_u32 v1,s52,v3
M a[4:7],a[228:231],v[76:79],a[4:7]
EX v60,v60
)"
R"(CV v81,v58,v59
M v[48:51],v[132:135],a[172:175],v[48:51]
EX v61,v61
W lgkmcnt(4)
M a[8:11],a[232:235],v[76:79],a[8:11]
EX v62,v62
CV v82,v60,v61
M v[52:55],v[136:139],a[160:163],v[32:35]
)"
R"(EX v63,v63
M a[12:15],a[236:239],v[76:79],a[12:15]
EX v64,v64
CV v83,v62,v63
M v[52:55],v[140:143],a[164:167],v[52:55]
EX v65,v65
W lgkmcnt(2)
M a[16:19],a[240:243],v[76:79],a[16:19]
)"
R"(EX v66,v66
CV v84,v64,v65
M v[52:55],v[144:147],a[168:171],v[52:55]
EX v67,v67
M a[20:23],a[244:247],v[76:79],a[20:23]
EX v68,v68
M v[52:55],v[148:151],a[172:175],v[52:55]
CV v85,v66,v67
)"
R"(EX v69,v69
W lgkmcnt(0)
M a[24:27],a[248:251],v[76:79],a[24:27]
EX v70,v70
M a[28:31],a[252:255],v[76:79],a[28:31]
CV v86,v68,v69
M v[208:211],v[224:227],v[76:79],v[208:211]
EX v71,v71
)"
R"(s_nop 0
CV v87,v70,v71
W vmcnt(8) lgkmcnt(0)
s_barrier
M v[56:59],v[88:91],a[176:179],v[36:39]
R v[88:91],v1 offset:0
MX v12,v40,v41,v42
MX v12,v12,v43,v44
)"
R"(M a[32:35],a[192:195],v[80:83],a[32:35]
SM m0,s54
s_nop 0
B v0,s[60:63],s43 offen offset:0 sc1 lds
M v[56:59],v[92:95],a[180:183],v[56:59]
R v[92:95],v1 offset:1024
MX v12,v12,v45,v46
MX v12,v12,v47,v48
)"
R"(M a[36:39],a[196:199],v[80:83],a[36:39]
MX v12,v12,v49,v50
MX v12,v12,v51,v52
MX v12,v12,v53,v54
M v[56:59],v[96:99],a[184:187],v[56:59]
R v[96:99],v1 offset:2048
v_max_f32 v12,v12,v55
v_mov_b32 v15,v12
)"
R"(M a[40:43],a[200:203],v[80:83],a[40:43]
s_nop 0
v_permlane32_swap_b32 v12,v15
v_max_f32 v12,v12,v15
v_mov_b32 v15,v12
M v[56:59],v[100:103],a[188:191],v[56:59]
R v[100:103],v1 offset:3072
v_permlane16_swap_b32 v12,v15
)"
R"(v_max_f32 v12,v12,v15
M a[44:47],a[204:207],v[80:83],a[44:47]
AD v12,0x42ac0000,v12
v_sub_f32 v32,0,v12
v_sub_f32 v33,0,v12
M v[60:63],v[104:107],a[176:179],v[36:39]
R v[104:107],v1 offset:4096
v_sub_f32 v34,0,v12
)"
R"(v_sub_f32 v35,0,v12
M a[48:51],a[208:211],v[80:83],a[48:51]
v_sub_f32 v40,v40,v12
v_sub_f32 v41,v41,v12
v_sub_f32 v42,v42,v12
M v[60:63],v[108:111],a[180:183],v[60:63]
R v[108:111],v1 offset:5120
v_sub_f32 v43,v43,v12
)"
R"(v_sub_f32 v44,v44,v12
M a[52:55],a[212:215],v[80:83],a[52:55]
v_sub_f32 v45,v45,v12
v_sub_f32 v46,v46,v12
v_sub_f32 v47,v47,v12
M v[60:63],v[112:115],a[184:187],v[60:63]
R v[112:115],v1 offset:6144
v_sub_f32 v48,v48,v12
)"
R"(v_sub_f32 v49,v49,v12
M a[56:59],a[216:219],v[80:83],a[56:59]
v_sub_f32 v50,v50,v12
v_sub_f32 v51,v51,v12
M v[60:63],v[116:119],a[188:191],v[60:63]
R v[116:119],v1 offset:7168
v_sub_f32 v52,v52,v12
v_sub_f32 v53,v53,v12
)"
R"(M a[60:63],a[220:223],v[80:83],a[60:63]
v_sub_f32 v54,v54,v12
v_sub_f32 v55,v55,v12
M v[64:67],v[120:123],a[176:179],v[36:39]
R v[120:123],v1 offset:8192
EX v40,v40
M v[212:215],v[224:227],v[80:83],v[212:215]
EX v41,v41
)"
R"(M v[64:67],v[124:127],a[180:183],v[64:67]
R v[124:127],v1 offset:9216
B v0,s[60:63],s43 offen offset:1024 sc1 lds
M a[32:35],a[224:227],v[84:87],a[32:35]
EX v42,v42
CV v72,v40,v41
M v[64:67],v[128:131],a[184:187],v[64:67]
R v[128:131],v1 offset:10240
)"
R"(EX v43,v43
M a[36:39],a[228:231],v[84:87],a[36:39]
EX v44,v44
CV v73,v42,v43
M v[64:67],v[132:135],a[188:191],v[64:67]
R v[132:135],v1 offset:11264
EX v45,v45
M a[40:43],a[232:235],v[84:87],a[40:43]
)"
R"(EX v46,v46
CV v74,v44,v45
M v[68:71],v[136:139],a[176:179],v[36:39]
R v[136:139],v1 offset:12288
EX v47,v47
M a[44:47],a[236:239],v[84:87],a[44:47]
EX v48,v48
CV v75,v46,v47
)"
R"(M v[68:71],v[140:143],a[180:183],v[68:71]
R v[140:143],v1 offset:13312
EX v49,v49
M a[48:51],a[240:243],v[84:87],a[48:51]
EX v50,v50
CV v76,v48,v49
M v[68:71],v[144:147],a[184:187],v[68:71]
R v[144:147],v1 offset:14336
)"
R"(EX v51,v51
M a[52:55],a[244:247],v[84:87],a[52:55]
EX v52,v52
M v[68:71],v[148:151],a[188:191],v[68:71]
R v[148:151],v1 offset:15360
SA s40,s40,1
CV v77,v50,v51
M a[56:59],a[248:251],v[84:87],a[56:59]
)"
R"(EX v53,v53
M a[60:63],a[252:255],v[84:87],a[60:63]
EX v54,v54
CV v78,v52,v53
M v[212:215],v[224:227],v[84:87],v[212:215]
EX v55,v55
s_nop 0
CV v79,v54,v55
)"
R"(s_nop 7
MX v12,v56,v57,v58
MX v12,v12,v59,v60
MX v12,v12,v61,v62
MX v12,v12,v63,v64
MX v12,v12,v65,v66
MX v12,v12,v67,v68
MX v12,v12,v69,v70
)"
R"(v_max_f32 v12,v12,v71
v_mov_b32 v15,v12
s_nop 1
v_permlane32_swap_b32 v12,v15
v_max_f32 v12,v12,v15
v_mov_b32 v15,v12
s_nop 1
v_permlane16_swap_b32 v12,v15
)"
R"(v_max_f32 v12,v12,v15
AD v12,0x42ac0000,v12
v_sub_f32 v36,0,v12
v_sub_f32 v37,0,v12
v_sub_f32 v38,0,v12
v_sub_f32 v39,0,v12
v_sub_f32 v56,v56,v12
v_sub_f32 v57,v57,v12
)"
R"(v_sub_f32 v58,v58,v12
v_sub_f32 v59,v59,v12
v_sub_f32 v60,v60,v12
v_sub_f32 v61,v61,v12
v_sub_f32 v62,v62,v12
v_sub_f32 v63,v63,v12
v_sub_f32 v64,v64,v12
v_sub_f32 v65,v65,v12
)"
R"(v_sub_f32 v66,v66,v12
v_sub_f32 v67,v67,v12
v_sub_f32 v68,v68,v12
v_sub_f32 v69,v69,v12
v_sub_f32 v70,v70,v12
v_sub_f32 v71,v71,v12
s_cmp_ge_u32 s40,s41
s_cbranch_scc1 DMF_%=
)"
R"(DLBF_%=:
W lgkmcnt(14)
M v[40:43],v[88:91],a[128:131],v[24:27]
EX v56,v56
M a[64:67],a[192:195],v[72:75],a[64:67]
B v0,s[60:63],s43 offen offset:2048 sc1 lds
EX v57,v57
M v[40:43],v[92:95],a[132:135],v[40:43]
)"
R"(EX v58,v58
M a[68:71],a[196:199],v[72:75],a[68:71]
CV v80,v56,v57
W lgkmcnt(12)
M v[40:43],v[96:99],a[136:139],v[40:43]
M a[72:75],a[200:203],v[72:75],a[72:75]
EX v59,v59
M v[40:43],v[100:103],a[140:143],v[40:43]
)"
R"(EX v60,v60
M a[76:79],a[204:207],v[72:75],a[76:79]
W lgkmcnt(10)
M v[44:47],v[104:107],a[128:131],v[24:27]
CV v81,v58,v59
M a[80:83],a[208:211],v[72:75],a[80:83]
EX v61,v61
M v[44:47],v[108:111],a[132:135],v[44:47]
)"
R"(M a[84:87],a[212:215],v[72:75],a[84:87]
EX v62,v62
W lgkmcnt(8)
M v[44:47],v[112:115],a[136:139],v[44:47]
M a[88:91],a[216:219],v[72:75],a[88:91]
M v[44:47],v[116:119],a[140:143],v[44:47]
CV v82,v60,v61
M a[92:95],a[220:223],v[72:75],a[92:95]
)"
R"(EX v63,v63
W lgkmcnt(6)
M v[48:51],v[120:123],a[128:131],v[24:27]
EX v64,v64
M v[216:219],v[224:227],v[72:75],v[216:219]
M v[48:51],v[124:127],a[132:135],v[48:51]
B v0,s[60:63],s43 offen offset:3072 sc1 lds
CV v83,v62,v63
)"
R"(M a[64:67],a[224:227],v[76:79],a[64:67]
EX v65,v65
W lgkmcnt(4)
M v[48:51],v[128:131],a[136:139],v[48:51]
M a[68:71],a[228:231],v[76:79],a[68:71]
EX v66,v66
M v[48:51],v[132:135],a[140:143],v[48:51]
CV v84,v64,v65
)"
R"(M a[72:75],a[232:235],v[76:79],a[72:75]
EX v67,v67
W lgkmcnt(2)
M v[52:55],v[136:139],a[128:131],v[24:27]
M a[76:79],a[236:239],v[76:79],a[76:79]
EX v68,v68
M v[52:55],v[140:143],a[132:135],v[52:55]
SA s43,s43,0x4000
)"
R"(s_and_b32 s46,s43,0xc000
SA s54,s44,s46
M a[80:83],a[240:243],v[76:79],a[80:83]
CV v85,v66,v67
W lgkmcnt(0)
M v[52:55],v[144:147],a[136:139],v[52:55]
EX v69,v69
M a[84:87],a[244:247],v[76:79],a[84:87]
)"
R"(EX v70,v70
M v[52:55],v[148:151],a[140:143],v[52:55]
M a[88:91],a[248:251],v[76:79],a[88:91]
CV v86,v68,v69
M a[92:95],a[252:255],v[76:79],a[92:95]
EX v71,v71
M v[216:219],v[224:227],v[76:79],v[216:219]
CV v87,v70,v71
)"
R"(M v[56:59],v[88:91],a[144:147],v[28:31]
EX v40,v40
M a[96:99],a[192:195],v[80:83],a[96:99]
R a[192:195],v2 offset:0
SM m0,s55
s_nop 0
B v0,s[64:67],s47 offen offset:0 sc1 lds
M v[56:59],v[92:95],a[148:151],v[56:59]
)"
R"(EX v41,v41
M a[100:103],a[196:199],v[80:83],a[100:103]
R a[196:199],v2 offset:1024
EX v42,v42
M v[56:59],v[96:99],a[152:155],v[56:59]
CV v72,v40,v41
M a[104:107],a[200:203],v[80:83],a[104:107]
R a[200:203],v2 offset:2048
)"
R"(EX v43,v43
M v[56:59],v[100:103],a[156:159],v[56:59]
EX v44,v44
M a[108:111],a[204:207],v[80:83],a[108:111]
R a[204:207],v2 offset:3072
M v[60:63],v[104:107],a[144:147],v[28:31]
CV v73,v42,v43
M a[112:115],a[208:211],v[80:83],a[112:115]
)"
R"(R a[208:211],v2 offset:4096
EX v45,v45
M v[60:63],v[108:111],a[148:151],v[60:63]
M a[116:119],a[212:215],v[80:83],a[116:119]
R a[212:215],v2 offset:5120
EX v46,v46
M v[60:63],v[112:115],a[152:155],v[60:63]
M a[120:123],a[216:219],v[80:83],a[120:123]
)"
R"(R a[216:219],v2 offset:6144
M v[60:63],v[116:119],a[156:159],v[60:63]
CV v74,v44,v45
M a[124:127],a[220:223],v[80:83],a[124:127]
R a[220:223],v2 offset:7168
EX v47,v47
M v[64:67],v[120:123],a[144:147],v[28:31]
EX v48,v48
)"
R"(M v[220:223],v[224:227],v[80:83],v[220:223]
M v[64:67],v[124:127],a[148:151],v[64:67]
B v0,s[64:67],s47 offen offset:1024 sc1 lds
CV v75,v46,v47
M a[96:99],a[224:227],v[84:87],a[96:99]
R a[224:227],v2 offset:8192
EX v49,v49
M v[64:67],v[128:131],a[152:155],v[64:67]
)"
R"(M a[100:103],a[228:231],v[84:87],a[100:103]
R a[228:231],v2 offset:9216
EX v50,v50
M v[64:67],v[132:135],a[156:159],v[64:67]
CV v76,v48,v49
M a[104:107],a[232:235],v[84:87],a[104:107]
R a[232:235],v2 offset:10240
EX v51,v51
)"
R"(M v[68:71],v[136:139],a[144:147],v[28:31]
M a[108:111],a[236:239],v[84:87],a[108:111]
R a[236:239],v2 offset:11264
EX v52,v52
M v[68:71],v[140:143],a[148:151],v[68:71]
CV v77,v50,v51
M a[112:115],a[240:243],v[84:87],a[112:115]
R a[240:243],v2 offset:12288
)"
R"(M v[68:71],v[144:147],a[152:155],v[68:71]
EX v53,v53
M a[116:119],a[244:247],v[84:87],a[116:119]
R a[244:247],v2 offset:13312
EX v54,v54
M v[68:71],v[148:151],a[156:159],v[68:71]
M a[120:123],a[248:251],v[84:87],a[120:123]
R a[248:251],v2 offset:14336
)"
R"(CV v78,v52,v53
M a[124:127],a[252:255],v[84:87],a[124:127]
R a[252:255],v2 offset:15360
EX v55,v55
M v[220:223],v[224:227],v[84:87],v[220:223]
CV v79,v54,v55
M v[40:43],v[88:91],a[160:163],v[32:35]
EX v56,v56
)"
R"(W lgkmcnt(14)
M a[0:3],a[192:195],v[72:75],a[0:3]
B v0,s[64:67],s47 offen offset:2048 sc1 lds
EX v57,v57
M v[40:43],v[92:95],a[164:167],v[40:43]
EX v58,v58
M a[4:7],a[196:199],v[72:75],a[4:7]
CV v80,v56,v57
)"
R"(M v[40:43],v[96:99],a[168:171],v[40:43]
SA s53,s53,0x4000
s_and_b32 s53,s53,0xc000
v_add_u32 v2,s53,v4
W lgkmcnt(12)
M a[8:11],a[200:203],v[72:75],a[8:11]
EX v59,v59
M v[40:43],v[100:103],a[172:175],v[40:43]
)"
R"(EX v60,v60
M a[12:15],a[204:207],v[72:75],a[12:15]
M v[44:47],v[104:107],a[160:163],v[32:35]
CV v81,v58,v59
W lgkmcnt(10)
M a[16:19],a[208:211],v[72:75],a[16:19]
EX v61,v61
M v[44:47],v[108:111],a[164:167],v[44:47]
)"
R"(M a[20:23],a[212:215],v[72:75],a[20:23]
EX v62,v62
M v[44:47],v[112:115],a[168:171],v[44:47]
W lgkmcnt(8)
M a[24:27],a[216:219],v[72:75],a[24:27]
M v[44:47],v[116:119],a[172:175],v[44:47]
CV v82,v60,v61
M a[28:31],a[220:223],v[72:75],a[28:31]
)"
R"(EX v63,v63
M v[48:51],v[120:123],a[160:163],v[32:35]
EX v64,v64
M v[208:211],v[224:227],v[72:75],v[208:211]
M v[48:51],v[124:127],a[164:167],v[48:51]
B v0,s[64:67],s47 offen offset:3072 sc1 lds
CV v83,v62,v63
W lgkmcnt(6)
)"
R"(M a[0:3],a[224:227],v[76:79],a[0:3]
EX v65,v65
M v[48:51],v[128:131],a[168:171],v[48:51]
SA s52,s52,0x4000
s_and_b32 s52,s52,0xc000
v_add_u32 v1,s52,v3
M a[4:7],a[228:231],v[76:79],a[4:7]
EX v66,v66
)"
R"(M v[48:51],v[132:135],a[172:175],v[48:51]
CV v84,v64,v65
W lgkmcnt(4)
M a[8:11],a[232:235],v[76:79],a[8:11]
EX v67,v67
M v[52:55],v[136:139],a[160:163],v[32:35]
M a[12:15],a[236:239],v[76:79],a[12:15]
EX v68,v68
)"
R"(M v[52:55],v[140:143],a[164:167],v[52:55]
CV v85,v66,v67
W lgkmcnt(2)
M a[16:19],a[240:243],v[76:79],a[16:19]
M v[52:55],v[144:147],a[168:171],v[52:55]
SA s47,s47,0x4000
s_xor_b32 s58,s47,0x8000
s_and_b32 s58,s58,0xc000
)"
R"(SA s55,s45,s58
M a[20:23],a[244:247],v[76:79],a[20:23]
EX v69,v69
M v[52:55],v[148:151],a[172:175],v[52:55]
EX v70,v70
W lgkmcnt(0)
M a[24:27],a[248:251],v[76:79],a[24:27]
CV v86,v68,v69
)"
R"(M a[28:31],a[252:255],v[76:79],a[28:31]
EX v71,v71
M v[208:211],v[224:227],v[76:79],v[208:211]
CV v87,v70,v71
W vmcnt(8) lgkmcnt(0)
s_barrier
M v[56:59],v[88:91],a[176:179],v[36:39]
R v[88:91],v1 offset:0
)"
R"(EX v40,v40
M a[32:35],a[192:195],v[80:83],a[32:35]
SM m0,s54
s_nop 0
B v0,s[60:63],s43 offen offset:0 sc1 lds
M v[56:59],v[92:95],a[180:183],v[56:59]
R v[92:95],v1 offset:1024
EX v41,v41
)"
R"(M a[36:39],a[196:199],v[80:83],a[36:39]
EX v42,v42
CV v72,v40,v41
M v[56:59],v[96:99],a[184:187],v[56:59]
R v[96:99],v1 offset:2048
M a[40:43],a[200:203],v[80:83],a[40:43]
EX v43,v43
M v[56:59],v[100:103],a[188:191],v[56:59]
)"
R"(R v[100:103],v1 offset:3072
EX v44,v44
M a[44:47],a[204:207],v[80:83],a[44:47]
M v[60:63],v[104:107],a[176:179],v[36:39]
R v[104:107],v1 offset:4096
CV v73,v42,v43
M a[48:51],a[208:211],v[80:83],a[48:51]
EX v45,v45
)"
R"(M v[60:63],v[108:111],a[180:183],v[60:63]
R v[108:111],v1 offset:5120
M a[52:55],a[212:215],v[80:83],a[52:55]
EX v46,v46
M v[60:63],v[112:115],a[184:187],v[60:63]
R v[112:115],v1 offset:6144
M a[56:59],a[216:219],v[80:83],a[56:59]
M v[60:63],v[116:119],a[188:191],v[60:63]
)"
R"(R v[116:119],v1 offset:7168
CV v74,v44,v45
M a[60:63],a[220:223],v[80:83],a[60:63]
EX v47,v47
M v[64:67],v[120:123],a[176:179],v[36:39]
R v[120:123],v1 offset:8192
EX v48,v48
M v[212:215],v[224:227],v[80:83],v[212:215]
)"
R"(M v[64:67],v[124:127],a[180:183],v[64:67]
R v[124:127],v1 offset:9216
B v0,s[60:63],s43 offen offset:1024 sc1 lds
CV v75,v46,v47
M a[32:35],a[224:227],v[84:87],a[32:35]
EX v49,v49
M v[64:67],v[128:131],a[184:187],v[64:67]
R v[128:131],v1 offset:10240
)"
R"(M a[36:39],a[228:231],v[84:87],a[36:39]
EX v50,v50
M v[64:67],v[132:135],a[188:191],v[64:67]
R v[132:135],v1 offset:11264
CV v76,v48,v49
M a[40:43],a[232:235],v[84:87],a[40:43]
EX v51,v51
M v[68:71],v[136:139],a[176:179],v[36:39]
)"
R"(R v[136:139],v1 offset:12288
M a[44:47],a[236:239],v[84:87],a[44:47]
EX v52,v52
M v[68:71],v[140:143],a[180:183],v[68:71]
R v[140:143],v1 offset:13312
CV v77,v50,v51
M a[48:51],a[240:243],v[84:87],a[48:51]
M v[68:71],v[144:147],a[184:187],v[68:71]
)"
R"(R v[144:147],v1 offset:14336
EX v53,v53
M a[52:55],a[244:247],v[84:87],a[52:55]
EX v54,v54
M v[68:71],v[148:151],a[188:191],v[68:71]
R v[148:151],v1 offset:15360
SA s40,s40,1
M a[56:59],a[248:251],v[84:87],a[56:59]
)"
R"(CV v78,v52,v53
M a[60:63],a[252:255],v[84:87],a[60:63]
EX v55,v55
M v[212:215],v[224:227],v[84:87],v[212:215]
CV v79,v54,v55
s_cmp_lt_u32 s40,s41
s_cbranch_scc1 DLBF_%=
DMF_%=:
)"
R"(W lgkmcnt(14)
M v[40:43],v[88:91],a[128:131],v[24:27]
EX v56,v56
M a[64:67],a[192:195],v[72:75],a[64:67]
B v0,s[60:63],s43 offen offset:2048 sc1 lds
EX v57,v57
M v[40:43],v[92:95],a[132:135],v[40:43]
EX v58,v58
)"
R"(M a[68:71],a[196:199],v[72:75],a[68:71]
CV v80,v56,v57
W lgkmcnt(12)
M v[40:43],v[96:99],a[136:139],v[40:43]
M a[72:75],a[200:203],v[72:75],a[72:75]
EX v59,v59
M v[40:43],v[100:103],a[140:143],v[40:43]
EX v60,v60
)"
R"(M a[76:79],a[204:207],v[72:75],a[76:79]
W lgkmcnt(10)
M v[44:47],v[104:107],a[128:131],v[24:27]
CV v81,v58,v59
M a[80:83],a[208:211],v[72:75],a[80:83]
EX v61,v61
M v[44:47],v[108:111],a[132:135],v[44:47]
M a[84:87],a[212:215],v[72:75],a[84:87]
)"
R"(EX v62,v62
W lgkmcnt(8)
M v[44:47],v[112:115],a[136:139],v[44:47]
M a[88:91],a[216:219],v[72:75],a[88:91]
M v[44:47],v[116:119],a[140:143],v[44:47]
CV v82,v60,v61
M a[92:95],a[220:223],v[72:75],a[92:95]
EX v63,v63
)"
R"(W lgkmcnt(6)
M v[48:51],v[120:123],a[128:131],v[24:27]
EX v64,v64
M v[216:219],v[224:227],v[72:75],v[216:219]
M v[48:51],v[124:127],a[132:135],v[48:51]
B v0,s[60:63],s43 offen offset:3072 sc1 lds
CV v83,v62,v63
M a[64:67],a[224:227],v[76:79],a[64:67]
)"
R"(EX v65,v65
W lgkmcnt(4)
M v[48:51],v[128:131],a[136:139],v[48:51]
M a[68:71],a[228:231],v[76:79],a[68:71]
EX v66,v66
M v[48:51],v[132:135],a[140:143],v[48:51]
CV v84,v64,v65
M a[72:75],a[232:235],v[76:79],a[72:75]
)"
R"(EX v67,v67
W lgkmcnt(2)
M v[52:55],v[136:139],a[128:131],v[24:27]
M a[76:79],a[236:239],v[76:79],a[76:79]
EX v68,v68
M v[52:55],v[140:143],a[132:135],v[52:55]
SA s43,s43,0x4000
s_and_b32 s46,s43,0xc000
)"
R"(SA s54,s44,s46
M a[80:83],a[240:243],v[76:79],a[80:83]
CV v85,v66,v67
W lgkmcnt(0)
M v[52:55],v[144:147],a[136:139],v[52:55]
EX v69,v69
M a[84:87],a[244:247],v[76:79],a[84:87]
EX v70,v70
)"
R"(M v[52:55],v[148:151],a[140:143],v[52:55]
M a[88:91],a[248:251],v[76:79],a[88:91]
CV v86,v68,v69
M a[92:95],a[252:255],v[76:79],a[92:95]
EX v71,v71
M v[216:219],v[224:227],v[76:79],v[216:219]
CV v87,v70,v71
M v[56:59],v[88:91],a[144:147],v[28:31]
)"
R"(AD v40,v40,v152
AD v41,v41,v153
AD v42,v42,v154
M a[96:99],a[192:195],v[80:83],a[96:99]
R a[192:195],v2 offset:0
SM m0,s55
s_nop 0
B v0,s[64:67],s47 offen offset:0 sc1 lds
)"
R"(M v[56:59],v[92:95],a[148:151],v[56:59]
AD v43,v43,v155
AD v44,v44,v156
AD v45,v45,v157
M a[100:103],a[196:199],v[80:83],a[100:103]
R a[196:199],v2 offset:1024
AD v46,v46,v158
AD v47,v47,v159
)"
R"(M v[56:59],v[96:99],a[152:155],v[56:59]
AD v48,v48,v160
AD v49,v49,v161
M a[104:107],a[200:203],v[80:83],a[104:107]
R a[200:203],v2 offset:2048
AD v50,v50,v162
AD v51,v51,v163
M v[56:59],v[100:103],a[156:159],v[56:59]
)"
R"(AD v52,v52,v164
AD v53,v53,v165
M a[108:111],a[204:207],v[80:83],a[108:111]
R a[204:207],v2 offset:3072
AD v54,v54,v166
M v[60:63],v[104:107],a[144:147],v[28:31]
AD v55,v55,v167
M a[112:115],a[208:211],v[80:83],a[112:115]
)"
R"(R a[208:211],v2 offset:4096
EX v40,v40
M v[60:63],v[108:111],a[148:151],v[60:63]
EX v41,v41
M a[116:119],a[212:215],v[80:83],a[116:119]
R a[212:215],v2 offset:5120
EX v42,v42
M v[60:63],v[112:115],a[152:155],v[60:63]
)"
R"(M a[120:123],a[216:219],v[80:83],a[120:123]
R a[216:219],v2 offset:6144
CV v72,v40,v41
M v[60:63],v[116:119],a[156:159],v[60:63]
EX v43,v43
M a[124:127],a[220:223],v[80:83],a[124:127]
R a[220:223],v2 offset:7168
EX v44,v44
)"
R"(M v[64:67],v[120:123],a[144:147],v[28:31]
CV v73,v42,v43
M v[220:223],v[224:227],v[80:83],v[220:223]
EX v45,v45
M v[64:67],v[124:127],a[148:151],v[64:67]
B v0,s[64:67],s47 offen offset:1024 sc1 lds
EX v46,v46
M a[96:99],a[224:227],v[84:87],a[96:99]
)"
R"(R a[224:227],v2 offset:8192
CV v74,v44,v45
M v[64:67],v[128:131],a[152:155],v[64:67]
EX v47,v47
M a[100:103],a[228:231],v[84:87],a[100:103]
R a[228:231],v2 offset:9216
EX v48,v48
M v[64:67],v[132:135],a[156:159],v[64:67]
)"
R"(CV v75,v46,v47
M a[104:107],a[232:235],v[84:87],a[104:107]
R a[232:235],v2 offset:10240
EX v49,v49
M v[68:71],v[136:139],a[144:147],v[28:31]
EX v50,v50
M a[108:111],a[236:239],v[84:87],a[108:111]
R a[236:239],v2 offset:11264
)"
R"(CV v76,v48,v49
M v[68:71],v[140:143],a[148:151],v[68:71]
EX v51,v51
M a[112:115],a[240:243],v[84:87],a[112:115]
R a[240:243],v2 offset:12288
EX v52,v52
M v[68:71],v[144:147],a[152:155],v[68:71]
CV v77,v50,v51
)"
R"(M a[116:119],a[244:247],v[84:87],a[116:119]
R a[244:247],v2 offset:13312
EX v53,v53
M v[68:71],v[148:151],a[156:159],v[68:71]
EX v54,v54
M a[120:123],a[248:251],v[84:87],a[120:123]
R a[248:251],v2 offset:14336
CV v78,v52,v53
)"
R"(M a[124:127],a[252:255],v[84:87],a[124:127]
R a[252:255],v2 offset:15360
EX v55,v55
M v[220:223],v[224:227],v[84:87],v[220:223]
CV v79,v54,v55
M v[40:43],v[88:91],a[160:163],v[32:35]
AD v56,v56,v168
AD v57,v57,v169
)"
R"(AD v58,v58,v170
W lgkmcnt(14)
M a[0:3],a[192:195],v[72:75],a[0:3]
B v0,s[64:67],s47 offen offset:2048 sc1 lds
AD v59,v59,v171
AD v60,v60,v172
M v[40:43],v[92:95],a[164:167],v[40:43]
AD v61,v61,v173
)"
R"(AD v62,v62,v174
M a[4:7],a[196:199],v[72:75],a[4:7]
AD v63,v63,v175
AD v64,v64,v176
M v[40:43],v[96:99],a[168:171],v[40:43]
SA s53,s53,0x4000
s_and_b32 s53,s53,0xc000
v_add_u32 v2,s53,v4
)"
R"(W lgkmcnt(12)
M a[8:11],a[200:203],v[72:75],a[8:11]
AD v65,v65,v177
AD v66,v66,v178
AD v67,v67,v179
M v[40:43],v[100:103],a[172:175],v[40:43]
AD v68,v68,v180
AD v69,v69,v181
)"
R"(M a[12:15],a[204:207],v[72:75],a[12:15]
AD v70,v70,v182
M v[44:47],v[104:107],a[160:163],v[32:35]
AD v71,v71,v183
W lgkmcnt(10)
M a[16:19],a[208:211],v[72:75],a[16:19]
EX v56,v56
M v[44:47],v[108:111],a[164:167],v[44:47]
)"
R"(EX v57,v57
M a[20:23],a[212:215],v[72:75],a[20:23]
EX v58,v58
M v[44:47],v[112:115],a[168:171],v[44:47]
W lgkmcnt(8)
M a[24:27],a[216:219],v[72:75],a[24:27]
CV v80,v56,v57
M v[44:47],v[116:119],a[172:175],v[44:47]
)"
R"(EX v59,v59
M a[28:31],a[220:223],v[72:75],a[28:31]
EX v60,v60
M v[48:51],v[120:123],a[160:163],v[32:35]
CV v81,v58,v59
M v[208:211],v[224:227],v[72:75],v[208:211]
EX v61,v61
M v[48:51],v[124:127],a[164:167],v[48:51]
)"
R"(B v0,s[64:67],s47 offen offset:3072 sc1 lds
EX v62,v62
W lgkmcnt(6)
M a[0:3],a[224:227],v[76:79],a[0:3]
CV v82,v60,v61
M v[48:51],v[128:131],a[168:171],v[48:51]
SA s52,s52,0x4000
s_and_b32 s52,s52,0xc000
)"
R"(v_add_u32 v1,s52,v3
M a[4:7],a[228:231],v[76:79],a[4:7]
EX v63,v63
M v[48:51],v[132:135],a[172:175],v[48:51]
EX v64,v64
CV v83,v62,v63
W lgkmcnt(4)
M a[8:11],a[232:235],v[76:79],a[8:11]
)"
R"(EX v65,v65
M v[52:55],v[136:139],a[160:163],v[32:35]
EX v66,v66
M a[12:15],a[236:239],v[76:79],a[12:15]
CV v84,v64,v65
M v[52:55],v[140:143],a[164:167],v[52:55]
EX v67,v67
W lgkmcnt(2)
)"
R"(M a[16:19],a[240:243],v[76:79],a[16:19]
EX v68,v68
M v[52:55],v[144:147],a[168:171],v[52:55]
SA s47,s47,0x4000
s_xor_b32 s58,s47,0x8000
s_and_b32 s58,s58,0xc000
SA s55,s45,s58
M a[20:23],a[244:247],v[76:79],a[20:23]
)"
R"(CV v85,v66,v67
EX v69,v69
M v[52:55],v[148:151],a[172:175],v[52:55]
EX v70,v70
W lgkmcnt(0)
M a[24:27],a[248:251],v[76:79],a[24:27]
CV v86,v68,v69
M a[28:31],a[252:255],v[76:79],a[28:31]
)"
R"(EX v71,v71
M v[208:211],v[224:227],v[76:79],v[208:211]
CV v87,v70,v71
W vmcnt(8) lgkmcnt(0)
s_barrier
M v[56:59],v[88:91],a[176:179],v[36:39]
R v[88:91],v1 offset:0
EX v40,v40
)"
R"(M a[32:35],a[192:195],v[80:83],a[32:35]
SM m0,s54
s_nop 0
B v0,s[60:63],s43 offen offset:0 sc1 lds
M v[56:59],v[92:95],a[180:183],v[56:59]
R v[92:95],v1 offset:1024
EX v41,v41
M a[36:39],a[196:199],v[80:83],a[36:39]
)"
R"(EX v42,v42
CV v72,v40,v41
M v[56:59],v[96:99],a[184:187],v[56:59]
R v[96:99],v1 offset:2048
M a[40:43],a[200:203],v[80:83],a[40:43]
EX v43,v43
M v[56:59],v[100:103],a[188:191],v[56:59]
R v[100:103],v1 offset:3072
)"
R"(EX v44,v44
M a[44:47],a[204:207],v[80:83],a[44:47]
M v[60:63],v[104:107],a[176:179],v[36:39]
R v[104:107],v1 offset:4096
CV v73,v42,v43
M a[48:51],a[208:211],v[80:83],a[48:51]
EX v45,v45
M v[60:63],v[108:111],a[180:183],v[60:63]
)"
R"(R v[108:111],v1 offset:5120
M a[52:55],a[212:215],v[80:83],a[52:55]
EX v46,v46
M v[60:63],v[112:115],a[184:187],v[60:63]
R v[112:115],v1 offset:6144
M a[56:59],a[216:219],v[80:83],a[56:59]
M v[60:63],v[116:119],a[188:191],v[60:63]
R v[116:119],v1 offset:7168
)"
R"(CV v74,v44,v45
M a[60:63],a[220:223],v[80:83],a[60:63]
EX v47,v47
M v[64:67],v[120:123],a[176:179],v[36:39]
R v[120:123],v1 offset:8192
EX v48,v48
M v[212:215],v[224:227],v[80:83],v[212:215]
M v[64:67],v[124:127],a[180:183],v[64:67]
)"
R"(R v[124:127],v1 offset:9216
B v0,s[60:63],s43 offen offset:1024 sc1 lds
CV v75,v46,v47
M a[32:35],a[224:227],v[84:87],a[32:35]
EX v49,v49
M v[64:67],v[128:131],a[184:187],v[64:67]
R v[128:131],v1 offset:10240
M a[36:39],a[228:231],v[84:87],a[36:39]
)"
R"(EX v50,v50
M v[64:67],v[132:135],a[188:191],v[64:67]
R v[132:135],v1 offset:11264
CV v76,v48,v49
M a[40:43],a[232:235],v[84:87],a[40:43]
EX v51,v51
M v[68:71],v[136:139],a[176:179],v[36:39]
R v[136:139],v1 offset:12288
)"
R"(M a[44:47],a[236:239],v[84:87],a[44:47]
EX v52,v52
M v[68:71],v[140:143],a[180:183],v[68:71]
R v[140:143],v1 offset:13312
CV v77,v50,v51
M a[48:51],a[240:243],v[84:87],a[48:51]
M v[68:71],v[144:147],a[184:187],v[68:71]
R v[144:147],v1 offset:14336
)"
R"(EX v53,v53
M a[52:55],a[244:247],v[84:87],a[52:55]
EX v54,v54
M v[68:71],v[148:151],a[188:191],v[68:71]
R v[148:151],v1 offset:15360
SA s40,s40,1
M a[56:59],a[248:251],v[84:87],a[56:59]
CV v78,v52,v53
)"
R"(M a[60:63],a[252:255],v[84:87],a[60:63]
EX v55,v55
M v[212:215],v[224:227],v[84:87],v[212:215]
CV v79,v54,v55
s_branch LEF_%=
T0MF_%=:
W lgkmcnt(12)
M v[40:43],v[88:91],a[128:131],v[24:27]
)"
R"(M v[40:43],v[92:95],a[132:135],v[40:43]
M v[40:43],v[96:99],a[136:139],v[40:43]
M v[40:43],v[100:103],a[140:143],v[40:43]
W lgkmcnt(8)
M v[44:47],v[104:107],a[128:131],v[24:27]
M v[44:47],v[108:111],a[132:135],v[44:47]
M v[44:47],v[112:115],a[136:139],v[44:47]
M v[44:47],v[116:119],a[140:143],v[44:47]
)"
R"(W lgkmcnt(4)
M v[48:51],v[120:123],a[128:131],v[24:27]
M v[48:51],v[124:127],a[132:135],v[48:51]
M v[48:51],v[128:131],a[136:139],v[48:51]
M v[48:51],v[132:135],a[140:143],v[48:51]
W lgkmcnt(0)
M v[52:55],v[136:139],a[128:131],v[24:27]
M v[52:55],v[140:143],a[132:135],v[52:55]
)"
R"(M v[52:55],v[144:147],a[136:139],v[52:55]
M v[52:55],v[148:151],a[140:143],v[52:55]
W lgkmcnt(0)
s_nop 7
M v[56:59],v[88:91],a[144:147],v[28:31]
R a[192:195],v2 offset:0
AD v40,v40,v152
AD v41,v41,v153
)"
R"(M v[56:59],v[92:95],a[148:151],v[56:59]
R a[196:199],v2 offset:1024
AD v42,v42,v154
AD v43,v43,v155
M v[56:59],v[96:99],a[152:155],v[56:59]
R a[200:203],v2 offset:2048
AD v44,v44,v156
AD v45,v45,v157
)"
R"(M v[56:59],v[100:103],a[156:159],v[56:59]
R a[204:207],v2 offset:3072
AD v46,v46,v158
AD v47,v47,v159
M v[60:63],v[104:107],a[144:147],v[28:31]
R a[208:211],v2 offset:4096
AD v48,v48,v160
AD v49,v49,v161
)"
R"(M v[60:63],v[108:111],a[148:151],v[60:63]
R a[212:215],v2 offset:5120
AD v50,v50,v162
AD v51,v51,v163
M v[60:63],v[112:115],a[152:155],v[60:63]
R a[216:219],v2 offset:6144
AD v52,v52,v164
AD v53,v53,v165
)"
R"(M v[60:63],v[116:119],a[156:159],v[60:63]
R a[220:223],v2 offset:7168
AD v54,v54,v166
AD v55,v55,v167
M v[64:67],v[120:123],a[144:147],v[28:31]
R a[224:227],v2 offset:8192
MX v12,v40,v41,v42
MX v12,v12,v43,v44
)"
R"(M v[64:67],v[124:127],a[148:151],v[64:67]
R a[228:231],v2 offset:9216
MX v12,v12,v45,v46
MX v12,v12,v47,v48
M v[64:67],v[128:131],a[152:155],v[64:67]
R a[232:235],v2 offset:10240
MX v12,v12,v49,v50
MX v12,v12,v51,v52
)"
R"(M v[64:67],v[132:135],a[156:159],v[64:67]
R a[236:239],v2 offset:11264
MX v12,v12,v53,v54
v_max_f32 v12,v12,v55
M v[68:71],v[136:139],a[144:147],v[28:31]
R a[240:243],v2 offset:12288
v_mov_b32 v15,v12
s_nop 1
)"
R"(v_permlane32_swap_b32 v12,v15
M v[68:71],v[140:143],a[148:151],v[68:71]
R a[244:247],v2 offset:13312
v_max_f32 v12,v12,v15
v_mov_b32 v15,v12
M v[68:71],v[144:147],a[152:155],v[68:71]
R a[248:251],v2 offset:14336
v_permlane16_swap_b32 v12,v15
)"
R"(v_max_f32 v12,v12,v15
M v[68:71],v[148:151],a[156:159],v[68:71]
R a[252:255],v2 offset:15360
AD v12,0x42ac0000,v12
v_sub_f32 v24,0,v12
v_sub_f32 v25,0,v12
v_sub_f32 v26,0,v12
v_sub_f32 v27,0,v12
)"
R"(v_sub_f32 v40,v40,v12
v_sub_f32 v41,v41,v12
v_sub_f32 v42,v42,v12
v_sub_f32 v43,v43,v12
v_sub_f32 v44,v44,v12
v_sub_f32 v45,v45,v12
v_sub_f32 v46,v46,v12
v_sub_f32 v47,v47,v12
)"
R"(v_sub_f32 v48,v48,v12
v_sub_f32 v49,v49,v12
v_sub_f32 v50,v50,v12
v_sub_f32 v51,v51,v12
v_sub_f32 v52,v52,v12
v_sub_f32 v53,v53,v12
v_sub_f32 v54,v54,v12
v_sub_f32 v55,v55,v12
)"
R"(EX v40,v40
EX v41,v41
EX v42,v42
CV v72,v40,v41
EX v43,v43
EX v44,v44
CV v73,v42,v43
EX v45,v45
)"
R"(EX v46,v46
CV v74,v44,v45
EX v47,v47
EX v48,v48
CV v75,v46,v47
EX v49,v49
EX v50,v50
CV v76,v48,v49
)"
R"(EX v51,v51
EX v52,v52
CV v77,v50,v51
EX v53,v53
EX v54,v54
CV v78,v52,v53
EX v55,v55
s_nop 0
)"
R"(CV v79,v54,v55
M v[40:43],v[88:91],a[160:163],v[32:35]
AD v56,v56,v168
AD v57,v57,v169
AD v58,v58,v170
W lgkmcnt(14)
M a[0:3],a[192:195],v[72:75],a[0:3]
AD v59,v59,v171
)"
R"(AD v60,v60,v172
AD v61,v61,v173
M v[40:43],v[92:95],a[164:167],v[40:43]
AD v62,v62,v174
AD v63,v63,v175
AD v64,v64,v176
M a[4:7],a[196:199],v[72:75],a[4:7]
AD v65,v65,v177
)"
R"(AD v66,v66,v178
AD v67,v67,v179
M v[40:43],v[96:99],a[168:171],v[40:43]
SA s53,s53,0x4000
s_and_b32 s53,s53,0xc000
v_add_u32 v2,s53,v4
W lgkmcnt(12)
M a[8:11],a[200:203],v[72:75],a[8:11]
)"
R"(AD v68,v68,v180
AD v69,v69,v181
AD v70,v70,v182
M v[40:43],v[100:103],a[172:175],v[40:43]
AD v71,v71,v183
MX v12,v56,v57,v58
MX v12,v12,v59,v60
M a[12:15],a[204:207],v[72:75],a[12:15]
)"
R"(MX v12,v12,v61,v62
MX v12,v12,v63,v64
MX v12,v12,v65,v66
M v[44:47],v[104:107],a[160:163],v[32:35]
MX v12,v12,v67,v68
MX v12,v12,v69,v70
v_max_f32 v12,v12,v71
W lgkmcnt(10)
)"
R"(M a[16:19],a[208:211],v[72:75],a[16:19]
v_mov_b32 v15,v12
s_nop 1
v_permlane32_swap_b32 v12,v15
v_max_f32 v12,v12,v15
M v[44:47],v[108:111],a[164:167],v[44:47]
v_mov_b32 v15,v12
s_nop 1
)"
R"(v_permlane16_swap_b32 v12,v15
v_max_f32 v12,v12,v15
M a[20:23],a[212:215],v[72:75],a[20:23]
AD v12,0x42ac0000,v12
v_sub_f32 v28,0,v12
v_sub_f32 v29,0,v12
M v[44:47],v[112:115],a[168:171],v[44:47]
v_sub_f32 v30,0,v12
)"
R"(v_sub_f32 v31,0,v12
W lgkmcnt(8)
M a[24:27],a[216:219],v[72:75],a[24:27]
v_sub_f32 v56,v56,v12
v_sub_f32 v57,v57,v12
M v[44:47],v[116:119],a[172:175],v[44:47]
v_sub_f32 v58,v58,v12
v_sub_f32 v59,v59,v12
)"
R"(v_sub_f32 v60,v60,v12
M a[28:31],a[220:223],v[72:75],a[28:31]
v_sub_f32 v61,v61,v12
v_sub_f32 v62,v62,v12
v_sub_f32 v63,v63,v12
M v[48:51],v[120:123],a[160:163],v[32:35]
v_sub_f32 v64,v64,v12
v_sub_f32 v65,v65,v12
)"
R"(v_sub_f32 v66,v66,v12
M v[208:211],v[224:227],v[72:75],v[208:211]
v_sub_f32 v67,v67,v12
v_sub_f32 v68,v68,v12
M v[48:51],v[124:127],a[164:167],v[48:51]
v_sub_f32 v69,v69,v12
v_sub_f32 v70,v70,v12
v_sub_f32 v71,v71,v12
)"
R"(W lgkmcnt(6)
M a[0:3],a[224:227],v[76:79],a[0:3]
EX v56,v56
M v[48:51],v[128:131],a[168:171],v[48:51]
SA s52,s52,0x4000
s_and_b32 s52,s52,0xc000
v_add_u32 v1,s52,v3
M a[4:7],a[228:231],v[76:79],a[4:7]
)"
R"(EX v57,v57
M v[48:51],v[132:135],a[172:175],v[48:51]
EX v58,v58
CV v80,v56,v57
W lgkmcnt(4)
M a[8:11],a[232:235],v[76:79],a[8:11]
EX v59,v59
M v[52:55],v[136:139],a[160:163],v[32:35]
)"
R"(EX v60,v60
CV v81,v58,v59
M a[12:15],a[236:239],v[76:79],a[12:15]
EX v61,v61
M v[52:55],v[140:143],a[164:167],v[52:55]
EX v62,v62
CV v82,v60,v61
W lgkmcnt(2)
)"
R"(M a[16:19],a[240:243],v[76:79],a[16:19]
EX v63,v63
M v[52:55],v[144:147],a[168:171],v[52:55]
EX v64,v64
CV v83,v62,v63
M a[20:23],a[244:247],v[76:79],a[20:23]
EX v65,v65
M v[52:55],v[148:151],a[172:175],v[52:55]
)"
R"(EX v66,v66
CV v84,v64,v65
W lgkmcnt(0)
M a[24:27],a[248:251],v[76:79],a[24:27]
EX v67,v67
M a[28:31],a[252:255],v[76:79],a[28:31]
EX v68,v68
CV v85,v66,v67
)"
R"(M v[208:211],v[224:227],v[76:79],v[208:211]
EX v69,v69
EX v70,v70
CV v86,v68,v69
EX v71,v71
s_nop 0
CV v87,v70,v71
W vmcnt(8) lgkmcnt(0)
)"
R"(s_barrier
M v[56:59],v[88:91],a[176:179],v[36:39]
R v[88:91],v1 offset:0
MX v12,v40,v41,v42
MX v12,v12,v43,v44
M a[32:35],a[192:195],v[80:83],a[32:35]
SM m0,s54
s_nop 0
)"
R"(B v0,s[60:63],s43 offen offset:0 sc1 lds
M v[56:59],v[92:95],a[180:183],v[56:59]
R v[92:95],v1 offset:1024
MX v12,v12,v45,v46
MX v12,v12,v47,v48
M a[36:39],a[196:199],v[80:83],a[36:39]
MX v12,v12,v49,v50
MX v12,v12,v51,v52
)"
R"(MX v12,v12,v53,v54
M v[56:59],v[96:99],a[184:187],v[56:59]
R v[96:99],v1 offset:2048
v_max_f32 v12,v12,v55
v_mov_b32 v15,v12
M a[40:43],a[200:203],v[80:83],a[40:43]
s_nop 0
v_permlane32_swap_b32 v12,v15
)"
R"(v_max_f32 v12,v12,v15
v_mov_b32 v15,v12
M v[56:59],v[100:103],a[188:191],v[56:59]
R v[100:103],v1 offset:3072
v_permlane16_swap_b32 v12,v15
v_max_f32 v12,v12,v15
M a[44:47],a[204:207],v[80:83],a[44:47]
AD v12,0x42ac0000,v12
)"
R"(v_sub_f32 v32,0,v12
v_sub_f32 v33,0,v12
M v[60:63],v[104:107],a[176:179],v[36:39]
R v[104:107],v1 offset:4096
v_sub_f32 v34,0,v12
v_sub_f32 v35,0,v12
M a[48:51],a[208:211],v[80:83],a[48:51]
v_sub_f32 v40,v40,v12
)"
R"(v_sub_f32 v41,v41,v12
v_sub_f32 v42,v42,v12
M v[60:63],v[108:111],a[180:183],v[60:63]
R v[108:111],v1 offset:5120
v_sub_f32 v43,v43,v12
v_sub_f32 v44,v44,v12
M a[52:55],a[212:215],v[80:83],a[52:55]
v_sub_f32 v45,v45,v12
)"
R"(v_sub_f32 v46,v46,v12
v_sub_f32 v47,v47,v12
M v[60:63],v[112:115],a[184:187],v[60:63]
R v[112:115],v1 offset:6144
v_sub_f32 v48,v48,v12
v_sub_f32 v49,v49,v12
M a[56:59],a[216:219],v[80:83],a[56:59]
v_sub_f32 v50,v50,v12
)"
R"(v_sub_f32 v51,v51,v12
M v[60:63],v[116:119],a[188:191],v[60:63]
R v[116:119],v1 offset:7168
v_sub_f32 v52,v52,v12
v_sub_f32 v53,v53,v12
M a[60:63],a[220:223],v[80:83],a[60:63]
v_sub_f32 v54,v54,v12
v_sub_f32 v55,v55,v12
)"
R"(M v[64:67],v[120:123],a[176:179],v[36:39]
R v[120:123],v1 offset:8192
EX v40,v40
M v[212:215],v[224:227],v[80:83],v[212:215]
EX v41,v41
M v[64:67],v[124:127],a[180:183],v[64:67]
R v[124:127],v1 offset:9216
B v0,s[60:63],s43 offen offset:1024 sc1 lds
)"
R"(M a[32:35],a[224:227],v[84:87],a[32:35]
EX v42,v42
CV v72,v40,v41
M v[64:67],v[128:131],a[184:187],v[64:67]
R v[128:131],v1 offset:10240
EX v43,v43
M a[36:39],a[228:231],v[84:87],a[36:39]
EX v44,v44
)"
R"(CV v73,v42,v43
M v[64:67],v[132:135],a[188:191],v[64:67]
R v[132:135],v1 offset:11264
EX v45,v45
M a[40:43],a[232:235],v[84:87],a[40:43]
EX v46,v46
CV v74,v44,v45
M v[68:71],v[136:139],a[176:179],v[36:39]
)"
R"(R v[136:139],v1 offset:12288
EX v47,v47
M a[44:47],a[236:239],v[84:87],a[44:47]
EX v48,v48
CV v75,v46,v47
M v[68:71],v[140:143],a[180:183],v[68:71]
R v[140:143],v1 offset:13312
EX v49,v49
)"
R"(M a[48:51],a[240:243],v[84:87],a[48:51]
EX v50,v50
CV v76,v48,v49
M v[68:71],v[144:147],a[184:187],v[68:71]
R v[144:147],v1 offset:14336
EX v51,v51
M a[52:55],a[244:247],v[84:87],a[52:55]
EX v52,v52
)"
R"(M v[68:71],v[148:151],a[188:191],v[68:71]
R v[148:151],v1 offset:15360
SA s40,s40,1
CV v77,v50,v51
M a[56:59],a[248:251],v[84:87],a[56:59]
EX v53,v53
M a[60:63],a[252:255],v[84:87],a[60:63]
EX v54,v54
)"
R"(CV v78,v52,v53
M v[212:215],v[224:227],v[84:87],v[212:215]
EX v55,v55
s_nop 0
CV v79,v54,v55
s_nop 7
MX v12,v56,v57,v58
MX v12,v12,v59,v60
)"
R"(MX v12,v12,v61,v62
MX v12,v12,v63,v64
MX v12,v12,v65,v66
MX v12,v12,v67,v68
MX v12,v12,v69,v70
v_max_f32 v12,v12,v71
v_mov_b32 v15,v12
s_nop 1
)"
R"(v_permlane32_swap_b32 v12,v15
v_max_f32 v12,v12,v15
v_mov_b32 v15,v12
s_nop 1
v_permlane16_swap_b32 v12,v15
v_max_f32 v12,v12,v15
AD v12,0x42ac0000,v12
v_sub_f32 v36,0,v12
)"
R"(v_sub_f32 v37,0,v12
v_sub_f32 v38,0,v12
v_sub_f32 v39,0,v12
v_sub_f32 v56,v56,v12
v_sub_f32 v57,v57,v12
v_sub_f32 v58,v58,v12
v_sub_f32 v59,v59,v12
v_sub_f32 v60,v60,v12
)"
R"(v_sub_f32 v61,v61,v12
v_sub_f32 v62,v62,v12
v_sub_f32 v63,v63,v12
v_sub_f32 v64,v64,v12
v_sub_f32 v65,v65,v12
v_sub_f32 v66,v66,v12
v_sub_f32 v67,v67,v12
v_sub_f32 v68,v68,v12
)"
R"(v_sub_f32 v69,v69,v12
v_sub_f32 v70,v70,v12
v_sub_f32 v71,v71,v12
LEF_%=:
s_nop 7
s_nop 3
v_cmp_nle_f32 vcc,v208,v228
s_nop 1
)"
R"(s_or_b64 s[84:85],s[84:85],vcc
v_rcp_f32 v208,v208
AR v184,a0
AR v185,a1
AR v186,a2
AR v187,a3
AR v188,a4
AR v189,a5
)"
R"(AR v190,a6
AR v191,a7
MU v184,v184,v208
MU v185,v185,v208
MU v186,v186,v208
MU v187,v187,v208
MU v188,v188,v208
MU v189,v189,v208
)"
R"(MU v190,v190,v208
MU v191,v191,v208
CV v192,v184,v185
CV v193,v186,v187
CV v194,v188,v189
CV v195,v190,v191
GS v229,v[192:195],s[78:79] offset:0 sc1
AR v196,a8
)"
R"(AR v197,a9
AR v198,a10
AR v199,a11
AR v200,a12
AR v201,a13
AR v202,a14
AR v203,a15
MU v196,v196,v208
)"
R"(MU v197,v197,v208
MU v198,v198,v208
MU v199,v199,v208
MU v200,v200,v208
MU v201,v201,v208
MU v202,v202,v208
MU v203,v203,v208
CV v204,v196,v197
)"
R"(CV v205,v198,v199
CV v206,v200,v201
CV v207,v202,v203
GS v230,v[204:207],s[78:79] offset:0 sc1
AR v184,a16
AR v185,a17
AR v186,a18
AR v187,a19
)"
R"(AR v188,a20
AR v189,a21
AR v190,a22
AR v191,a23
MU v184,v184,v208
MU v185,v185,v208
MU v186,v186,v208
MU v187,v187,v208
)"
R"(MU v188,v188,v208
MU v189,v189,v208
MU v190,v190,v208
MU v191,v191,v208
CV v192,v184,v185
CV v193,v186,v187
CV v194,v188,v189
CV v195,v190,v191
)"
R"(GS v231,v[192:195],s[78:79] offset:0 sc1
AR v196,a24
AR v197,a25
AR v198,a26
AR v199,a27
AR v200,a28
AR v201,a29
AR v202,a30
)"
R"(AR v203,a31
MU v196,v196,v208
MU v197,v197,v208
MU v198,v198,v208
MU v199,v199,v208
MU v200,v200,v208
MU v201,v201,v208
MU v202,v202,v208
)"
R"(MU v203,v203,v208
CV v204,v196,v197
CV v205,v198,v199
CV v206,v200,v201
CV v207,v202,v203
GS v232,v[204:207],s[78:79] offset:0 sc1
v_cmp_nle_f32 vcc,v212,v228
s_nop 1
)"
R"(s_or_b64 s[84:85],s[84:85],vcc
v_rcp_f32 v212,v212
AR v184,a32
AR v185,a33
AR v186,a34
AR v187,a35
AR v188,a36
AR v189,a37
)"
R"(AR v190,a38
AR v191,a39
MU v184,v184,v212
MU v185,v185,v212
MU v186,v186,v212
MU v187,v187,v212
MU v188,v188,v212
MU v189,v189,v212
)"
R"(MU v190,v190,v212
MU v191,v191,v212
CV v192,v184,v185
CV v193,v186,v187
CV v194,v188,v189
CV v195,v190,v191
GS v229,v[192:195],s[78:79] offset:1024 sc1
AR v196,a40
)"
R"(AR v197,a41
AR v198,a42
AR v199,a43
AR v200,a44
AR v201,a45
AR v202,a46
AR v203,a47
MU v196,v196,v212
)"
R"(MU v197,v197,v212
MU v198,v198,v212
MU v199,v199,v212
MU v200,v200,v212
MU v201,v201,v212
MU v202,v202,v212
MU v203,v203,v212
CV v204,v196,v197
)"
R"(CV v205,v198,v199
CV v206,v200,v201
CV v207,v202,v203
GS v230,v[204:207],s[78:79] offset:1024 sc1
AR v184,a48
AR v185,a49
AR v186,a50
AR v187,a51
)"
R"(AR v188,a52
AR v189,a53
AR v190,a54
AR v191,a55
MU v184,v184,v212
MU v185,v185,v212
MU v186,v186,v212
MU v187,v187,v212
)"
R"(MU v188,v188,v212
MU v189,v189,v212
MU v190,v190,v212
MU v191,v191,v212
CV v192,v184,v185
CV v193,v186,v187
CV v194,v188,v189
CV v195,v190,v191
)"
R"(GS v231,v[192:195],s[78:79] offset:1024 sc1
AR v196,a56
AR v197,a57
AR v198,a58
AR v199,a59
AR v200,a60
AR v201,a61
AR v202,a62
)"
R"(AR v203,a63
MU v196,v196,v212
MU v197,v197,v212
MU v198,v198,v212
MU v199,v199,v212
MU v200,v200,v212
MU v201,v201,v212
MU v202,v202,v212
)"
R"(MU v203,v203,v212
CV v204,v196,v197
CV v205,v198,v199
CV v206,v200,v201
CV v207,v202,v203
GS v232,v[204:207],s[78:79] offset:1024 sc1
s_cmp_ge_u32 s40,s42
s_cbranch_scc1 SMF_%=
)"
R"(SLBF_%=:
W lgkmcnt(14)
M v[40:43],v[88:91],a[160:163],v[32:35]
EX v56,v56
M a[64:67],a[192:195],v[72:75],a[64:67]
B v0,s[60:63],s43 offen offset:2048 sc1 lds
EX v57,v57
M v[40:43],v[92:95],a[164:167],v[40:43]
)"
R"(EX v58,v58
M a[68:71],a[196:199],v[72:75],a[68:71]
CV v80,v56,v57
W lgkmcnt(12)
M v[40:43],v[96:99],a[168:171],v[40:43]
M a[72:75],a[200:203],v[72:75],a[72:75]
EX v59,v59
M v[40:43],v[100:103],a[172:175],v[40:43]
)"
R"(B v0,s[60:63],s43 offen offset:3072 sc1 lds
EX v60,v60
M a[76:79],a[204:207],v[72:75],a[76:79]
W lgkmcnt(10)
M v[44:47],v[104:107],a[160:163],v[32:35]
CV v81,v58,v59
M a[80:83],a[208:211],v[72:75],a[80:83]
EX v61,v61
)"
R"(M v[44:47],v[108:111],a[164:167],v[44:47]
M a[84:87],a[212:215],v[72:75],a[84:87]
EX v62,v62
W lgkmcnt(8)
M v[44:47],v[112:115],a[168:171],v[44:47]
SM m0,s55
s_nop 0
B v0,s[64:67],s47 offen offset:0 sc1 lds
)"
R"(SA s43,s43,0x4000
s_and_b32 s46,s43,0xc000
SA s54,s44,s46
M a[88:91],a[216:219],v[72:75],a[88:91]
M v[44:47],v[116:119],a[172:175],v[44:47]
CV v82,v60,v61
M a[92:95],a[220:223],v[72:75],a[92:95]
EX v63,v63
)"
R"(W lgkmcnt(6)
M v[48:51],v[120:123],a[160:163],v[32:35]
EX v64,v64
M v[216:219],v[224:227],v[72:75],v[216:219]
M v[48:51],v[124:127],a[164:167],v[48:51]
B v0,s[64:67],s47 offen offset:1024 sc1 lds
CV v83,v62,v63
M a[64:67],a[224:227],v[76:79],a[64:67]
)"
R"(EX v65,v65
W lgkmcnt(4)
M v[48:51],v[128:131],a[168:171],v[48:51]
SA s52,s52,0x4000
s_and_b32 s52,s52,0xc000
v_add_u32 v1,s52,v3
M a[68:71],a[228:231],v[76:79],a[68:71]
EX v66,v66
)"
R"(M v[48:51],v[132:135],a[172:175],v[48:51]
CV v84,v64,v65
M a[72:75],a[232:235],v[76:79],a[72:75]
B v0,s[64:67],s47 offen offset:2048 sc1 lds
EX v67,v67
W lgkmcnt(2)
M v[52:55],v[136:139],a[160:163],v[32:35]
M a[76:79],a[236:239],v[76:79],a[76:79]
)"
R"(EX v68,v68
M v[52:55],v[140:143],a[164:167],v[52:55]
CV v85,v66,v67
M a[80:83],a[240:243],v[76:79],a[80:83]
W lgkmcnt(0)
M v[52:55],v[144:147],a[168:171],v[52:55]
EX v69,v69
M a[84:87],a[244:247],v[76:79],a[84:87]
)"
R"(B v0,s[64:67],s47 offen offset:3072 sc1 lds
EX v70,v70
M v[52:55],v[148:151],a[172:175],v[52:55]
SA s47,s47,0x4000
s_xor_b32 s58,s47,0x8000
s_and_b32 s58,s58,0xc000
SA s55,s45,s58
M a[88:91],a[248:251],v[76:79],a[88:91]
)"
R"(CV v86,v68,v69
M a[92:95],a[252:255],v[76:79],a[92:95]
EX v71,v71
M v[216:219],v[224:227],v[76:79],v[216:219]
CV v87,v70,v71
W vmcnt(8) lgkmcnt(0)
s_barrier
M v[56:59],v[88:91],a[176:179],v[36:39]
)"
R"(R v[88:91],v1 offset:0
EX v40,v40
M a[96:99],a[192:195],v[80:83],a[96:99]
R a[192:195],v2 offset:0
SM m0,s54
s_nop 0
B v0,s[60:63],s43 offen offset:0 sc1 lds
M v[56:59],v[92:95],a[180:183],v[56:59]
)"
R"(R v[92:95],v1 offset:1024
EX v41,v41
M a[100:103],a[196:199],v[80:83],a[100:103]
R a[196:199],v2 offset:1024
EX v42,v42
M v[56:59],v[96:99],a[184:187],v[56:59]
R v[96:99],v1 offset:2048
CV v72,v40,v41
)"
R"(M a[104:107],a[200:203],v[80:83],a[104:107]
R a[200:203],v2 offset:2048
EX v43,v43
M v[56:59],v[100:103],a[188:191],v[56:59]
R v[100:103],v1 offset:3072
EX v44,v44
M a[108:111],a[204:207],v[80:83],a[108:111]
R a[204:207],v2 offset:3072
)"
R"(M v[60:63],v[104:107],a[176:179],v[36:39]
R v[104:107],v1 offset:4096
CV v73,v42,v43
M a[112:115],a[208:211],v[80:83],a[112:115]
R a[208:211],v2 offset:4096
EX v45,v45
M v[60:63],v[108:111],a[180:183],v[60:63]
R v[108:111],v1 offset:5120
)"
R"(M a[116:119],a[212:215],v[80:83],a[116:119]
R a[212:215],v2 offset:5120
EX v46,v46
M v[60:63],v[112:115],a[184:187],v[60:63]
R v[112:115],v1 offset:6144
M a[120:123],a[216:219],v[80:83],a[120:123]
R a[216:219],v2 offset:6144
M v[60:63],v[116:119],a[188:191],v[60:63]
)"
R"(R v[116:119],v1 offset:7168
CV v74,v44,v45
M a[124:127],a[220:223],v[80:83],a[124:127]
R a[220:223],v2 offset:7168
EX v47,v47
M v[64:67],v[120:123],a[176:179],v[36:39]
R v[120:123],v1 offset:8192
EX v48,v48
)"
R"(M v[220:223],v[224:227],v[80:83],v[220:223]
M v[64:67],v[124:127],a[180:183],v[64:67]
R v[124:127],v1 offset:9216
B v0,s[60:63],s43 offen offset:1024 sc1 lds
CV v75,v46,v47
M a[96:99],a[224:227],v[84:87],a[96:99]
R a[224:227],v2 offset:8192
EX v49,v49
)"
R"(M v[64:67],v[128:131],a[184:187],v[64:67]
R v[128:131],v1 offset:10240
M a[100:103],a[228:231],v[84:87],a[100:103]
R a[228:231],v2 offset:9216
EX v50,v50
M v[64:67],v[132:135],a[188:191],v[64:67]
R v[132:135],v1 offset:11264
CV v76,v48,v49
)"
R"(M a[104:107],a[232:235],v[84:87],a[104:107]
R a[232:235],v2 offset:10240
EX v51,v51
M v[68:71],v[136:139],a[176:179],v[36:39]
R v[136:139],v1 offset:12288
M a[108:111],a[236:239],v[84:87],a[108:111]
R a[236:239],v2 offset:11264
EX v52,v52
)"
R"(M v[68:71],v[140:143],a[180:183],v[68:71]
R v[140:143],v1 offset:13312
CV v77,v50,v51
M a[112:115],a[240:243],v[84:87],a[112:115]
R a[240:243],v2 offset:12288
M v[68:71],v[144:147],a[184:187],v[68:71]
R v[144:147],v1 offset:14336
EX v53,v53
)"
R"(M a[116:119],a[244:247],v[84:87],a[116:119]
R a[244:247],v2 offset:13312
SA s40,s40,1
M v[68:71],v[148:151],a[188:191],v[68:71]
R v[148:151],v1 offset:15360
EX v54,v54
M a[120:123],a[248:251],v[84:87],a[120:123]
R a[248:251],v2 offset:14336
)"
R"(CV v78,v52,v53
M a[124:127],a[252:255],v[84:87],a[124:127]
R a[252:255],v2 offset:15360
EX v55,v55
M v[220:223],v[224:227],v[84:87],v[220:223]
SA s53,s53,0x4000
s_and_b32 s53,s53,0xc000
v_add_u32 v2,s53,v4
)"
R"(CV v79,v54,v55
s_cmp_lt_u32 s40,s42
s_cbranch_scc1 SLBF_%=
SMF_%=:
W lgkmcnt(14)
M v[40:43],v[88:91],a[160:163],v[32:35]
EX v56,v56
M a[64:67],a[192:195],v[72:75],a[64:67]
)"
R"(B v0,s[60:63],s43 offen offset:2048 sc1 lds
EX v57,v57
M v[40:43],v[92:95],a[164:167],v[40:43]
EX v58,v58
M a[68:71],a[196:199],v[72:75],a[68:71]
CV v80,v56,v57
W lgkmcnt(12)
M v[40:43],v[96:99],a[168:171],v[40:43]
)"
R"(M a[72:75],a[200:203],v[72:75],a[72:75]
EX v59,v59
M v[40:43],v[100:103],a[172:175],v[40:43]
B v0,s[60:63],s43 offen offset:3072 sc1 lds
EX v60,v60
M a[76:79],a[204:207],v[72:75],a[76:79]
W lgkmcnt(10)
M v[44:47],v[104:107],a[160:163],v[32:35]
)"
R"(CV v81,v58,v59
M a[80:83],a[208:211],v[72:75],a[80:83]
EX v61,v61
M v[44:47],v[108:111],a[164:167],v[44:47]
M a[84:87],a[212:215],v[72:75],a[84:87]
EX v62,v62
W lgkmcnt(8)
M v[44:47],v[112:115],a[168:171],v[44:47]
)"
R"(SM m0,s55
s_nop 0
B v0,s[64:67],s47 offen offset:0 sc1 lds
SA s43,s43,0x4000
s_and_b32 s46,s43,0xc000
SA s54,s44,s46
M a[88:91],a[216:219],v[72:75],a[88:91]
M v[44:47],v[116:119],a[172:175],v[44:47]
)"
R"(CV v82,v60,v61
M a[92:95],a[220:223],v[72:75],a[92:95]
EX v63,v63
W lgkmcnt(6)
M v[48:51],v[120:123],a[160:163],v[32:35]
EX v64,v64
M v[216:219],v[224:227],v[72:75],v[216:219]
M v[48:51],v[124:127],a[164:167],v[48:51]
)"
R"(B v0,s[64:67],s47 offen offset:1024 sc1 lds
CV v83,v62,v63
M a[64:67],a[224:227],v[76:79],a[64:67]
EX v65,v65
W lgkmcnt(4)
M v[48:51],v[128:131],a[168:171],v[48:51]
SA s52,s52,0x4000
s_and_b32 s52,s52,0xc000
)"
R"(v_add_u32 v1,s52,v3
M a[68:71],a[228:231],v[76:79],a[68:71]
EX v66,v66
M v[48:51],v[132:135],a[172:175],v[48:51]
CV v84,v64,v65
M a[72:75],a[232:235],v[76:79],a[72:75]
B v0,s[64:67],s47 offen offset:2048 sc1 lds
EX v67,v67
)"
R"(W lgkmcnt(2)
M v[52:55],v[136:139],a[160:163],v[32:35]
M a[76:79],a[236:239],v[76:79],a[76:79]
EX v68,v68
M v[52:55],v[140:143],a[164:167],v[52:55]
CV v85,v66,v67
M a[80:83],a[240:243],v[76:79],a[80:83]
W lgkmcnt(0)
)"
R"(M v[52:55],v[144:147],a[168:171],v[52:55]
EX v69,v69
M a[84:87],a[244:247],v[76:79],a[84:87]
B v0,s[64:67],s47 offen offset:3072 sc1 lds
EX v70,v70
M v[52:55],v[148:151],a[172:175],v[52:55]
SA s47,s47,0x4000
s_xor_b32 s58,s47,0x8000
)"
R"(s_and_b32 s58,s58,0xc000
SA s55,s45,s58
M a[88:91],a[248:251],v[76:79],a[88:91]
CV v86,v68,v69
M a[92:95],a[252:255],v[76:79],a[92:95]
EX v71,v71
M v[216:219],v[224:227],v[76:79],v[216:219]
CV v87,v70,v71
)"
R"(W vmcnt(8) lgkmcnt(0)
s_barrier
M v[56:59],v[88:91],a[176:179],v[36:39]
R v[88:91],v1 offset:0
AD v40,v40,v152
AD v41,v41,v153
M a[96:99],a[192:195],v[80:83],a[96:99]
R a[192:195],v2 offset:0
)"
R"(SM m0,s54
s_nop 0
B v0,s[60:63],s43 offen offset:0 sc1 lds
M v[56:59],v[92:95],a[180:183],v[56:59]
R v[92:95],v1 offset:1024
AD v42,v42,v154
AD v43,v43,v155
M a[100:103],a[196:199],v[80:83],a[100:103]
)"
R"(R a[196:199],v2 offset:1024
AD v44,v44,v156
AD v45,v45,v157
M v[56:59],v[96:99],a[184:187],v[56:59]
R v[96:99],v1 offset:2048
AD v46,v46,v158
AD v47,v47,v159
M a[104:107],a[200:203],v[80:83],a[104:107]
)"
R"(R a[200:203],v2 offset:2048
AD v48,v48,v160
AD v49,v49,v161
M v[56:59],v[100:103],a[188:191],v[56:59]
R v[100:103],v1 offset:3072
AD v50,v50,v162
AD v51,v51,v163
M a[108:111],a[204:207],v[80:83],a[108:111]
)"
R"(R a[204:207],v2 offset:3072
AD v52,v52,v164
AD v53,v53,v165
M v[60:63],v[104:107],a[176:179],v[36:39]
R v[104:107],v1 offset:4096
AD v54,v54,v166
AD v55,v55,v167
M a[112:115],a[208:211],v[80:83],a[112:115]
)"
R"(R a[208:211],v2 offset:4096
EX v40,v40
M v[60:63],v[108:111],a[180:183],v[60:63]
R v[108:111],v1 offset:5120
EX v41,v41
M a[116:119],a[212:215],v[80:83],a[116:119]
R a[212:215],v2 offset:5120
EX v42,v42
)"
R"(M v[60:63],v[112:115],a[184:187],v[60:63]
R v[112:115],v1 offset:6144
M a[120:123],a[216:219],v[80:83],a[120:123]
R a[216:219],v2 offset:6144
CV v72,v40,v41
M v[60:63],v[116:119],a[188:191],v[60:63]
R v[116:119],v1 offset:7168
EX v43,v43
)"
R"(M a[124:127],a[220:223],v[80:83],a[124:127]
R a[220:223],v2 offset:7168
EX v44,v44
M v[64:67],v[120:123],a[176:179],v[36:39]
R v[120:123],v1 offset:8192
CV v73,v42,v43
M v[220:223],v[224:227],v[80:83],v[220:223]
EX v45,v45
)"
R"(M v[64:67],v[124:127],a[180:183],v[64:67]
R v[124:127],v1 offset:9216
B v0,s[60:63],s43 offen offset:1024 sc1 lds
M a[96:99],a[224:227],v[84:87],a[96:99]
R a[224:227],v2 offset:8192
EX v46,v46
M v[64:67],v[128:131],a[184:187],v[64:67]
R v[128:131],v1 offset:10240
)"
R"(CV v74,v44,v45
M a[100:103],a[228:231],v[84:87],a[100:103]
R a[228:231],v2 offset:9216
EX v47,v47
M v[64:67],v[132:135],a[188:191],v[64:67]
R v[132:135],v1 offset:11264
EX v48,v48
M a[104:107],a[232:235],v[84:87],a[104:107]
)"
R"(R a[232:235],v2 offset:10240
CV v75,v46,v47
M v[68:71],v[136:139],a[176:179],v[36:39]
R v[136:139],v1 offset:12288
EX v49,v49
M a[108:111],a[236:239],v[84:87],a[108:111]
R a[236:239],v2 offset:11264
EX v50,v50
)"
R"(M v[68:71],v[140:143],a[180:183],v[68:71]
R v[140:143],v1 offset:13312
CV v76,v48,v49
M a[112:115],a[240:243],v[84:87],a[112:115]
R a[240:243],v2 offset:12288
EX v51,v51
M v[68:71],v[144:147],a[184:187],v[68:71]
R v[144:147],v1 offset:14336
)"
R"(EX v52,v52
M a[116:119],a[244:247],v[84:87],a[116:119]
R a[244:247],v2 offset:13312
SA s40,s40,1
CV v77,v50,v51
M v[68:71],v[148:151],a[188:191],v[68:71]
R v[148:151],v1 offset:15360
EX v53,v53
)"
R"(M a[120:123],a[248:251],v[84:87],a[120:123]
R a[248:251],v2 offset:14336
EX v54,v54
M a[124:127],a[252:255],v[84:87],a[124:127]
R a[252:255],v2 offset:15360
CV v78,v52,v53
M v[220:223],v[224:227],v[84:87],v[220:223]
SA s53,s53,0x4000
)"
R"(s_and_b32 s53,s53,0xc000
v_add_u32 v2,s53,v4
EX v55,v55
s_nop 0
CV v79,v54,v55
W lgkmcnt(15)
M a[64:67],a[192:195],v[72:75],a[64:67]
AD v56,v56,v168
)"
R"(AD v57,v57,v169
AD v58,v58,v170
M a[68:71],a[196:199],v[72:75],a[68:71]
AD v59,v59,v171
AD v60,v60,v172
AD v61,v61,v173
M a[72:75],a[200:203],v[72:75],a[72:75]
AD v62,v62,v174
)"
R"(AD v63,v63,v175
AD v64,v64,v176
M a[76:79],a[204:207],v[72:75],a[76:79]
AD v65,v65,v177
AD v66,v66,v178
AD v67,v67,v179
M a[80:83],a[208:211],v[72:75],a[80:83]
AD v68,v68,v180
)"
R"(AD v69,v69,v181
AD v70,v70,v182
M a[84:87],a[212:215],v[72:75],a[84:87]
AD v71,v71,v183
EX v56,v56
M a[88:91],a[216:219],v[72:75],a[88:91]
EX v57,v57
M a[92:95],a[220:223],v[72:75],a[92:95]
)"
R"(EX v58,v58
CV v80,v56,v57
M v[216:219],v[224:227],v[72:75],v[216:219]
EX v59,v59
W lgkmcnt(8)
M a[64:67],a[224:227],v[76:79],a[64:67]
EX v60,v60
CV v81,v58,v59
)"
R"(M a[68:71],a[228:231],v[76:79],a[68:71]
EX v61,v61
M a[72:75],a[232:235],v[76:79],a[72:75]
EX v62,v62
CV v82,v60,v61
M a[76:79],a[236:239],v[76:79],a[76:79]
EX v63,v63
W lgkmcnt(0)
)"
R"(M a[80:83],a[240:243],v[76:79],a[80:83]
EX v64,v64
CV v83,v62,v63
M a[84:87],a[244:247],v[76:79],a[84:87]
EX v65,v65
M a[88:91],a[248:251],v[76:79],a[88:91]
EX v66,v66
CV v84,v64,v65
)"
R"(M a[92:95],a[252:255],v[76:79],a[92:95]
EX v67,v67
M v[216:219],v[224:227],v[76:79],v[216:219]
EX v68,v68
CV v85,v66,v67
EX v69,v69
EX v70,v70
CV v86,v68,v69
)"
R"(EX v71,v71
s_nop 0
CV v87,v70,v71
M a[96:99],a[192:195],v[80:83],a[96:99]
M a[100:103],a[196:199],v[80:83],a[100:103]
M a[104:107],a[200:203],v[80:83],a[104:107]
M a[108:111],a[204:207],v[80:83],a[108:111]
M a[112:115],a[208:211],v[80:83],a[112:115]
)"
R"(M a[116:119],a[212:215],v[80:83],a[116:119]
M a[120:123],a[216:219],v[80:83],a[120:123]
M a[124:127],a[220:223],v[80:83],a[124:127]
M v[220:223],v[224:227],v[80:83],v[220:223]
M a[96:99],a[224:227],v[84:87],a[96:99]
M a[100:103],a[228:231],v[84:87],a[100:103]
M a[104:107],a[232:235],v[84:87],a[104:107]
M a[108:111],a[236:239],v[84:87],a[108:111]
)"
R"(M a[112:115],a[240:243],v[84:87],a[112:115]
M a[116:119],a[244:247],v[84:87],a[116:119]
M a[120:123],a[248:251],v[84:87],a[120:123]
M a[124:127],a[252:255],v[84:87],a[124:127]
M v[220:223],v[224:227],v[84:87],v[220:223]
s_nop 7
s_nop 3
v_cmp_nle_f32 vcc,v216,v228
)"
R"(s_nop 1
s_or_b64 s[84:85],s[84:85],vcc
v_rcp_f32 v216,v216
AR v184,a64
AR v185,a65
AR v186,a66
AR v187,a67
AR v188,a68
)"
R"(AR v189,a69
AR v190,a70
AR v191,a71
MU v184,v184,v216
MU v185,v185,v216
MU v186,v186,v216
MU v187,v187,v216
MU v188,v188,v216
)"
R"(MU v189,v189,v216
MU v190,v190,v216
MU v191,v191,v216
CV v192,v184,v185
CV v193,v186,v187
CV v194,v188,v189
CV v195,v190,v191
GS v229,v[192:195],s[80:81] offset:0 sc1
)"
R"(AR v196,a72
AR v197,a73
AR v198,a74
AR v199,a75
AR v200,a76
AR v201,a77
AR v202,a78
AR v203,a79
)"
R"(MU v196,v196,v216
MU v197,v197,v216
MU v198,v198,v216
MU v199,v199,v216
MU v200,v200,v216
MU v201,v201,v216
MU v202,v202,v216
MU v203,v203,v216
)"
R"(CV v204,v196,v197
CV v205,v198,v199
CV v206,v200,v201
CV v207,v202,v203
GS v230,v[204:207],s[80:81] offset:0 sc1
AR v184,a80
AR v185,a81
AR v186,a82
)"
R"(AR v187,a83
AR v188,a84
AR v189,a85
AR v190,a86
AR v191,a87
MU v184,v184,v216
MU v185,v185,v216
MU v186,v186,v216
)"
R"(MU v187,v187,v216
MU v188,v188,v216
MU v189,v189,v216
MU v190,v190,v216
MU v191,v191,v216
CV v192,v184,v185
CV v193,v186,v187
CV v194,v188,v189
)"
R"(CV v195,v190,v191
GS v231,v[192:195],s[80:81] offset:0 sc1
AR v196,a88
AR v197,a89
AR v198,a90
AR v199,a91
AR v200,a92
AR v201,a93
)"
R"(AR v202,a94
AR v203,a95
MU v196,v196,v216
MU v197,v197,v216
MU v198,v198,v216
MU v199,v199,v216
MU v200,v200,v216
MU v201,v201,v216
)"
R"(MU v202,v202,v216
MU v203,v203,v216
CV v204,v196,v197
CV v205,v198,v199
CV v206,v200,v201
CV v207,v202,v203
GS v232,v[204:207],s[80:81] offset:0 sc1
v_cmp_nle_f32 vcc,v220,v228
)"
R"(s_nop 1
s_or_b64 s[84:85],s[84:85],vcc
v_rcp_f32 v220,v220
AR v184,a96
AR v185,a97
AR v186,a98
AR v187,a99
AR v188,a100
)"
R"(AR v189,a101
AR v190,a102
AR v191,a103
MU v184,v184,v220
MU v185,v185,v220
MU v186,v186,v220
MU v187,v187,v220
MU v188,v188,v220
)"
R"(MU v189,v189,v220
MU v190,v190,v220
MU v191,v191,v220
CV v192,v184,v185
CV v193,v186,v187
CV v194,v188,v189
CV v195,v190,v191
GS v229,v[192:195],s[80:81] offset:1024 sc1
)"
R"(AR v196,a104
AR v197,a105
AR v198,a106
AR v199,a107
AR v200,a108
AR v201,a109
AR v202,a110
AR v203,a111
)"
R"(MU v196,v196,v220
MU v197,v197,v220
MU v198,v198,v220
MU v199,v199,v220
MU v200,v200,v220
MU v201,v201,v220
MU v202,v202,v220
MU v203,v203,v220
)"
R"(CV v204,v196,v197
CV v205,v198,v199
CV v206,v200,v201
CV v207,v202,v203
GS v230,v[204:207],s[80:81] offset:1024 sc1
AR v184,a112
AR v185,a113
AR v186,a114
)"
R"(AR v187,a115
AR v188,a116
AR v189,a117
AR v190,a118
AR v191,a119
MU v184,v184,v220
MU v185,v185,v220
MU v186,v186,v220
)"
R"(MU v187,v187,v220
MU v188,v188,v220
MU v189,v189,v220
MU v190,v190,v220
MU v191,v191,v220
CV v192,v184,v185
CV v193,v186,v187
CV v194,v188,v189
)"
R"(CV v195,v190,v191
GS v231,v[192:195],s[80:81] offset:1024 sc1
AR v196,a120
AR v197,a121
AR v198,a122
AR v199,a123
AR v200,a124
AR v201,a125
)"
R"(AR v202,a126
AR v203,a127
MU v196,v196,v220
MU v197,v197,v220
MU v198,v198,v220
MU v199,v199,v220
MU v200,v200,v220
MU v201,v201,v220
)"
R"(MU v202,v202,v220
MU v203,v203,v220
CV v204,v196,v197
CV v205,v198,v199
CV v206,v200,v201
CV v207,v202,v203
GS v232,v[204:207],s[80:81] offset:1024 sc1
s_bitcmp1_b32 s42,0
)"
R"(s_cbranch_scc1 AEF_%=
s_barrier
AEF_%=:
s_cmp_lg_u64 s[84:85],0
s_cselect_b32 s46,1,0
v_mov_b32 v9,s46
s_lshl_b32 s46,%17,2
SA s46,s46,%18
)"
R"(SA s46,s46,65536
v_mov_b32 v10,s46
ds_write_b32 v10,v9 offset:0
W lgkmcnt(0)
s_barrier
SA s46,%18,65536
v_mov_b32 v10,s46
R v[12:15],v10 offset:0
)"
R"(W lgkmcnt(0)
v_or3_b32 v12,v12,v13,v14
v_or_b32 v12,v12,v15
s_nop 1
v_readfirstlane_b32 s46,v12
s_nop 3
s_cmp_eq_u32 s46,0
s_cbranch_scc1 AOK_%=
)"
R"(W vmcnt(0) lgkmcnt(0)
s_barrier
SM s43,0
SA s54,s44,0
SM s47,0
SA s55,s45,32768
SM m0,s54
s_nop 0
)"
R"(B v0,s[60:63],s43 offen offset:0 sc1 lds
B v0,s[60:63],s43 offen offset:1024 sc1 lds
B v0,s[60:63],s43 offen offset:2048 sc1 lds
B v0,s[60:63],s43 offen offset:3072 sc1 lds
SM m0,s55
s_nop 0
B v0,s[64:67],s47 offen offset:0 sc1 lds
B v0,s[64:67],s47 offen offset:1024 sc1 lds
)"
R"(B v0,s[64:67],s47 offen offset:2048 sc1 lds
B v0,s[64:67],s47 offen offset:3072 sc1 lds
SM s43,16384
SA s54,s44,16384
SM s47,16384
SA s55,s45,49152
SM m0,s54
s_nop 0
)"
R"(B v0,s[60:63],s43 offen offset:0 sc1 lds
B v0,s[60:63],s43 offen offset:1024 sc1 lds
B v0,s[60:63],s43 offen offset:2048 sc1 lds
B v0,s[60:63],s43 offen offset:3072 sc1 lds
SM m0,s55
s_nop 0
B v0,s[64:67],s47 offen offset:0 sc1 lds
B v0,s[64:67],s47 offen offset:1024 sc1 lds
)"
R"(B v0,s[64:67],s47 offen offset:2048 sc1 lds
B v0,s[64:67],s47 offen offset:3072 sc1 lds
SM s43,32768
SA s54,s44,32768
SM s47,32768
SA s55,s45,0
SM m0,s54
s_nop 0
)"
R"(B v0,s[60:63],s43 offen offset:0 sc1 lds
B v0,s[60:63],s43 offen offset:1024 sc1 lds
B v0,s[60:63],s43 offen offset:2048 sc1 lds
B v0,s[60:63],s43 offen offset:3072 sc1 lds
SM m0,s55
s_nop 0
B v0,s[64:67],s47 offen offset:0 sc1 lds
B v0,s[64:67],s47 offen offset:1024 sc1 lds
)"
R"(B v0,s[64:67],s47 offen offset:2048 sc1 lds
B v0,s[64:67],s47 offen offset:3072 sc1 lds
SM s43,49152
SA s54,s44,49152
SM s47,49152
SA s55,s45,16384
SM s52,0
SM s53,0x8000
)"
R"(SM s40,0
v_mov_b32 v1,v3
v_add_u32 v2,0x8000,v4
v_mov_b32 v208,0
v_mov_b32 v209,0
v_mov_b32 v210,0
v_mov_b32 v211,0
v_mov_b32 v24,0
)"
R"(v_mov_b32 v25,0
v_mov_b32 v26,0
v_mov_b32 v27,0
SM s48,0xff800000
v_mov_b32 v212,0
v_mov_b32 v213,0
v_mov_b32 v214,0
v_mov_b32 v215,0
)"
R"(v_mov_b32 v28,0
v_mov_b32 v29,0
v_mov_b32 v30,0
v_mov_b32 v31,0
SM s49,0xff800000
v_mov_b32 v216,0
v_mov_b32 v217,0
v_mov_b32 v218,0
)"
R"(v_mov_b32 v219,0
v_mov_b32 v32,0
v_mov_b32 v33,0
v_mov_b32 v34,0
v_mov_b32 v35,0
SM s50,0xff800000
v_mov_b32 v220,0
v_mov_b32 v221,0
)"
R"(v_mov_b32 v222,0
v_mov_b32 v223,0
v_mov_b32 v36,0
v_mov_b32 v37,0
v_mov_b32 v38,0
v_mov_b32 v39,0
SM s51,0xff800000
AW a0,0
)"
R"(AW a1,0
AW a2,0
AW a3,0
AW a4,0
AW a5,0
AW a6,0
AW a7,0
AW a8,0
)"
R"(AW a9,0
AW a10,0
AW a11,0
AW a12,0
AW a13,0
AW a14,0
AW a15,0
AW a16,0
)"
R"(AW a17,0
AW a18,0
AW a19,0
AW a20,0
AW a21,0
AW a22,0
AW a23,0
AW a24,0
)"
R"(AW a25,0
AW a26,0
AW a27,0
AW a28,0
AW a29,0
AW a30,0
AW a31,0
AW a32,0
)"
R"(AW a33,0
AW a34,0
AW a35,0
AW a36,0
AW a37,0
AW a38,0
AW a39,0
AW a40,0
)"
R"(AW a41,0
AW a42,0
AW a43,0
AW a44,0
AW a45,0
AW a46,0
AW a47,0
AW a48,0
)"
R"(AW a49,0
AW a50,0
AW a51,0
AW a52,0
AW a53,0
AW a54,0
AW a55,0
AW a56,0
)"
R"(AW a57,0
AW a58,0
AW a59,0
AW a60,0
AW a61,0
AW a62,0
AW a63,0
AW a64,0
)"
R"(AW a65,0
AW a66,0
AW a67,0
AW a68,0
AW a69,0
AW a70,0
AW a71,0
AW a72,0
)"
R"(AW a73,0
AW a74,0
AW a75,0
AW a76,0
AW a77,0
AW a78,0
AW a79,0
AW a80,0
)"
R"(AW a81,0
AW a82,0
AW a83,0
AW a84,0
AW a85,0
AW a86,0
AW a87,0
AW a88,0
)"
R"(AW a89,0
AW a90,0
AW a91,0
AW a92,0
AW a93,0
AW a94,0
AW a95,0
AW a96,0
)"
R"(AW a97,0
AW a98,0
AW a99,0
AW a100,0
AW a101,0
AW a102,0
AW a103,0
AW a104,0
)"
R"(AW a105,0
AW a106,0
AW a107,0
AW a108,0
AW a109,0
AW a110,0
AW a111,0
AW a112,0
)"
R"(AW a113,0
AW a114,0
AW a115,0
AW a116,0
AW a117,0
AW a118,0
AW a119,0
AW a120,0
)"
R"(AW a121,0
AW a122,0
AW a123,0
AW a124,0
AW a125,0
AW a126,0
AW a127,0
W vmcnt(0)
)"
R"(s_barrier
R v[88:91],v1 offset:0
R v[92:95],v1 offset:1024
R v[96:99],v1 offset:2048
R v[100:103],v1 offset:3072
R v[104:107],v1 offset:4096
R v[108:111],v1 offset:5120
R v[112:115],v1 offset:6144
)"
R"(R v[116:119],v1 offset:7168
R v[120:123],v1 offset:8192
R v[124:127],v1 offset:9216
R v[128:131],v1 offset:10240
R v[132:135],v1 offset:11264
R v[136:139],v1 offset:12288
R v[140:143],v1 offset:13312
R v[144:147],v1 offset:14336
)"
R"(R v[148:151],v1 offset:15360
s_cmp_eq_u32 s41,0
s_cbranch_scc1 T0M_%=
W lgkmcnt(12)
M v[40:43],v[88:91],a[128:131],v[24:27]
M v[40:43],v[92:95],a[132:135],v[40:43]
M v[40:43],v[96:99],a[136:139],v[40:43]
M v[40:43],v[100:103],a[140:143],v[40:43]
)"
R"(W lgkmcnt(8)
M v[44:47],v[104:107],a[128:131],v[24:27]
M v[44:47],v[108:111],a[132:135],v[44:47]
M v[44:47],v[112:115],a[136:139],v[44:47]
M v[44:47],v[116:119],a[140:143],v[44:47]
W lgkmcnt(4)
M v[48:51],v[120:123],a[128:131],v[24:27]
M v[48:51],v[124:127],a[132:135],v[48:51]
)"
R"(M v[48:51],v[128:131],a[136:139],v[48:51]
M v[48:51],v[132:135],a[140:143],v[48:51]
W lgkmcnt(0)
M v[52:55],v[136:139],a[128:131],v[24:27]
M v[52:55],v[140:143],a[132:135],v[52:55]
M v[52:55],v[144:147],a[136:139],v[52:55]
M v[52:55],v[148:151],a[140:143],v[52:55]
W lgkmcnt(0)
)"
R"(M v[56:59],v[88:91],a[144:147],v[28:31]
R a[192:195],v2 offset:0
MX v12,v40,v41,v42
MX v12,v12,v43,v44
M v[56:59],v[92:95],a[148:151],v[56:59]
R a[196:199],v2 offset:1024
MX v12,v12,v45,v46
MX v12,v12,v47,v48
)"
R"(M v[56:59],v[96:99],a[152:155],v[56:59]
R a[200:203],v2 offset:2048
MX v12,v12,v49,v50
MX v12,v12,v51,v52
M v[56:59],v[100:103],a[156:159],v[56:59]
R a[204:207],v2 offset:3072
MX v12,v12,v53,v54
v_max_f32 v12,v12,v55
)"
R"(M v[60:63],v[104:107],a[144:147],v[28:31]
R a[208:211],v2 offset:4096
v_cmp_lt_f32 vcc,s48,v12
s_cbranch_vccnz TR9_%=
BK9_%=:
M v[60:63],v[108:111],a[148:151],v[60:63]
R a[212:215],v2 offset:5120
EX v40,v40
)"
R"(M v[60:63],v[112:115],a[152:155],v[60:63]
R a[216:219],v2 offset:6144
EX v41,v41
M v[60:63],v[116:119],a[156:159],v[60:63]
R a[220:223],v2 offset:7168
EX v42,v42
M v[64:67],v[120:123],a[144:147],v[28:31]
R a[224:227],v2 offset:8192
)"
R"(CV v72,v40,v41
M v[64:67],v[124:127],a[148:151],v[64:67]
R a[228:231],v2 offset:9216
EX v43,v43
M v[64:67],v[128:131],a[152:155],v[64:67]
R a[232:235],v2 offset:10240
EX v44,v44
M v[64:67],v[132:135],a[156:159],v[64:67]
)"
R"(R a[236:239],v2 offset:11264
CV v73,v42,v43
M v[68:71],v[136:139],a[144:147],v[28:31]
R a[240:243],v2 offset:12288
EX v45,v45
M v[68:71],v[140:143],a[148:151],v[68:71]
R a[244:247],v2 offset:13312
EX v46,v46
)"
R"(M v[68:71],v[144:147],a[152:155],v[68:71]
R a[248:251],v2 offset:14336
CV v74,v44,v45
M v[68:71],v[148:151],a[156:159],v[68:71]
R a[252:255],v2 offset:15360
EX v47,v47
EX v48,v48
CV v75,v46,v47
)"
R"(EX v49,v49
EX v50,v50
CV v76,v48,v49
EX v51,v51
s_nop 0
CV v77,v50,v51
M v[40:43],v[88:91],a[160:163],v[32:35]
MX v12,v56,v57,v58
)"
R"(MX v12,v12,v59,v60
MX v12,v12,v61,v62
W lgkmcnt(14)
M a[0:3],a[192:195],v[72:75],a[0:3]
MX v12,v12,v63,v64
MX v12,v12,v65,v66
M v[40:43],v[92:95],a[164:167],v[40:43]
MX v12,v12,v67,v68
)"
R"(M a[4:7],a[196:199],v[72:75],a[4:7]
MX v12,v12,v69,v70
v_max_f32 v12,v12,v71
M v[40:43],v[96:99],a[168:171],v[40:43]
SA s53,s53,0x4000
s_and_b32 s53,s53,0xc000
v_add_u32 v2,s53,v4
W lgkmcnt(12)
)"
R"(M a[8:11],a[200:203],v[72:75],a[8:11]
v_cmp_lt_f32 vcc,s49,v12
EX v52,v52
M v[40:43],v[100:103],a[172:175],v[40:43]
EX v53,v53
M a[12:15],a[204:207],v[72:75],a[12:15]
M v[44:47],v[104:107],a[160:163],v[32:35]
EX v54,v54
)"
R"(W lgkmcnt(10)
M a[16:19],a[208:211],v[72:75],a[16:19]
CV v78,v52,v53
M v[44:47],v[108:111],a[164:167],v[44:47]
EX v55,v55
M a[20:23],a[212:215],v[72:75],a[20:23]
CV v79,v54,v55
s_cbranch_vccnz TR10_%=
)"
R"(BK10_%=:
M v[44:47],v[112:115],a[168:171],v[44:47]
W lgkmcnt(8)
M a[24:27],a[216:219],v[72:75],a[24:27]
M v[44:47],v[116:119],a[172:175],v[44:47]
EX v56,v56
M a[28:31],a[220:223],v[72:75],a[28:31]
EX v57,v57
)"
R"(M v[48:51],v[120:123],a[160:163],v[32:35]
EX v58,v58
M v[208:211],v[224:227],v[72:75],v[208:211]
M v[48:51],v[124:127],a[164:167],v[48:51]
CV v80,v56,v57
W lgkmcnt(6)
M a[0:3],a[224:227],v[76:79],a[0:3]
EX v59,v59
)"
R"(M v[48:51],v[128:131],a[168:171],v[48:51]
SA s52,s52,0x4000
s_and_b32 s52,s52,0xc000
v_add_u32 v1,s52,v3
M a[4:7],a[228:231],v[76:79],a[4:7]
EX v60,v60
CV v81,v58,v59
M v[48:51],v[132:135],a[172:175],v[48:51]
)"
R"(EX v61,v61
W lgkmcnt(4)
M a[8:11],a[232:235],v[76:79],a[8:11]
EX v62,v62
M v[52:55],v[136:139],a[160:163],v[32:35]
CV v82,v60,v61
M a[12:15],a[236:239],v[76:79],a[12:15]
EX v63,v63
)"
R"(M v[52:55],v[140:143],a[164:167],v[52:55]
W lgkmcnt(2)
M a[16:19],a[240:243],v[76:79],a[16:19]
EX v64,v64
M v[52:55],v[144:147],a[168:171],v[52:55]
CV v83,v62,v63
M a[20:23],a[244:247],v[76:79],a[20:23]
EX v65,v65
)"
R"(M v[52:55],v[148:151],a[172:175],v[52:55]
EX v66,v66
W lgkmcnt(0)
M a[24:27],a[248:251],v[76:79],a[24:27]
CV v84,v64,v65
M a[28:31],a[252:255],v[76:79],a[28:31]
EX v67,v67
M v[208:211],v[224:227],v[76:79],v[208:211]
)"
R"(CV v85,v66,v67
W vmcnt(8) lgkmcnt(0)
s_barrier
M v[56:59],v[88:91],a[176:179],v[36:39]
R v[88:91],v1 offset:0
MX v12,v40,v41,v42
MX v12,v12,v43,v44
M a[32:35],a[192:195],v[80:83],a[32:35]
)"
R"(SM m0,s54
s_nop 0
B v0,s[60:63],s43 offen offset:0 sc1 lds
M v[56:59],v[92:95],a[180:183],v[56:59]
R v[92:95],v1 offset:1024
MX v12,v12,v45,v46
MX v12,v12,v47,v48
M a[36:39],a[196:199],v[80:83],a[36:39]
)"
R"(MX v12,v12,v49,v50
MX v12,v12,v51,v52
MX v12,v12,v53,v54
M v[56:59],v[96:99],a[184:187],v[56:59]
R v[96:99],v1 offset:2048
v_max_f32 v12,v12,v55
v_cmp_lt_f32 vcc,s50,v12
M a[40:43],a[200:203],v[80:83],a[40:43]
)"
R"(EX v68,v68
M v[56:59],v[100:103],a[188:191],v[56:59]
R v[100:103],v1 offset:3072
EX v69,v69
M a[44:47],a[204:207],v[80:83],a[44:47]
M v[60:63],v[104:107],a[176:179],v[36:39]
R v[104:107],v1 offset:4096
EX v70,v70
)"
R"(M a[48:51],a[208:211],v[80:83],a[48:51]
CV v86,v68,v69
M v[60:63],v[108:111],a[180:183],v[60:63]
R v[108:111],v1 offset:5120
EX v71,v71
M a[52:55],a[212:215],v[80:83],a[52:55]
CV v87,v70,v71
s_cbranch_vccnz TR11_%=
)"
R"(BK11_%=:
M v[60:63],v[112:115],a[184:187],v[60:63]
R v[112:115],v1 offset:6144
M a[56:59],a[216:219],v[80:83],a[56:59]
M v[60:63],v[116:119],a[188:191],v[60:63]
R v[116:119],v1 offset:7168
EX v40,v40
M a[60:63],a[220:223],v[80:83],a[60:63]
)"
R"(EX v41,v41
M v[64:67],v[120:123],a[176:179],v[36:39]
R v[120:123],v1 offset:8192
EX v42,v42
M v[212:215],v[224:227],v[80:83],v[212:215]
M v[64:67],v[124:127],a[180:183],v[64:67]
R v[124:127],v1 offset:9216
B v0,s[60:63],s43 offen offset:1024 sc1 lds
)"
R"(CV v72,v40,v41
M a[32:35],a[224:227],v[84:87],a[32:35]
EX v43,v43
M v[64:67],v[128:131],a[184:187],v[64:67]
R v[128:131],v1 offset:10240
EX v44,v44
M a[36:39],a[228:231],v[84:87],a[36:39]
CV v73,v42,v43
)"
R"(M v[64:67],v[132:135],a[188:191],v[64:67]
R v[132:135],v1 offset:11264
EX v45,v45
M a[40:43],a[232:235],v[84:87],a[40:43]
EX v46,v46
M v[68:71],v[136:139],a[176:179],v[36:39]
R v[136:139],v1 offset:12288
CV v74,v44,v45
)"
R"(M a[44:47],a[236:239],v[84:87],a[44:47]
EX v47,v47
M v[68:71],v[140:143],a[180:183],v[68:71]
R v[140:143],v1 offset:13312
M a[48:51],a[240:243],v[84:87],a[48:51]
EX v48,v48
M v[68:71],v[144:147],a[184:187],v[68:71]
R v[144:147],v1 offset:14336
)"
R"(CV v75,v46,v47
M a[52:55],a[244:247],v[84:87],a[52:55]
EX v49,v49
M v[68:71],v[148:151],a[188:191],v[68:71]
R v[148:151],v1 offset:15360
SA s40,s40,1
M a[56:59],a[248:251],v[84:87],a[56:59]
EX v50,v50
)"
R"(CV v76,v48,v49
M a[60:63],a[252:255],v[84:87],a[60:63]
EX v51,v51
M v[212:215],v[224:227],v[84:87],v[212:215]
CV v77,v50,v51
s_cmp_ge_u32 s40,s41
s_cbranch_scc1 DM_%=
DLB_%=:
)"
R"(W lgkmcnt(14)
M v[40:43],v[88:91],a[128:131],v[24:27]
MX v12,v56,v57,v58
MX v12,v12,v59,v60
MX v12,v12,v61,v62
M a[64:67],a[192:195],v[72:75],a[64:67]
B v0,s[60:63],s43 offen offset:2048 sc1 lds
MX v12,v12,v63,v64
)"
R"(MX v12,v12,v65,v66
M v[40:43],v[92:95],a[132:135],v[40:43]
MX v12,v12,v67,v68
M a[68:71],a[196:199],v[72:75],a[68:71]
MX v12,v12,v69,v70
v_max_f32 v12,v12,v71
W lgkmcnt(12)
M v[40:43],v[96:99],a[136:139],v[40:43]
)"
R"(v_cmp_lt_f32 vcc,s51,v12
M a[72:75],a[200:203],v[72:75],a[72:75]
EX v52,v52
M v[40:43],v[100:103],a[140:143],v[40:43]
EX v53,v53
M a[76:79],a[204:207],v[72:75],a[76:79]
W lgkmcnt(10)
M v[44:47],v[104:107],a[128:131],v[24:27]
)"
R"(EX v54,v54
M a[80:83],a[208:211],v[72:75],a[80:83]
CV v78,v52,v53
M v[44:47],v[108:111],a[132:135],v[44:47]
EX v55,v55
M a[84:87],a[212:215],v[72:75],a[84:87]
CV v79,v54,v55
s_cbranch_vccnz TR12_%=
)"
R"(BK12_%=:
W lgkmcnt(8)
M v[44:47],v[112:115],a[136:139],v[44:47]
M a[88:91],a[216:219],v[72:75],a[88:91]
M v[44:47],v[116:119],a[140:143],v[44:47]
EX v56,v56
M a[92:95],a[220:223],v[72:75],a[92:95]
EX v57,v57
)"
R"(W lgkmcnt(6)
M v[48:51],v[120:123],a[128:131],v[24:27]
EX v58,v58
M v[216:219],v[224:227],v[72:75],v[216:219]
M v[48:51],v[124:127],a[132:135],v[48:51]
B v0,s[60:63],s43 offen offset:3072 sc1 lds
CV v80,v56,v57
M a[64:67],a[224:227],v[76:79],a[64:67]
)"
R"(EX v59,v59
W lgkmcnt(4)
M v[48:51],v[128:131],a[136:139],v[48:51]
EX v60,v60
M a[68:71],a[228:231],v[76:79],a[68:71]
CV v81,v58,v59
M v[48:51],v[132:135],a[140:143],v[48:51]
EX v61,v61
)"
R"(M a[72:75],a[232:235],v[76:79],a[72:75]
EX v62,v62
W lgkmcnt(2)
M v[52:55],v[136:139],a[128:131],v[24:27]
CV v82,v60,v61
M a[76:79],a[236:239],v[76:79],a[76:79]
EX v63,v63
M v[52:55],v[140:143],a[132:135],v[52:55]
)"
R"(SA s43,s43,0x4000
s_and_b32 s46,s43,0xc000
SA s54,s44,s46
M a[80:83],a[240:243],v[76:79],a[80:83]
EX v64,v64
W lgkmcnt(0)
M v[52:55],v[144:147],a[136:139],v[52:55]
CV v83,v62,v63
)"
R"(M a[84:87],a[244:247],v[76:79],a[84:87]
EX v65,v65
M v[52:55],v[148:151],a[140:143],v[52:55]
EX v66,v66
M a[88:91],a[248:251],v[76:79],a[88:91]
CV v84,v64,v65
M a[92:95],a[252:255],v[76:79],a[92:95]
EX v67,v67
)"
R"(M v[216:219],v[224:227],v[76:79],v[216:219]
CV v85,v66,v67
M v[56:59],v[88:91],a[144:147],v[28:31]
MX v12,v40,v41,v42
MX v12,v12,v43,v44
MX v12,v12,v45,v46
M a[96:99],a[192:195],v[80:83],a[96:99]
R a[192:195],v2 offset:0
)"
R"(SM m0,s55
s_nop 0
B v0,s[64:67],s47 offen offset:0 sc1 lds
M v[56:59],v[92:95],a[148:151],v[56:59]
MX v12,v12,v47,v48
MX v12,v12,v49,v50
MX v12,v12,v51,v52
M a[100:103],a[196:199],v[80:83],a[100:103]
)"
R"(R a[196:199],v2 offset:1024
MX v12,v12,v53,v54
v_max_f32 v12,v12,v55
M v[56:59],v[96:99],a[152:155],v[56:59]
v_cmp_lt_f32 vcc,s48,v12
M a[104:107],a[200:203],v[80:83],a[104:107]
R a[200:203],v2 offset:2048
EX v68,v68
)"
R"(M v[56:59],v[100:103],a[156:159],v[56:59]
EX v69,v69
M a[108:111],a[204:207],v[80:83],a[108:111]
R a[204:207],v2 offset:3072
M v[60:63],v[104:107],a[144:147],v[28:31]
EX v70,v70
M a[112:115],a[208:211],v[80:83],a[112:115]
R a[208:211],v2 offset:4096
)"
R"(CV v86,v68,v69
M v[60:63],v[108:111],a[148:151],v[60:63]
EX v71,v71
M a[116:119],a[212:215],v[80:83],a[116:119]
R a[212:215],v2 offset:5120
CV v87,v70,v71
s_cbranch_vccnz TR13_%=
BK13_%=:
)"
R"(M v[60:63],v[112:115],a[152:155],v[60:63]
M a[120:123],a[216:219],v[80:83],a[120:123]
R a[216:219],v2 offset:6144
M v[60:63],v[116:119],a[156:159],v[60:63]
EX v40,v40
M a[124:127],a[220:223],v[80:83],a[124:127]
R a[220:223],v2 offset:7168
EX v41,v41
)"
R"(M v[64:67],v[120:123],a[144:147],v[28:31]
EX v42,v42
M v[220:223],v[224:227],v[80:83],v[220:223]
M v[64:67],v[124:127],a[148:151],v[64:67]
B v0,s[64:67],s47 offen offset:1024 sc1 lds
CV v72,v40,v41
M a[96:99],a[224:227],v[84:87],a[96:99]
R a[224:227],v2 offset:8192
)"
R"(EX v43,v43
M v[64:67],v[128:131],a[152:155],v[64:67]
EX v44,v44
M a[100:103],a[228:231],v[84:87],a[100:103]
R a[228:231],v2 offset:9216
CV v73,v42,v43
M v[64:67],v[132:135],a[156:159],v[64:67]
EX v45,v45
)"
R"(M a[104:107],a[232:235],v[84:87],a[104:107]
R a[232:235],v2 offset:10240
EX v46,v46
M v[68:71],v[136:139],a[144:147],v[28:31]
CV v74,v44,v45
M a[108:111],a[236:239],v[84:87],a[108:111]
R a[236:239],v2 offset:11264
EX v47,v47
)"
R"(M v[68:71],v[140:143],a[148:151],v[68:71]
M a[112:115],a[240:243],v[84:87],a[112:115]
R a[240:243],v2 offset:12288
EX v48,v48
M v[68:71],v[144:147],a[152:155],v[68:71]
CV v75,v46,v47
M a[116:119],a[244:247],v[84:87],a[116:119]
R a[244:247],v2 offset:13312
)"
R"(EX v49,v49
M v[68:71],v[148:151],a[156:159],v[68:71]
EX v50,v50
M a[120:123],a[248:251],v[84:87],a[120:123]
R a[248:251],v2 offset:14336
CV v76,v48,v49
M a[124:127],a[252:255],v[84:87],a[124:127]
R a[252:255],v2 offset:15360
)"
R"(EX v51,v51
M v[220:223],v[224:227],v[84:87],v[220:223]
CV v77,v50,v51
M v[40:43],v[88:91],a[160:163],v[32:35]
MX v12,v56,v57,v58
MX v12,v12,v59,v60
MX v12,v12,v61,v62
W lgkmcnt(14)
)"
R"(M a[0:3],a[192:195],v[72:75],a[0:3]
B v0,s[64:67],s47 offen offset:2048 sc1 lds
MX v12,v12,v63,v64
MX v12,v12,v65,v66
M v[40:43],v[92:95],a[164:167],v[40:43]
MX v12,v12,v67,v68
M a[4:7],a[196:199],v[72:75],a[4:7]
MX v12,v12,v69,v70
)"
R"(v_max_f32 v12,v12,v71
M v[40:43],v[96:99],a[168:171],v[40:43]
SA s53,s53,0x4000
s_and_b32 s53,s53,0xc000
v_add_u32 v2,s53,v4
W lgkmcnt(12)
M a[8:11],a[200:203],v[72:75],a[8:11]
v_cmp_lt_f32 vcc,s49,v12
)"
R"(EX v52,v52
M v[40:43],v[100:103],a[172:175],v[40:43]
EX v53,v53
M a[12:15],a[204:207],v[72:75],a[12:15]
M v[44:47],v[104:107],a[160:163],v[32:35]
EX v54,v54
W lgkmcnt(10)
M a[16:19],a[208:211],v[72:75],a[16:19]
)"
R"(CV v78,v52,v53
M v[44:47],v[108:111],a[164:167],v[44:47]
EX v55,v55
M a[20:23],a[212:215],v[72:75],a[20:23]
CV v79,v54,v55
s_cbranch_vccnz TR14_%=
BK14_%=:
M v[44:47],v[112:115],a[168:171],v[44:47]
)"
R"(W lgkmcnt(8)
M a[24:27],a[216:219],v[72:75],a[24:27]
M v[44:47],v[116:119],a[172:175],v[44:47]
EX v56,v56
M a[28:31],a[220:223],v[72:75],a[28:31]
EX v57,v57
M v[48:51],v[120:123],a[160:163],v[32:35]
EX v58,v58
)"
R"(M v[208:211],v[224:227],v[72:75],v[208:211]
M v[48:51],v[124:127],a[164:167],v[48:51]
B v0,s[64:67],s47 offen offset:3072 sc1 lds
CV v80,v56,v57
W lgkmcnt(6)
M a[0:3],a[224:227],v[76:79],a[0:3]
EX v59,v59
M v[48:51],v[128:131],a[168:171],v[48:51]
)"
R"(SA s52,s52,0x4000
s_and_b32 s52,s52,0xc000
v_add_u32 v1,s52,v3
M a[4:7],a[228:231],v[76:79],a[4:7]
EX v60,v60
CV v81,v58,v59
M v[48:51],v[132:135],a[172:175],v[48:51]
EX v61,v61
)"
R"(W lgkmcnt(4)
M a[8:11],a[232:235],v[76:79],a[8:11]
EX v62,v62
M v[52:55],v[136:139],a[160:163],v[32:35]
CV v82,v60,v61
M a[12:15],a[236:239],v[76:79],a[12:15]
EX v63,v63
M v[52:55],v[140:143],a[164:167],v[52:55]
)"
R"(W lgkmcnt(2)
M a[16:19],a[240:243],v[76:79],a[16:19]
EX v64,v64
M v[52:55],v[144:147],a[168:171],v[52:55]
SA s47,s47,0x4000
s_xor_b32 s58,s47,0x8000
s_and_b32 s58,s58,0xc000
SA s55,s45,s58
)"
R"(M a[20:23],a[244:247],v[76:79],a[20:23]
CV v83,v62,v63
EX v65,v65
M v[52:55],v[148:151],a[172:175],v[52:55]
EX v66,v66
W lgkmcnt(0)
M a[24:27],a[248:251],v[76:79],a[24:27]
CV v84,v64,v65
)"
R"(M a[28:31],a[252:255],v[76:79],a[28:31]
EX v67,v67
M v[208:211],v[224:227],v[76:79],v[208:211]
CV v85,v66,v67
W vmcnt(8) lgkmcnt(0)
s_barrier
M v[56:59],v[88:91],a[176:179],v[36:39]
R v[88:91],v1 offset:0
)"
R"(MX v12,v40,v41,v42
MX v12,v12,v43,v44
M a[32:35],a[192:195],v[80:83],a[32:35]
SM m0,s54
s_nop 0
B v0,s[60:63],s43 offen offset:0 sc1 lds
M v[56:59],v[92:95],a[180:183],v[56:59]
R v[92:95],v1 offset:1024
)"
R"(MX v12,v12,v45,v46
MX v12,v12,v47,v48
M a[36:39],a[196:199],v[80:83],a[36:39]
MX v12,v12,v49,v50
MX v12,v12,v51,v52
MX v12,v12,v53,v54
M v[56:59],v[96:99],a[184:187],v[56:59]
R v[96:99],v1 offset:2048
)"
R"(v_max_f32 v12,v12,v55
v_cmp_lt_f32 vcc,s50,v12
M a[40:43],a[200:203],v[80:83],a[40:43]
EX v68,v68
M v[56:59],v[100:103],a[188:191],v[56:59]
R v[100:103],v1 offset:3072
EX v69,v69
M a[44:47],a[204:207],v[80:83],a[44:47]
)"
R"(M v[60:63],v[104:107],a[176:179],v[36:39]
R v[104:107],v1 offset:4096
EX v70,v70
M a[48:51],a[208:211],v[80:83],a[48:51]
CV v86,v68,v69
M v[60:63],v[108:111],a[180:183],v[60:63]
R v[108:111],v1 offset:5120
EX v71,v71
)"
R"(M a[52:55],a[212:215],v[80:83],a[52:55]
CV v87,v70,v71
s_cbranch_vccnz TR15_%=
BK15_%=:
M v[60:63],v[112:115],a[184:187],v[60:63]
R v[112:115],v1 offset:6144
M a[56:59],a[216:219],v[80:83],a[56:59]
M v[60:63],v[116:119],a[188:191],v[60:63]
)"
R"(R v[116:119],v1 offset:7168
EX v40,v40
M a[60:63],a[220:223],v[80:83],a[60:63]
EX v41,v41
M v[64:67],v[120:123],a[176:179],v[36:39]
R v[120:123],v1 offset:8192
EX v42,v42
M v[212:215],v[224:227],v[80:83],v[212:215]
)"
R"(M v[64:67],v[124:127],a[180:183],v[64:67]
R v[124:127],v1 offset:9216
B v0,s[60:63],s43 offen offset:1024 sc1 lds
CV v72,v40,v41
M a[32:35],a[224:227],v[84:87],a[32:35]
EX v43,v43
M v[64:67],v[128:131],a[184:187],v[64:67]
R v[128:131],v1 offset:10240
)"
R"(EX v44,v44
M a[36:39],a[228:231],v[84:87],a[36:39]
CV v73,v42,v43
M v[64:67],v[132:135],a[188:191],v[64:67]
R v[132:135],v1 offset:11264
EX v45,v45
M a[40:43],a[232:235],v[84:87],a[40:43]
EX v46,v46
)"
R"(M v[68:71],v[136:139],a[176:179],v[36:39]
R v[136:139],v1 offset:12288
CV v74,v44,v45
M a[44:47],a[236:239],v[84:87],a[44:47]
EX v47,v47
M v[68:71],v[140:143],a[180:183],v[68:71]
R v[140:143],v1 offset:13312
M a[48:51],a[240:243],v[84:87],a[48:51]
)"
R"(EX v48,v48
M v[68:71],v[144:147],a[184:187],v[68:71]
R v[144:147],v1 offset:14336
CV v75,v46,v47
M a[52:55],a[244:247],v[84:87],a[52:55]
EX v49,v49
M v[68:71],v[148:151],a[188:191],v[68:71]
R v[148:151],v1 offset:15360
)"
R"(SA s40,s40,1
M a[56:59],a[248:251],v[84:87],a[56:59]
EX v50,v50
CV v76,v48,v49
M a[60:63],a[252:255],v[84:87],a[60:63]
EX v51,v51
M v[212:215],v[224:227],v[84:87],v[212:215]
CV v77,v50,v51
)"
R"(s_cmp_lt_u32 s40,s41
s_cbranch_scc1 DLB_%=
DM_%=:
W lgkmcnt(14)
M v[40:43],v[88:91],a[128:131],v[24:27]
MX v12,v56,v57,v58
MX v12,v12,v59,v60
MX v12,v12,v61,v62
)"
R"(M a[64:67],a[192:195],v[72:75],a[64:67]
B v0,s[60:63],s43 offen offset:2048 sc1 lds
MX v12,v12,v63,v64
MX v12,v12,v65,v66
M v[40:43],v[92:95],a[132:135],v[40:43]
MX v12,v12,v67,v68
M a[68:71],a[196:199],v[72:75],a[68:71]
MX v12,v12,v69,v70
)"
R"(v_max_f32 v12,v12,v71
W lgkmcnt(12)
M v[40:43],v[96:99],a[136:139],v[40:43]
v_cmp_lt_f32 vcc,s51,v12
M a[72:75],a[200:203],v[72:75],a[72:75]
EX v52,v52
M v[40:43],v[100:103],a[140:143],v[40:43]
EX v53,v53
)"
R"(M a[76:79],a[204:207],v[72:75],a[76:79]
W lgkmcnt(10)
M v[44:47],v[104:107],a[128:131],v[24:27]
EX v54,v54
M a[80:83],a[208:211],v[72:75],a[80:83]
CV v78,v52,v53
M v[44:47],v[108:111],a[132:135],v[44:47]
EX v55,v55
)"
R"(M a[84:87],a[212:215],v[72:75],a[84:87]
CV v79,v54,v55
s_cbranch_vccnz TR16_%=
BK16_%=:
W lgkmcnt(8)
M v[44:47],v[112:115],a[136:139],v[44:47]
M a[88:91],a[216:219],v[72:75],a[88:91]
M v[44:47],v[116:119],a[140:143],v[44:47]
)"
R"(EX v56,v56
M a[92:95],a[220:223],v[72:75],a[92:95]
EX v57,v57
W lgkmcnt(6)
M v[48:51],v[120:123],a[128:131],v[24:27]
EX v58,v58
M v[216:219],v[224:227],v[72:75],v[216:219]
M v[48:51],v[124:127],a[132:135],v[48:51]
)"
R"(B v0,s[60:63],s43 offen offset:3072 sc1 lds
CV v80,v56,v57
M a[64:67],a[224:227],v[76:79],a[64:67]
EX v59,v59
W lgkmcnt(4)
M v[48:51],v[128:131],a[136:139],v[48:51]
EX v60,v60
M a[68:71],a[228:231],v[76:79],a[68:71]
)"
R"(CV v81,v58,v59
M v[48:51],v[132:135],a[140:143],v[48:51]
EX v61,v61
M a[72:75],a[232:235],v[76:79],a[72:75]
EX v62,v62
W lgkmcnt(2)
M v[52:55],v[136:139],a[128:131],v[24:27]
CV v82,v60,v61
)"
R"(M a[76:79],a[236:239],v[76:79],a[76:79]
EX v63,v63
M v[52:55],v[140:143],a[132:135],v[52:55]
SA s43,s43,0x4000
s_and_b32 s46,s43,0xc000
SA s54,s44,s46
M a[80:83],a[240:243],v[76:79],a[80:83]
EX v64,v64
)"
R"(W lgkmcnt(0)
M v[52:55],v[144:147],a[136:139],v[52:55]
CV v83,v62,v63
M a[84:87],a[244:247],v[76:79],a[84:87]
EX v65,v65
M v[52:55],v[148:151],a[140:143],v[52:55]
EX v66,v66
M a[88:91],a[248:251],v[76:79],a[88:91]
)"
R"(CV v84,v64,v65
M a[92:95],a[252:255],v[76:79],a[92:95]
EX v67,v67
M v[216:219],v[224:227],v[76:79],v[216:219]
CV v85,v66,v67
M v[56:59],v[88:91],a[144:147],v[28:31]
AD v40,v40,v152
AD v41,v41,v153
)"
R"(AD v42,v42,v154
M a[96:99],a[192:195],v[80:83],a[96:99]
R a[192:195],v2 offset:0
SM m0,s55
s_nop 0
B v0,s[64:67],s47 offen offset:0 sc1 lds
M v[56:59],v[92:95],a[148:151],v[56:59]
AD v43,v43,v155
)"
R"(AD v44,v44,v156
AD v45,v45,v157
M a[100:103],a[196:199],v[80:83],a[100:103]
R a[196:199],v2 offset:1024
AD v46,v46,v158
AD v47,v47,v159
M v[56:59],v[96:99],a[152:155],v[56:59]
AD v48,v48,v160
)"
R"(AD v49,v49,v161
AD v50,v50,v162
M a[104:107],a[200:203],v[80:83],a[104:107]
R a[200:203],v2 offset:2048
AD v51,v51,v163
AD v52,v52,v164
M v[56:59],v[100:103],a[156:159],v[56:59]
AD v53,v53,v165
)"
R"(AD v54,v54,v166
AD v55,v55,v167
M a[108:111],a[204:207],v[80:83],a[108:111]
R a[204:207],v2 offset:3072
MX v12,v40,v41,v42
MX v12,v12,v43,v44
M v[60:63],v[104:107],a[144:147],v[28:31]
MX v12,v12,v45,v46
)"
R"(M a[112:115],a[208:211],v[80:83],a[112:115]
R a[208:211],v2 offset:4096
MX v12,v12,v47,v48
MX v12,v12,v49,v50
M v[60:63],v[108:111],a[148:151],v[60:63]
MX v12,v12,v51,v52
MX v12,v12,v53,v54
M a[116:119],a[212:215],v[80:83],a[116:119]
)"
R"(R a[212:215],v2 offset:5120
v_max_f32 v12,v12,v55
v_cmp_lt_f32 vcc,s48,v12
M v[60:63],v[112:115],a[152:155],v[60:63]
M a[120:123],a[216:219],v[80:83],a[120:123]
R a[216:219],v2 offset:6144
EX v68,v68
M v[60:63],v[116:119],a[156:159],v[60:63]
)"
R"(EX v69,v69
M a[124:127],a[220:223],v[80:83],a[124:127]
R a[220:223],v2 offset:7168
EX v70,v70
M v[64:67],v[120:123],a[144:147],v[28:31]
CV v86,v68,v69
EX v71,v71
M v[220:223],v[224:227],v[80:83],v[220:223]
)"
R"(CV v87,v70,v71
M v[64:67],v[124:127],a[148:151],v[64:67]
B v0,s[64:67],s47 offen offset:1024 sc1 lds
s_cbranch_vccnz TR17_%=
BK17_%=:
M a[96:99],a[224:227],v[84:87],a[96:99]
R a[224:227],v2 offset:8192
EX v40,v40
)"
R"(M v[64:67],v[128:131],a[152:155],v[64:67]
EX v41,v41
M a[100:103],a[228:231],v[84:87],a[100:103]
R a[228:231],v2 offset:9216
EX v42,v42
M v[64:67],v[132:135],a[156:159],v[64:67]
CV v72,v40,v41
EX v43,v43
)"
R"(M a[104:107],a[232:235],v[84:87],a[104:107]
R a[232:235],v2 offset:10240
EX v44,v44
M v[68:71],v[136:139],a[144:147],v[28:31]
CV v73,v42,v43
M a[108:111],a[236:239],v[84:87],a[108:111]
R a[236:239],v2 offset:11264
EX v45,v45
)"
R"(M v[68:71],v[140:143],a[148:151],v[68:71]
EX v46,v46
M a[112:115],a[240:243],v[84:87],a[112:115]
R a[240:243],v2 offset:12288
CV v74,v44,v45
M v[68:71],v[144:147],a[152:155],v[68:71]
EX v47,v47
M a[116:119],a[244:247],v[84:87],a[116:119]
)"
R"(R a[244:247],v2 offset:13312
EX v48,v48
M v[68:71],v[148:151],a[156:159],v[68:71]
CV v75,v46,v47
EX v49,v49
M a[120:123],a[248:251],v[84:87],a[120:123]
R a[248:251],v2 offset:14336
EX v50,v50
)"
R"(M a[124:127],a[252:255],v[84:87],a[124:127]
R a[252:255],v2 offset:15360
CV v76,v48,v49
M v[220:223],v[224:227],v[84:87],v[220:223]
EX v51,v51
s_nop 0
CV v77,v50,v51
M v[40:43],v[88:91],a[160:163],v[32:35]
)"
R"(AD v56,v56,v168
AD v57,v57,v169
AD v58,v58,v170
W lgkmcnt(14)
M a[0:3],a[192:195],v[72:75],a[0:3]
B v0,s[64:67],s47 offen offset:2048 sc1 lds
AD v59,v59,v171
AD v60,v60,v172
)"
R"(M v[40:43],v[92:95],a[164:167],v[40:43]
AD v61,v61,v173
AD v62,v62,v174
AD v63,v63,v175
M a[4:7],a[196:199],v[72:75],a[4:7]
AD v64,v64,v176
AD v65,v65,v177
M v[40:43],v[96:99],a[168:171],v[40:43]
)"
R"(SA s53,s53,0x4000
s_and_b32 s53,s53,0xc000
v_add_u32 v2,s53,v4
W lgkmcnt(12)
M a[8:11],a[200:203],v[72:75],a[8:11]
AD v66,v66,v178
AD v67,v67,v179
AD v68,v68,v180
)"
R"(M v[40:43],v[100:103],a[172:175],v[40:43]
AD v69,v69,v181
AD v70,v70,v182
AD v71,v71,v183
M a[12:15],a[204:207],v[72:75],a[12:15]
MX v12,v56,v57,v58
MX v12,v12,v59,v60
M v[44:47],v[104:107],a[160:163],v[32:35]
)"
R"(MX v12,v12,v61,v62
W lgkmcnt(10)
M a[16:19],a[208:211],v[72:75],a[16:19]
MX v12,v12,v63,v64
MX v12,v12,v65,v66
M v[44:47],v[108:111],a[164:167],v[44:47]
MX v12,v12,v67,v68
MX v12,v12,v69,v70
)"
R"(M a[20:23],a[212:215],v[72:75],a[20:23]
v_max_f32 v12,v12,v71
v_cmp_lt_f32 vcc,s49,v12
M v[44:47],v[112:115],a[168:171],v[44:47]
W lgkmcnt(8)
M a[24:27],a[216:219],v[72:75],a[24:27]
EX v52,v52
M v[44:47],v[116:119],a[172:175],v[44:47]
)"
R"(EX v53,v53
M a[28:31],a[220:223],v[72:75],a[28:31]
EX v54,v54
M v[48:51],v[120:123],a[160:163],v[32:35]
CV v78,v52,v53
EX v55,v55
M v[208:211],v[224:227],v[72:75],v[208:211]
CV v79,v54,v55
)"
R"(M v[48:51],v[124:127],a[164:167],v[48:51]
B v0,s[64:67],s47 offen offset:3072 sc1 lds
s_cbranch_vccnz TR18_%=
BK18_%=:
W lgkmcnt(6)
M a[0:3],a[224:227],v[76:79],a[0:3]
EX v56,v56
M v[48:51],v[128:131],a[168:171],v[48:51]
)"
R"(SA s52,s52,0x4000
s_and_b32 s52,s52,0xc000
v_add_u32 v1,s52,v3
M a[4:7],a[228:231],v[76:79],a[4:7]
EX v57,v57
M v[48:51],v[132:135],a[172:175],v[48:51]
EX v58,v58
CV v80,v56,v57
)"
R"(W lgkmcnt(4)
M a[8:11],a[232:235],v[76:79],a[8:11]
EX v59,v59
M v[52:55],v[136:139],a[160:163],v[32:35]
EX v60,v60
CV v81,v58,v59
M a[12:15],a[236:239],v[76:79],a[12:15]
EX v61,v61
)"
R"(M v[52:55],v[140:143],a[164:167],v[52:55]
EX v62,v62
W lgkmcnt(2)
M a[16:19],a[240:243],v[76:79],a[16:19]
CV v82,v60,v61
EX v63,v63
M v[52:55],v[144:147],a[168:171],v[52:55]
SA s47,s47,0x4000
)"
R"(s_xor_b32 s58,s47,0x8000
s_and_b32 s58,s58,0xc000
SA s55,s45,s58
M a[20:23],a[244:247],v[76:79],a[20:23]
EX v64,v64
CV v83,v62,v63
M v[52:55],v[148:151],a[172:175],v[52:55]
EX v65,v65
)"
R"(W lgkmcnt(0)
M a[24:27],a[248:251],v[76:79],a[24:27]
EX v66,v66
M a[28:31],a[252:255],v[76:79],a[28:31]
CV v84,v64,v65
EX v67,v67
M v[208:211],v[224:227],v[76:79],v[208:211]
CV v85,v66,v67
)"
R"(W vmcnt(8) lgkmcnt(0)
s_barrier
M v[56:59],v[88:91],a[176:179],v[36:39]
R v[88:91],v1 offset:0
MX v12,v40,v41,v42
MX v12,v12,v43,v44
M a[32:35],a[192:195],v[80:83],a[32:35]
SM m0,s54
)"
R"(s_nop 0
B v0,s[60:63],s43 offen offset:0 sc1 lds
M v[56:59],v[92:95],a[180:183],v[56:59]
R v[92:95],v1 offset:1024
MX v12,v12,v45,v46
MX v12,v12,v47,v48
M a[36:39],a[196:199],v[80:83],a[36:39]
MX v12,v12,v49,v50
)"
R"(MX v12,v12,v51,v52
MX v12,v12,v53,v54
M v[56:59],v[96:99],a[184:187],v[56:59]
R v[96:99],v1 offset:2048
v_max_f32 v12,v12,v55
v_cmp_lt_f32 vcc,s50,v12
M a[40:43],a[200:203],v[80:83],a[40:43]
EX v68,v68
)"
R"(M v[56:59],v[100:103],a[188:191],v[56:59]
R v[100:103],v1 offset:3072
EX v69,v69
M a[44:47],a[204:207],v[80:83],a[44:47]
M v[60:63],v[104:107],a[176:179],v[36:39]
R v[104:107],v1 offset:4096
EX v70,v70
M a[48:51],a[208:211],v[80:83],a[48:51]
)"
R"(CV v86,v68,v69
M v[60:63],v[108:111],a[180:183],v[60:63]
R v[108:111],v1 offset:5120
EX v71,v71
M a[52:55],a[212:215],v[80:83],a[52:55]
CV v87,v70,v71
s_cbranch_vccnz TR19_%=
BK19_%=:
)"
R"(M v[60:63],v[112:115],a[184:187],v[60:63]
R v[112:115],v1 offset:6144
M a[56:59],a[216:219],v[80:83],a[56:59]
M v[60:63],v[116:119],a[188:191],v[60:63]
R v[116:119],v1 offset:7168
EX v40,v40
M a[60:63],a[220:223],v[80:83],a[60:63]
EX v41,v41
)"
R"(M v[64:67],v[120:123],a[176:179],v[36:39]
R v[120:123],v1 offset:8192
EX v42,v42
M v[212:215],v[224:227],v[80:83],v[212:215]
M v[64:67],v[124:127],a[180:183],v[64:67]
R v[124:127],v1 offset:9216
B v0,s[60:63],s43 offen offset:1024 sc1 lds
CV v72,v40,v41
)"
R"(M a[32:35],a[224:227],v[84:87],a[32:35]
EX v43,v43
M v[64:67],v[128:131],a[184:187],v[64:67]
R v[128:131],v1 offset:10240
EX v44,v44
M a[36:39],a[228:231],v[84:87],a[36:39]
CV v73,v42,v43
M v[64:67],v[132:135],a[188:191],v[64:67]
)"
R"(R v[132:135],v1 offset:11264
EX v45,v45
M a[40:43],a[232:235],v[84:87],a[40:43]
EX v46,v46
M v[68:71],v[136:139],a[176:179],v[36:39]
R v[136:139],v1 offset:12288
CV v74,v44,v45
M a[44:47],a[236:239],v[84:87],a[44:47]
)"
R"(EX v47,v47
M v[68:71],v[140:143],a[180:183],v[68:71]
R v[140:143],v1 offset:13312
M a[48:51],a[240:243],v[84:87],a[48:51]
EX v48,v48
M v[68:71],v[144:147],a[184:187],v[68:71]
R v[144:147],v1 offset:14336
CV v75,v46,v47
)"
R"(M a[52:55],a[244:247],v[84:87],a[52:55]
EX v49,v49
M v[68:71],v[148:151],a[188:191],v[68:71]
R v[148:151],v1 offset:15360
SA s40,s40,1
M a[56:59],a[248:251],v[84:87],a[56:59]
EX v50,v50
CV v76,v48,v49
)"
R"(M a[60:63],a[252:255],v[84:87],a[60:63]
EX v51,v51
M v[212:215],v[224:227],v[84:87],v[212:215]
CV v77,v50,v51
s_branch LE_%=
T0M_%=:
W lgkmcnt(12)
M v[40:43],v[88:91],a[128:131],v[24:27]
)"
R"(M v[40:43],v[92:95],a[132:135],v[40:43]
M v[40:43],v[96:99],a[136:139],v[40:43]
M v[40:43],v[100:103],a[140:143],v[40:43]
W lgkmcnt(8)
M v[44:47],v[104:107],a[128:131],v[24:27]
M v[44:47],v[108:111],a[132:135],v[44:47]
M v[44:47],v[112:115],a[136:139],v[44:47]
M v[44:47],v[116:119],a[140:143],v[44:47]
)"
R"(W lgkmcnt(4)
M v[48:51],v[120:123],a[128:131],v[24:27]
M v[48:51],v[124:127],a[132:135],v[48:51]
M v[48:51],v[128:131],a[136:139],v[48:51]
M v[48:51],v[132:135],a[140:143],v[48:51]
W lgkmcnt(0)
M v[52:55],v[136:139],a[128:131],v[24:27]
M v[52:55],v[140:143],a[132:135],v[52:55]
)"
R"(M v[52:55],v[144:147],a[136:139],v[52:55]
M v[52:55],v[148:151],a[140:143],v[52:55]
W lgkmcnt(0)
M v[56:59],v[88:91],a[144:147],v[28:31]
R a[192:195],v2 offset:0
AD v40,v40,v152
AD v41,v41,v153
M v[56:59],v[92:95],a[148:151],v[56:59]
)"
R"(R a[196:199],v2 offset:1024
AD v42,v42,v154
AD v43,v43,v155
M v[56:59],v[96:99],a[152:155],v[56:59]
R a[200:203],v2 offset:2048
AD v44,v44,v156
AD v45,v45,v157
M v[56:59],v[100:103],a[156:159],v[56:59]
)"
R"(R a[204:207],v2 offset:3072
AD v46,v46,v158
AD v47,v47,v159
M v[60:63],v[104:107],a[144:147],v[28:31]
R a[208:211],v2 offset:4096
AD v48,v48,v160
AD v49,v49,v161
M v[60:63],v[108:111],a[148:151],v[60:63]
)"
R"(R a[212:215],v2 offset:5120
AD v50,v50,v162
AD v51,v51,v163
M v[60:63],v[112:115],a[152:155],v[60:63]
R a[216:219],v2 offset:6144
AD v52,v52,v164
AD v53,v53,v165
M v[60:63],v[116:119],a[156:159],v[60:63]
)"
R"(R a[220:223],v2 offset:7168
AD v54,v54,v166
AD v55,v55,v167
M v[64:67],v[120:123],a[144:147],v[28:31]
R a[224:227],v2 offset:8192
MX v12,v40,v41,v42
MX v12,v12,v43,v44
M v[64:67],v[124:127],a[148:151],v[64:67]
)"
R"(R a[228:231],v2 offset:9216
MX v12,v12,v45,v46
MX v12,v12,v47,v48
M v[64:67],v[128:131],a[152:155],v[64:67]
R a[232:235],v2 offset:10240
MX v12,v12,v49,v50
MX v12,v12,v51,v52
M v[64:67],v[132:135],a[156:159],v[64:67]
)"
R"(R a[236:239],v2 offset:11264
MX v12,v12,v53,v54
v_max_f32 v12,v12,v55
M v[68:71],v[136:139],a[144:147],v[28:31]
R a[240:243],v2 offset:12288
v_cmp_lt_f32 vcc,s48,v12
s_cbranch_vccnz TR20_%=
BK20_%=:
)"
R"(M v[68:71],v[140:143],a[148:151],v[68:71]
R a[244:247],v2 offset:13312
EX v40,v40
M v[68:71],v[144:147],a[152:155],v[68:71]
R a[248:251],v2 offset:14336
EX v41,v41
M v[68:71],v[148:151],a[156:159],v[68:71]
R a[252:255],v2 offset:15360
)"
R"(EX v42,v42
CV v72,v40,v41
EX v43,v43
EX v44,v44
CV v73,v42,v43
EX v45,v45
EX v46,v46
CV v74,v44,v45
)"
R"(EX v47,v47
EX v48,v48
CV v75,v46,v47
EX v49,v49
EX v50,v50
CV v76,v48,v49
EX v51,v51
s_nop 0
)"
R"(CV v77,v50,v51
M v[40:43],v[88:91],a[160:163],v[32:35]
AD v56,v56,v168
AD v57,v57,v169
AD v58,v58,v170
W lgkmcnt(14)
M a[0:3],a[192:195],v[72:75],a[0:3]
AD v59,v59,v171
)"
R"(AD v60,v60,v172
AD v61,v61,v173
M v[40:43],v[92:95],a[164:167],v[40:43]
AD v62,v62,v174
AD v63,v63,v175
M a[4:7],a[196:199],v[72:75],a[4:7]
AD v64,v64,v176
AD v65,v65,v177
)"
R"(M v[40:43],v[96:99],a[168:171],v[40:43]
SA s53,s53,0x4000
s_and_b32 s53,s53,0xc000
v_add_u32 v2,s53,v4
W lgkmcnt(12)
M a[8:11],a[200:203],v[72:75],a[8:11]
AD v66,v66,v178
AD v67,v67,v179
)"
R"(AD v68,v68,v180
M v[40:43],v[100:103],a[172:175],v[40:43]
AD v69,v69,v181
AD v70,v70,v182
AD v71,v71,v183
M a[12:15],a[204:207],v[72:75],a[12:15]
MX v12,v56,v57,v58
MX v12,v12,v59,v60
)"
R"(M v[44:47],v[104:107],a[160:163],v[32:35]
MX v12,v12,v61,v62
W lgkmcnt(10)
M a[16:19],a[208:211],v[72:75],a[16:19]
MX v12,v12,v63,v64
MX v12,v12,v65,v66
M v[44:47],v[108:111],a[164:167],v[44:47]
MX v12,v12,v67,v68
)"
R"(MX v12,v12,v69,v70
M a[20:23],a[212:215],v[72:75],a[20:23]
v_max_f32 v12,v12,v71
v_cmp_lt_f32 vcc,s49,v12
M v[44:47],v[112:115],a[168:171],v[44:47]
W lgkmcnt(8)
M a[24:27],a[216:219],v[72:75],a[24:27]
EX v52,v52
)"
R"(M v[44:47],v[116:119],a[172:175],v[44:47]
EX v53,v53
M a[28:31],a[220:223],v[72:75],a[28:31]
EX v54,v54
M v[48:51],v[120:123],a[160:163],v[32:35]
CV v78,v52,v53
EX v55,v55
M v[208:211],v[224:227],v[72:75],v[208:211]
)"
R"(CV v79,v54,v55
M v[48:51],v[124:127],a[164:167],v[48:51]
s_cbranch_vccnz TR21_%=
BK21_%=:
W lgkmcnt(6)
M a[0:3],a[224:227],v[76:79],a[0:3]
EX v56,v56
M v[48:51],v[128:131],a[168:171],v[48:51]
)"
R"(SA s52,s52,0x4000
s_and_b32 s52,s52,0xc000
v_add_u32 v1,s52,v3
M a[4:7],a[228:231],v[76:79],a[4:7]
EX v57,v57
M v[48:51],v[132:135],a[172:175],v[48:51]
EX v58,v58
CV v80,v56,v57
)"
R"(W lgkmcnt(4)
M a[8:11],a[232:235],v[76:79],a[8:11]
EX v59,v59
M v[52:55],v[136:139],a[160:163],v[32:35]
EX v60,v60
CV v81,v58,v59
M a[12:15],a[236:239],v[76:79],a[12:15]
EX v61,v61
)"
R"(M v[52:55],v[140:143],a[164:167],v[52:55]
EX v62,v62
W lgkmcnt(2)
M a[16:19],a[240:243],v[76:79],a[16:19]
CV v82,v60,v61
EX v63,v63
M v[52:55],v[144:147],a[168:171],v[52:55]
EX v64,v64
)"
R"(M a[20:23],a[244:247],v[76:79],a[20:23]
CV v83,v62,v63
M v[52:55],v[148:151],a[172:175],v[52:55]
EX v65,v65
W lgkmcnt(0)
M a[24:27],a[248:251],v[76:79],a[24:27]
EX v66,v66
M a[28:31],a[252:255],v[76:79],a[28:31]
)"
R"(CV v84,v64,v65
EX v67,v67
M v[208:211],v[224:227],v[76:79],v[208:211]
CV v85,v66,v67
W vmcnt(8) lgkmcnt(0)
s_barrier
M v[56:59],v[88:91],a[176:179],v[36:39]
R v[88:91],v1 offset:0
)"
R"(MX v12,v40,v41,v42
MX v12,v12,v43,v44
M a[32:35],a[192:195],v[80:83],a[32:35]
SM m0,s54
s_nop 0
B v0,s[60:63],s43 offen offset:0 sc1 lds
M v[56:59],v[92:95],a[180:183],v[56:59]
R v[92:95],v1 offset:1024
)"
R"(MX v12,v12,v45,v46
MX v12,v12,v47,v48
M a[36:39],a[196:199],v[80:83],a[36:39]
MX v12,v12,v49,v50
MX v12,v12,v51,v52
MX v12,v12,v53,v54
M v[56:59],v[96:99],a[184:187],v[56:59]
R v[96:99],v1 offset:2048
)"
R"(v_max_f32 v12,v12,v55
v_cmp_lt_f32 vcc,s50,v12
M a[40:43],a[200:203],v[80:83],a[40:43]
EX v68,v68
M v[56:59],v[100:103],a[188:191],v[56:59]
R v[100:103],v1 offset:3072
EX v69,v69
M a[44:47],a[204:207],v[80:83],a[44:47]
)"
R"(M v[60:63],v[104:107],a[176:179],v[36:39]
R v[104:107],v1 offset:4096
EX v70,v70
M a[48:51],a[208:211],v[80:83],a[48:51]
CV v86,v68,v69
M v[60:63],v[108:111],a[180:183],v[60:63]
R v[108:111],v1 offset:5120
EX v71,v71
)"
R"(M a[52:55],a[212:215],v[80:83],a[52:55]
CV v87,v70,v71
s_cbranch_vccnz TR22_%=
BK22_%=:
M v[60:63],v[112:115],a[184:187],v[60:63]
R v[112:115],v1 offset:6144
M a[56:59],a[216:219],v[80:83],a[56:59]
M v[60:63],v[116:119],a[188:191],v[60:63]
)"
R"(R v[116:119],v1 offset:7168
EX v40,v40
M a[60:63],a[220:223],v[80:83],a[60:63]
EX v41,v41
M v[64:67],v[120:123],a[176:179],v[36:39]
R v[120:123],v1 offset:8192
EX v42,v42
M v[212:215],v[224:227],v[80:83],v[212:215]
)"
R"(M v[64:67],v[124:127],a[180:183],v[64:67]
R v[124:127],v1 offset:9216
B v0,s[60:63],s43 offen offset:1024 sc1 lds
CV v72,v40,v41
M a[32:35],a[224:227],v[84:87],a[32:35]
EX v43,v43
M v[64:67],v[128:131],a[184:187],v[64:67]
R v[128:131],v1 offset:10240
)"
R"(EX v44,v44
M a[36:39],a[228:231],v[84:87],a[36:39]
CV v73,v42,v43
M v[64:67],v[132:135],a[188:191],v[64:67]
R v[132:135],v1 offset:11264
EX v45,v45
M a[40:43],a[232:235],v[84:87],a[40:43]
EX v46,v46
)"
R"(M v[68:71],v[136:139],a[176:179],v[36:39]
R v[136:139],v1 offset:12288
CV v74,v44,v45
M a[44:47],a[236:239],v[84:87],a[44:47]
EX v47,v47
M v[68:71],v[140:143],a[180:183],v[68:71]
R v[140:143],v1 offset:13312
M a[48:51],a[240:243],v[84:87],a[48:51]
)"
R"(EX v48,v48
M v[68:71],v[144:147],a[184:187],v[68:71]
R v[144:147],v1 offset:14336
CV v75,v46,v47
M a[52:55],a[244:247],v[84:87],a[52:55]
EX v49,v49
M v[68:71],v[148:151],a[188:191],v[68:71]
R v[148:151],v1 offset:15360
)"
R"(SA s40,s40,1
M a[56:59],a[248:251],v[84:87],a[56:59]
EX v50,v50
CV v76,v48,v49
M a[60:63],a[252:255],v[84:87],a[60:63]
EX v51,v51
M v[212:215],v[224:227],v[84:87],v[212:215]
CV v77,v50,v51
)"
R"(LE_%=:
s_nop 7
s_nop 3
v_rcp_f32 v208,v208
AR v184,a0
AR v185,a1
AR v186,a2
AR v187,a3
)"
R"(AR v188,a4
AR v189,a5
AR v190,a6
AR v191,a7
MU v184,v184,v208
MU v185,v185,v208
MU v186,v186,v208
MU v187,v187,v208
)"
R"(MU v188,v188,v208
MU v189,v189,v208
MU v190,v190,v208
MU v191,v191,v208
CV v192,v184,v185
CV v193,v186,v187
CV v194,v188,v189
CV v195,v190,v191
)"
R"(GS v229,v[192:195],s[78:79] offset:0 sc1
AR v196,a8
AR v197,a9
AR v198,a10
AR v199,a11
AR v200,a12
AR v201,a13
AR v202,a14
)"
R"(AR v203,a15
MU v196,v196,v208
MU v197,v197,v208
MU v198,v198,v208
MU v199,v199,v208
MU v200,v200,v208
MU v201,v201,v208
MU v202,v202,v208
)"
R"(MU v203,v203,v208
CV v204,v196,v197
CV v205,v198,v199
CV v206,v200,v201
CV v207,v202,v203
GS v230,v[204:207],s[78:79] offset:0 sc1
AR v184,a16
AR v185,a17
)"
R"(AR v186,a18
AR v187,a19
AR v188,a20
AR v189,a21
AR v190,a22
AR v191,a23
MU v184,v184,v208
MU v185,v185,v208
)"
R"(MU v186,v186,v208
MU v187,v187,v208
MU v188,v188,v208
MU v189,v189,v208
MU v190,v190,v208
MU v191,v191,v208
CV v192,v184,v185
CV v193,v186,v187
)"
R"(CV v194,v188,v189
CV v195,v190,v191
GS v231,v[192:195],s[78:79] offset:0 sc1
AR v196,a24
AR v197,a25
AR v198,a26
AR v199,a27
AR v200,a28
)"
R"(AR v201,a29
AR v202,a30
AR v203,a31
MU v196,v196,v208
MU v197,v197,v208
MU v198,v198,v208
MU v199,v199,v208
MU v200,v200,v208
)"
R"(MU v201,v201,v208
MU v202,v202,v208
MU v203,v203,v208
CV v204,v196,v197
CV v205,v198,v199
CV v206,v200,v201
CV v207,v202,v203
GS v232,v[204:207],s[78:79] offset:0 sc1
)"
R"(v_rcp_f32 v212,v212
AR v184,a32
AR v185,a33
AR v186,a34
AR v187,a35
AR v188,a36
AR v189,a37
AR v190,a38
)"
R"(AR v191,a39
MU v184,v184,v212
MU v185,v185,v212
MU v186,v186,v212
MU v187,v187,v212
MU v188,v188,v212
MU v189,v189,v212
MU v190,v190,v212
)"
R"(MU v191,v191,v212
CV v192,v184,v185
CV v193,v186,v187
CV v194,v188,v189
CV v195,v190,v191
GS v229,v[192:195],s[78:79] offset:1024 sc1
AR v196,a40
AR v197,a41
)"
R"(AR v198,a42
AR v199,a43
AR v200,a44
AR v201,a45
AR v202,a46
AR v203,a47
MU v196,v196,v212
MU v197,v197,v212
)"
R"(MU v198,v198,v212
MU v199,v199,v212
MU v200,v200,v212
MU v201,v201,v212
MU v202,v202,v212
MU v203,v203,v212
CV v204,v196,v197
CV v205,v198,v199
)"
R"(CV v206,v200,v201
CV v207,v202,v203
GS v230,v[204:207],s[78:79] offset:1024 sc1
AR v184,a48
AR v185,a49
AR v186,a50
AR v187,a51
AR v188,a52
)"
R"(AR v189,a53
AR v190,a54
AR v191,a55
MU v184,v184,v212
MU v185,v185,v212
MU v186,v186,v212
MU v187,v187,v212
MU v188,v188,v212
)"
R"(MU v189,v189,v212
MU v190,v190,v212
MU v191,v191,v212
CV v192,v184,v185
CV v193,v186,v187
CV v194,v188,v189
CV v195,v190,v191
GS v231,v[192:195],s[78:79] offset:1024 sc1
)"
R"(AR v196,a56
AR v197,a57
AR v198,a58
AR v199,a59
AR v200,a60
AR v201,a61
AR v202,a62
AR v203,a63
)"
R"(MU v196,v196,v212
MU v197,v197,v212
MU v198,v198,v212
MU v199,v199,v212
MU v200,v200,v212
MU v201,v201,v212
MU v202,v202,v212
MU v203,v203,v212
)"
R"(CV v204,v196,v197
CV v205,v198,v199
CV v206,v200,v201
CV v207,v202,v203
GS v232,v[204:207],s[78:79] offset:1024 sc1
s_cmp_ge_u32 s40,s42
s_cbranch_scc1 SM_%=
SLB_%=:
)"
R"(W lgkmcnt(14)
M v[40:43],v[88:91],a[160:163],v[32:35]
MX v12,v56,v57,v58
MX v12,v12,v59,v60
MX v12,v12,v61,v62
M a[64:67],a[192:195],v[72:75],a[64:67]
B v0,s[60:63],s43 offen offset:2048 sc1 lds
MX v12,v12,v63,v64
)"
R"(MX v12,v12,v65,v66
M v[40:43],v[92:95],a[164:167],v[40:43]
MX v12,v12,v67,v68
M a[68:71],a[196:199],v[72:75],a[68:71]
MX v12,v12,v69,v70
v_max_f32 v12,v12,v71
W lgkmcnt(12)
M v[40:43],v[96:99],a[168:171],v[40:43]
)"
R"(v_cmp_lt_f32 vcc,s51,v12
M a[72:75],a[200:203],v[72:75],a[72:75]
EX v52,v52
M v[40:43],v[100:103],a[172:175],v[40:43]
B v0,s[60:63],s43 offen offset:3072 sc1 lds
EX v53,v53
M a[76:79],a[204:207],v[72:75],a[76:79]
W lgkmcnt(10)
)"
R"(M v[44:47],v[104:107],a[160:163],v[32:35]
EX v54,v54
M a[80:83],a[208:211],v[72:75],a[80:83]
CV v78,v52,v53
M v[44:47],v[108:111],a[164:167],v[44:47]
EX v55,v55
M a[84:87],a[212:215],v[72:75],a[84:87]
CV v79,v54,v55
)"
R"(s_cbranch_vccnz TR23_%=
BK23_%=:
W lgkmcnt(8)
M v[44:47],v[112:115],a[168:171],v[44:47]
SM m0,s55
s_nop 0
B v0,s[64:67],s47 offen offset:0 sc1 lds
SA s43,s43,0x4000
)"
R"(s_and_b32 s46,s43,0xc000
SA s54,s44,s46
M a[88:91],a[216:219],v[72:75],a[88:91]
M v[44:47],v[116:119],a[172:175],v[44:47]
EX v56,v56
M a[92:95],a[220:223],v[72:75],a[92:95]
EX v57,v57
W lgkmcnt(6)
)"
R"(M v[48:51],v[120:123],a[160:163],v[32:35]
EX v58,v58
M v[216:219],v[224:227],v[72:75],v[216:219]
M v[48:51],v[124:127],a[164:167],v[48:51]
B v0,s[64:67],s47 offen offset:1024 sc1 lds
CV v80,v56,v57
M a[64:67],a[224:227],v[76:79],a[64:67]
EX v59,v59
)"
R"(W lgkmcnt(4)
M v[48:51],v[128:131],a[168:171],v[48:51]
SA s52,s52,0x4000
s_and_b32 s52,s52,0xc000
v_add_u32 v1,s52,v3
M a[68:71],a[228:231],v[76:79],a[68:71]
EX v60,v60
CV v81,v58,v59
)"
R"(M v[48:51],v[132:135],a[172:175],v[48:51]
EX v61,v61
M a[72:75],a[232:235],v[76:79],a[72:75]
B v0,s[64:67],s47 offen offset:2048 sc1 lds
EX v62,v62
W lgkmcnt(2)
M v[52:55],v[136:139],a[160:163],v[32:35]
CV v82,v60,v61
)"
R"(M a[76:79],a[236:239],v[76:79],a[76:79]
EX v63,v63
M v[52:55],v[140:143],a[164:167],v[52:55]
M a[80:83],a[240:243],v[76:79],a[80:83]
EX v64,v64
W lgkmcnt(0)
M v[52:55],v[144:147],a[168:171],v[52:55]
CV v83,v62,v63
)"
R"(M a[84:87],a[244:247],v[76:79],a[84:87]
B v0,s[64:67],s47 offen offset:3072 sc1 lds
EX v65,v65
M v[52:55],v[148:151],a[172:175],v[52:55]
SA s47,s47,0x4000
s_xor_b32 s58,s47,0x8000
s_and_b32 s58,s58,0xc000
SA s55,s45,s58
)"
R"(M a[88:91],a[248:251],v[76:79],a[88:91]
EX v66,v66
CV v84,v64,v65
M a[92:95],a[252:255],v[76:79],a[92:95]
EX v67,v67
M v[216:219],v[224:227],v[76:79],v[216:219]
CV v85,v66,v67
W vmcnt(8) lgkmcnt(0)
)"
R"(s_barrier
M v[56:59],v[88:91],a[176:179],v[36:39]
R v[88:91],v1 offset:0
MX v12,v40,v41,v42
MX v12,v12,v43,v44
M a[96:99],a[192:195],v[80:83],a[96:99]
R a[192:195],v2 offset:0
SM m0,s54
)"
R"(s_nop 0
B v0,s[60:63],s43 offen offset:0 sc1 lds
M v[56:59],v[92:95],a[180:183],v[56:59]
R v[92:95],v1 offset:1024
MX v12,v12,v45,v46
MX v12,v12,v47,v48
M a[100:103],a[196:199],v[80:83],a[100:103]
R a[196:199],v2 offset:1024
)"
R"(MX v12,v12,v49,v50
MX v12,v12,v51,v52
M v[56:59],v[96:99],a[184:187],v[56:59]
R v[96:99],v1 offset:2048
MX v12,v12,v53,v54
v_max_f32 v12,v12,v55
M a[104:107],a[200:203],v[80:83],a[104:107]
R a[200:203],v2 offset:2048
)"
R"(v_cmp_lt_f32 vcc,s50,v12
M v[56:59],v[100:103],a[188:191],v[56:59]
R v[100:103],v1 offset:3072
EX v68,v68
M a[108:111],a[204:207],v[80:83],a[108:111]
R a[204:207],v2 offset:3072
EX v69,v69
M v[60:63],v[104:107],a[176:179],v[36:39]
)"
R"(R v[104:107],v1 offset:4096
EX v70,v70
M a[112:115],a[208:211],v[80:83],a[112:115]
R a[208:211],v2 offset:4096
CV v86,v68,v69
M v[60:63],v[108:111],a[180:183],v[60:63]
R v[108:111],v1 offset:5120
EX v71,v71
)"
R"(M a[116:119],a[212:215],v[80:83],a[116:119]
R a[212:215],v2 offset:5120
CV v87,v70,v71
s_cbranch_vccnz TR24_%=
BK24_%=:
M v[60:63],v[112:115],a[184:187],v[60:63]
R v[112:115],v1 offset:6144
M a[120:123],a[216:219],v[80:83],a[120:123]
)"
R"(R a[216:219],v2 offset:6144
M v[60:63],v[116:119],a[188:191],v[60:63]
R v[116:119],v1 offset:7168
EX v40,v40
M a[124:127],a[220:223],v[80:83],a[124:127]
R a[220:223],v2 offset:7168
EX v41,v41
M v[64:67],v[120:123],a[176:179],v[36:39]
)"
R"(R v[120:123],v1 offset:8192
EX v42,v42
M v[220:223],v[224:227],v[80:83],v[220:223]
M v[64:67],v[124:127],a[180:183],v[64:67]
R v[124:127],v1 offset:9216
B v0,s[60:63],s43 offen offset:1024 sc1 lds
CV v72,v40,v41
M a[96:99],a[224:227],v[84:87],a[96:99]
)"
R"(R a[224:227],v2 offset:8192
EX v43,v43
M v[64:67],v[128:131],a[184:187],v[64:67]
R v[128:131],v1 offset:10240
EX v44,v44
M a[100:103],a[228:231],v[84:87],a[100:103]
R a[228:231],v2 offset:9216
CV v73,v42,v43
)"
R"(M v[64:67],v[132:135],a[188:191],v[64:67]
R v[132:135],v1 offset:11264
EX v45,v45
M a[104:107],a[232:235],v[84:87],a[104:107]
R a[232:235],v2 offset:10240
EX v46,v46
M v[68:71],v[136:139],a[176:179],v[36:39]
R v[136:139],v1 offset:12288
)"
R"(CV v74,v44,v45
M a[108:111],a[236:239],v[84:87],a[108:111]
R a[236:239],v2 offset:11264
EX v47,v47
M v[68:71],v[140:143],a[180:183],v[68:71]
R v[140:143],v1 offset:13312
M a[112:115],a[240:243],v[84:87],a[112:115]
R a[240:243],v2 offset:12288
)"
R"(EX v48,v48
M v[68:71],v[144:147],a[184:187],v[68:71]
R v[144:147],v1 offset:14336
CV v75,v46,v47
M a[116:119],a[244:247],v[84:87],a[116:119]
R a[244:247],v2 offset:13312
SA s40,s40,1
M v[68:71],v[148:151],a[188:191],v[68:71]
)"
R"(R v[148:151],v1 offset:15360
EX v49,v49
M a[120:123],a[248:251],v[84:87],a[120:123]
R a[248:251],v2 offset:14336
EX v50,v50
M a[124:127],a[252:255],v[84:87],a[124:127]
R a[252:255],v2 offset:15360
CV v76,v48,v49
)"
R"(M v[220:223],v[224:227],v[84:87],v[220:223]
SA s53,s53,0x4000
s_and_b32 s53,s53,0xc000
v_add_u32 v2,s53,v4
EX v51,v51
s_nop 0
CV v77,v50,v51
s_cmp_lt_u32 s40,s42
)"
R"(s_cbranch_scc1 SLB_%=
SM_%=:
W lgkmcnt(14)
M v[40:43],v[88:91],a[160:163],v[32:35]
MX v12,v56,v57,v58
MX v12,v12,v59,v60
MX v12,v12,v61,v62
M a[64:67],a[192:195],v[72:75],a[64:67]
)"
R"(B v0,s[60:63],s43 offen offset:2048 sc1 lds
MX v12,v12,v63,v64
MX v12,v12,v65,v66
M v[40:43],v[92:95],a[164:167],v[40:43]
MX v12,v12,v67,v68
M a[68:71],a[196:199],v[72:75],a[68:71]
MX v12,v12,v69,v70
v_max_f32 v12,v12,v71
)"
R"(W lgkmcnt(12)
M v[40:43],v[96:99],a[168:171],v[40:43]
v_cmp_lt_f32 vcc,s51,v12
M a[72:75],a[200:203],v[72:75],a[72:75]
EX v52,v52
M v[40:43],v[100:103],a[172:175],v[40:43]
B v0,s[60:63],s43 offen offset:3072 sc1 lds
EX v53,v53
)"
R"(M a[76:79],a[204:207],v[72:75],a[76:79]
W lgkmcnt(10)
M v[44:47],v[104:107],a[160:163],v[32:35]
EX v54,v54
M a[80:83],a[208:211],v[72:75],a[80:83]
CV v78,v52,v53
M v[44:47],v[108:111],a[164:167],v[44:47]
EX v55,v55
)"
R"(M a[84:87],a[212:215],v[72:75],a[84:87]
CV v79,v54,v55
s_cbranch_vccnz TR25_%=
BK25_%=:
W lgkmcnt(8)
M v[44:47],v[112:115],a[168:171],v[44:47]
SM m0,s55
s_nop 0
)"
R"(B v0,s[64:67],s47 offen offset:0 sc1 lds
SA s43,s43,0x4000
s_and_b32 s46,s43,0xc000
SA s54,s44,s46
M a[88:91],a[216:219],v[72:75],a[88:91]
M v[44:47],v[116:119],a[172:175],v[44:47]
EX v56,v56
M a[92:95],a[220:223],v[72:75],a[92:95]
)"
R"(EX v57,v57
W lgkmcnt(6)
M v[48:51],v[120:123],a[160:163],v[32:35]
EX v58,v58
M v[216:219],v[224:227],v[72:75],v[216:219]
M v[48:51],v[124:127],a[164:167],v[48:51]
B v0,s[64:67],s47 offen offset:1024 sc1 lds
CV v80,v56,v57
)"
R"(M a[64:67],a[224:227],v[76:79],a[64:67]
EX v59,v59
W lgkmcnt(4)
M v[48:51],v[128:131],a[168:171],v[48:51]
SA s52,s52,0x4000
s_and_b32 s52,s52,0xc000
v_add_u32 v1,s52,v3
M a[68:71],a[228:231],v[76:79],a[68:71]
)"
R"(EX v60,v60
CV v81,v58,v59
M v[48:51],v[132:135],a[172:175],v[48:51]
EX v61,v61
M a[72:75],a[232:235],v[76:79],a[72:75]
B v0,s[64:67],s47 offen offset:2048 sc1 lds
EX v62,v62
W lgkmcnt(2)
)"
R"(M v[52:55],v[136:139],a[160:163],v[32:35]
CV v82,v60,v61
M a[76:79],a[236:239],v[76:79],a[76:79]
EX v63,v63
M v[52:55],v[140:143],a[164:167],v[52:55]
M a[80:83],a[240:243],v[76:79],a[80:83]
EX v64,v64
W lgkmcnt(0)
)"
R"(M v[52:55],v[144:147],a[168:171],v[52:55]
CV v83,v62,v63
M a[84:87],a[244:247],v[76:79],a[84:87]
B v0,s[64:67],s47 offen offset:3072 sc1 lds
EX v65,v65
M v[52:55],v[148:151],a[172:175],v[52:55]
SA s47,s47,0x4000
s_xor_b32 s58,s47,0x8000
)"
R"(s_and_b32 s58,s58,0xc000
SA s55,s45,s58
M a[88:91],a[248:251],v[76:79],a[88:91]
EX v66,v66
CV v84,v64,v65
M a[92:95],a[252:255],v[76:79],a[92:95]
EX v67,v67
M v[216:219],v[224:227],v[76:79],v[216:219]
)"
R"(CV v85,v66,v67
W vmcnt(8) lgkmcnt(0)
s_barrier
M v[56:59],v[88:91],a[176:179],v[36:39]
R v[88:91],v1 offset:0
AD v40,v40,v152
AD v41,v41,v153
M a[96:99],a[192:195],v[80:83],a[96:99]
)"
R"(R a[192:195],v2 offset:0
SM m0,s54
s_nop 0
B v0,s[60:63],s43 offen offset:0 sc1 lds
M v[56:59],v[92:95],a[180:183],v[56:59]
R v[92:95],v1 offset:1024
AD v42,v42,v154
AD v43,v43,v155
)"
R"(M a[100:103],a[196:199],v[80:83],a[100:103]
R a[196:199],v2 offset:1024
AD v44,v44,v156
AD v45,v45,v157
M v[56:59],v[96:99],a[184:187],v[56:59]
R v[96:99],v1 offset:2048
AD v46,v46,v158
AD v47,v47,v159
)"
R"(M a[104:107],a[200:203],v[80:83],a[104:107]
R a[200:203],v2 offset:2048
AD v48,v48,v160
AD v49,v49,v161
M v[56:59],v[100:103],a[188:191],v[56:59]
R v[100:103],v1 offset:3072
AD v50,v50,v162
AD v51,v51,v163
)"
R"(M a[108:111],a[204:207],v[80:83],a[108:111]
R a[204:207],v2 offset:3072
AD v52,v52,v164
AD v53,v53,v165
M v[60:63],v[104:107],a[176:179],v[36:39]
R v[104:107],v1 offset:4096
AD v54,v54,v166
AD v55,v55,v167
)"
R"(M a[112:115],a[208:211],v[80:83],a[112:115]
R a[208:211],v2 offset:4096
MX v12,v40,v41,v42
MX v12,v12,v43,v44
M v[60:63],v[108:111],a[180:183],v[60:63]
R v[108:111],v1 offset:5120
MX v12,v12,v45,v46
MX v12,v12,v47,v48
)"
R"(M a[116:119],a[212:215],v[80:83],a[116:119]
R a[212:215],v2 offset:5120
MX v12,v12,v49,v50
MX v12,v12,v51,v52
M v[60:63],v[112:115],a[184:187],v[60:63]
R v[112:115],v1 offset:6144
MX v12,v12,v53,v54
v_max_f32 v12,v12,v55
)"
R"(M a[120:123],a[216:219],v[80:83],a[120:123]
R a[216:219],v2 offset:6144
v_cmp_lt_f32 vcc,s50,v12
M v[60:63],v[116:119],a[188:191],v[60:63]
R v[116:119],v1 offset:7168
EX v68,v68
M a[124:127],a[220:223],v[80:83],a[124:127]
R a[220:223],v2 offset:7168
)"
R"(EX v69,v69
M v[64:67],v[120:123],a[176:179],v[36:39]
R v[120:123],v1 offset:8192
EX v70,v70
M v[220:223],v[224:227],v[80:83],v[220:223]
CV v86,v68,v69
EX v71,v71
M v[64:67],v[124:127],a[180:183],v[64:67]
)"
R"(R v[124:127],v1 offset:9216
B v0,s[60:63],s43 offen offset:1024 sc1 lds
CV v87,v70,v71
s_nop 1
M a[96:99],a[224:227],v[84:87],a[96:99]
R a[224:227],v2 offset:8192
s_cbranch_vccnz TR26_%=
BK26_%=:
)"
R"(M v[64:67],v[128:131],a[184:187],v[64:67]
R v[128:131],v1 offset:10240
EX v40,v40
M a[100:103],a[228:231],v[84:87],a[100:103]
R a[228:231],v2 offset:9216
EX v41,v41
M v[64:67],v[132:135],a[188:191],v[64:67]
R v[132:135],v1 offset:11264
)"
R"(EX v42,v42
M a[104:107],a[232:235],v[84:87],a[104:107]
R a[232:235],v2 offset:10240
CV v72,v40,v41
M v[68:71],v[136:139],a[176:179],v[36:39]
R v[136:139],v1 offset:12288
EX v43,v43
M a[108:111],a[236:239],v[84:87],a[108:111]
)"
R"(R a[236:239],v2 offset:11264
EX v44,v44
M v[68:71],v[140:143],a[180:183],v[68:71]
R v[140:143],v1 offset:13312
CV v73,v42,v43
M a[112:115],a[240:243],v[84:87],a[112:115]
R a[240:243],v2 offset:12288
EX v45,v45
)"
R"(M v[68:71],v[144:147],a[184:187],v[68:71]
R v[144:147],v1 offset:14336
EX v46,v46
M a[116:119],a[244:247],v[84:87],a[116:119]
R a[244:247],v2 offset:13312
SA s40,s40,1
CV v74,v44,v45
M v[68:71],v[148:151],a[188:191],v[68:71]
)"
R"(R v[148:151],v1 offset:15360
EX v47,v47
M a[120:123],a[248:251],v[84:87],a[120:123]
R a[248:251],v2 offset:14336
EX v48,v48
M a[124:127],a[252:255],v[84:87],a[124:127]
R a[252:255],v2 offset:15360
CV v75,v46,v47
)"
R"(M v[220:223],v[224:227],v[84:87],v[220:223]
SA s53,s53,0x4000
s_and_b32 s53,s53,0xc000
v_add_u32 v2,s53,v4
EX v49,v49
EX v50,v50
CV v76,v48,v49
EX v51,v51
)"
R"(s_nop 0
CV v77,v50,v51
W lgkmcnt(15)
M a[64:67],a[192:195],v[72:75],a[64:67]
AD v56,v56,v168
AD v57,v57,v169
AD v58,v58,v170
M a[68:71],a[196:199],v[72:75],a[68:71]
)"
R"(AD v59,v59,v171
AD v60,v60,v172
AD v61,v61,v173
M a[72:75],a[200:203],v[72:75],a[72:75]
AD v62,v62,v174
AD v63,v63,v175
AD v64,v64,v176
M a[76:79],a[204:207],v[72:75],a[76:79]
)"
R"(AD v65,v65,v177
AD v66,v66,v178
AD v67,v67,v179
M a[80:83],a[208:211],v[72:75],a[80:83]
AD v68,v68,v180
AD v69,v69,v181
AD v70,v70,v182
M a[84:87],a[212:215],v[72:75],a[84:87]
)"
R"(AD v71,v71,v183
MX v12,v56,v57,v58
MX v12,v12,v59,v60
M a[88:91],a[216:219],v[72:75],a[88:91]
MX v12,v12,v61,v62
MX v12,v12,v63,v64
MX v12,v12,v65,v66
M a[92:95],a[220:223],v[72:75],a[92:95]
)"
R"(MX v12,v12,v67,v68
MX v12,v12,v69,v70
v_max_f32 v12,v12,v71
M v[216:219],v[224:227],v[72:75],v[216:219]
v_cmp_lt_f32 vcc,s51,v12
EX v52,v52
EX v53,v53
EX v54,v54
)"
R"(CV v78,v52,v53
EX v55,v55
s_nop 0
CV v79,v54,v55
W lgkmcnt(8)
s_nop 0
M a[64:67],a[224:227],v[76:79],a[64:67]
s_cbranch_vccnz TR27_%=
)"
R"(BK27_%=:
EX v56,v56
M a[68:71],a[228:231],v[76:79],a[68:71]
EX v57,v57
M a[72:75],a[232:235],v[76:79],a[72:75]
EX v58,v58
CV v80,v56,v57
M a[76:79],a[236:239],v[76:79],a[76:79]
)"
R"(EX v59,v59
W lgkmcnt(0)
M a[80:83],a[240:243],v[76:79],a[80:83]
EX v60,v60
CV v81,v58,v59
M a[84:87],a[244:247],v[76:79],a[84:87]
EX v61,v61
M a[88:91],a[248:251],v[76:79],a[88:91]
)"
R"(EX v62,v62
CV v82,v60,v61
M a[92:95],a[252:255],v[76:79],a[92:95]
EX v63,v63
M v[216:219],v[224:227],v[76:79],v[216:219]
EX v64,v64
CV v83,v62,v63
EX v65,v65
)"
R"(EX v66,v66
CV v84,v64,v65
EX v67,v67
s_nop 0
CV v85,v66,v67
M a[96:99],a[192:195],v[80:83],a[96:99]
EX v68,v68
M a[100:103],a[196:199],v[80:83],a[100:103]
)"
R"(EX v69,v69
M a[104:107],a[200:203],v[80:83],a[104:107]
M a[108:111],a[204:207],v[80:83],a[108:111]
M a[112:115],a[208:211],v[80:83],a[112:115]
M a[116:119],a[212:215],v[80:83],a[116:119]
EX v70,v70
M a[120:123],a[216:219],v[80:83],a[120:123]
M a[124:127],a[220:223],v[80:83],a[124:127]
)"
R"(M v[220:223],v[224:227],v[80:83],v[220:223]
CV v86,v68,v69
EX v71,v71
s_nop 0
CV v87,v70,v71
s_nop 1
M a[96:99],a[224:227],v[84:87],a[96:99]
M a[100:103],a[228:231],v[84:87],a[100:103]
)"
R"(M a[104:107],a[232:235],v[84:87],a[104:107]
M a[108:111],a[236:239],v[84:87],a[108:111]
M a[112:115],a[240:243],v[84:87],a[112:115]
M a[116:119],a[244:247],v[84:87],a[116:119]
M a[120:123],a[248:251],v[84:87],a[120:123]
M a[124:127],a[252:255],v[84:87],a[124:127]
M v[220:223],v[224:227],v[84:87],v[220:223]
s_nop 7
)"
R"(s_nop 3
v_rcp_f32 v216,v216
AR v184,a64
AR v185,a65
AR v186,a66
AR v187,a67
AR v188,a68
AR v189,a69
)"
R"(AR v190,a70
AR v191,a71
MU v184,v184,v216
MU v185,v185,v216
MU v186,v186,v216
MU v187,v187,v216
MU v188,v188,v216
MU v189,v189,v216
)"
R"(MU v190,v190,v216
MU v191,v191,v216
CV v192,v184,v185
CV v193,v186,v187
CV v194,v188,v189
CV v195,v190,v191
GS v229,v[192:195],s[80:81] offset:0 sc1
AR v196,a72
)"
R"(AR v197,a73
AR v198,a74
AR v199,a75
AR v200,a76
AR v201,a77
AR v202,a78
AR v203,a79
MU v196,v196,v216
)"
R"(MU v197,v197,v216
MU v198,v198,v216
MU v199,v199,v216
MU v200,v200,v216
MU v201,v201,v216
MU v202,v202,v216
MU v203,v203,v216
CV v204,v196,v197
)"
R"(CV v205,v198,v199
CV v206,v200,v201
CV v207,v202,v203
GS v230,v[204:207],s[80:81] offset:0 sc1
AR v184,a80
AR v185,a81
AR v186,a82
AR v187,a83
)"
R"(AR v188,a84
AR v189,a85
AR v190,a86
AR v191,a87
MU v184,v184,v216
MU v185,v185,v216
MU v186,v186,v216
MU v187,v187,v216
)"
R"(MU v188,v188,v216
MU v189,v189,v216
MU v190,v190,v216
MU v191,v191,v216
CV v192,v184,v185
CV v193,v186,v187
CV v194,v188,v189
CV v195,v190,v191
)"
R"(GS v231,v[192:195],s[80:81] offset:0 sc1
AR v196,a88
AR v197,a89
AR v198,a90
AR v199,a91
AR v200,a92
AR v201,a93
AR v202,a94
)"
R"(AR v203,a95
MU v196,v196,v216
MU v197,v197,v216
MU v198,v198,v216
MU v199,v199,v216
MU v200,v200,v216
MU v201,v201,v216
MU v202,v202,v216
)"
R"(MU v203,v203,v216
CV v204,v196,v197
CV v205,v198,v199
CV v206,v200,v201
CV v207,v202,v203
GS v232,v[204:207],s[80:81] offset:0 sc1
v_rcp_f32 v220,v220
AR v184,a96
)"
R"(AR v185,a97
AR v186,a98
AR v187,a99
AR v188,a100
AR v189,a101
AR v190,a102
AR v191,a103
MU v184,v184,v220
)"
R"(MU v185,v185,v220
MU v186,v186,v220
MU v187,v187,v220
MU v188,v188,v220
MU v189,v189,v220
MU v190,v190,v220
MU v191,v191,v220
CV v192,v184,v185
)"
R"(CV v193,v186,v187
CV v194,v188,v189
CV v195,v190,v191
GS v229,v[192:195],s[80:81] offset:1024 sc1
AR v196,a104
AR v197,a105
AR v198,a106
AR v199,a107
)"
R"(AR v200,a108
AR v201,a109
AR v202,a110
AR v203,a111
MU v196,v196,v220
MU v197,v197,v220
MU v198,v198,v220
MU v199,v199,v220
)"
R"(MU v200,v200,v220
MU v201,v201,v220
MU v202,v202,v220
MU v203,v203,v220
CV v204,v196,v197
CV v205,v198,v199
CV v206,v200,v201
CV v207,v202,v203
)"
R"(GS v230,v[204:207],s[80:81] offset:1024 sc1
AR v184,a112
AR v185,a113
AR v186,a114
AR v187,a115
AR v188,a116
AR v189,a117
AR v190,a118
)"
R"(AR v191,a119
MU v184,v184,v220
MU v185,v185,v220
MU v186,v186,v220
MU v187,v187,v220
MU v188,v188,v220
MU v189,v189,v220
MU v190,v190,v220
)"
R"(MU v191,v191,v220
CV v192,v184,v185
CV v193,v186,v187
CV v194,v188,v189
CV v195,v190,v191
GS v231,v[192:195],s[80:81] offset:1024 sc1
AR v196,a120
AR v197,a121
)"
R"(AR v198,a122
AR v199,a123
AR v200,a124
AR v201,a125
AR v202,a126
AR v203,a127
MU v196,v196,v220
MU v197,v197,v220
)"
R"(MU v198,v198,v220
MU v199,v199,v220
MU v200,v200,v220
MU v201,v201,v220
MU v202,v202,v220
MU v203,v203,v220
CV v204,v196,v197
CV v205,v198,v199
)"
R"(CV v206,v200,v201
CV v207,v202,v203
GS v232,v[204:207],s[80:81] offset:1024 sc1
s_bitcmp1_b32 s42,0
s_cbranch_scc1 AE_%=
s_barrier
AE_%=:
AOK_%=:
)"
R"(W vmcnt(0) lgkmcnt(0)
s_barrier
s_cmp_lg_u32 %17,0
s_cbranch_scc1 NP28_%=
s_lshl_b32 s92,s95,6
s_mov_b64 s[90:91],%16
SA s90,s90,s92
s_addc_u32 s91,s91,0
)"
R"(SA s90,s90,0x5009000
s_addc_u32 s91,s91,0
v_mov_b32 v8,s88
v_mov_b32 v9,0
s_mov_b64 exec,1
s_nop 1
global_store_dword v9,v8,s[90:91] sc1
s_mov_b64 exec,-1
)"
R"(s_nop 1
s_lshl_b32 s92,s95,6
s_mov_b64 s[90:91],%16
SA s90,s90,s92
s_addc_u32 s91,s91,0
SA s90,s90,0x5005000
s_addc_u32 s91,s91,0
v_mov_b32 v8,s88
)"
R"(v_mov_b32 v9,0
s_mov_b64 exec,1
s_nop 1
global_store_dword v9,v8,s[90:91] sc1
s_mov_b64 exec,-1
s_nop 1
NP28_%=:
s_lshr_b32 s52,%19,3
)"
R"(s_and_b32 s53,%19,3
s_sub_u32 s54,15,%20
s_bitcmp1_b32 %19,2
s_cselect_b32 s54,s54,%20
s_lshl_b32 s55,s54,2
SA s55,s55,s53
v_mov_b32 v3,%6
v_lshrrev_b32 v20,4,%6
)"
R"(v_and_b32 v21,15,v20
v_lshrrev_b32 v22,4,v20
v_lshrrev_b32 v23,2,v21
s_sub_u32 s43,15,s54
SM s46,20480
SM s47,36864
s_cmp_lt_u32 s54,8
s_cselect_b32 s43,s54,s43
)"
R"(s_cselect_b32 s46,s46,s47
s_lshl_b32 s43,s43,6
SA s46,s46,s43
s_lshl_b32 s43,s52,12
SA s46,s46,s43
s_lshr_b32 s47,%19,2
s_lshl_b32 s47,s47,6
SA s47,s47,20480
)"
R"(v_and_b32 v24,7,v20
v_lshlrev_b32 v24,9,v24
v_add_u32 v24,s46,v24
v_and_b32 v25,31,v20
v_lshlrev_b32 v25,9,v25
v_add_u32 v25,s47,v25
v_cmp_gt_u32 vcc,32,v20
s_nop 1
)"
R"(v_cndmask_b32 v24,v25,v24,vcc
s_lshr_b32 s42,%17,1
s_lshl_b32 s42,s42,12
SA s42,s42,%18
v_add_u32 v4,s42,v3
v_add_u32 v5,0x10000,v4
v_add_u32 v6,0x20000,v4
v_lshl_add_u32 v26,v22,2,v23
)"
R"(v_lshlrev_b32 v27,8,v26
v_and_b32 v26,7,v26
v_lshl_add_u32 v27,v26,5,v27
v_and_b32 v26,3,v20
v_lshl_add_u32 v27,v26,3,v27
s_and_b32 s42,%17,1
s_lshl_b32 s42,s42,7
v_xor_b32 v27,s42,v27
)"
R"(SA s42,%18,16384
v_xor_b32 v8,0,v27
v_add_u32 v8,s42,v8
v_add_u32 v12,0x10000,v8
v_add_u32 v116,0x20000,v8
v_xor_b32 v9,32,v27
v_add_u32 v9,s42,v9
v_add_u32 v13,0x10000,v9
)"
R"(v_add_u32 v117,0x20000,v9
v_xor_b32 v10,64,v27
v_add_u32 v10,s42,v10
v_add_u32 v14,0x10000,v10
v_add_u32 v118,0x20000,v10
v_xor_b32 v11,96,v27
v_add_u32 v11,s42,v11
v_add_u32 v15,0x10000,v11
)"
R"(v_add_u32 v119,0x20000,v11
v_lshrrev_b32 v26,1,v21
v_and_b32 v27,1,v20
v_lshlrev_b32 v27,4,v27
v_lshl_add_u32 v27,v22,10,v27
v_add_u32 v28,0,v22
v_xor_b32 v28,v28,v26
v_lshl_add_u32 v16,v28,5,v27
)"
R"(v_add_u32 v16,0,v16
v_add_u32 v28,4,v22
v_xor_b32 v28,v28,v26
v_lshl_add_u32 v17,v28,5,v27
v_add_u32 v17,3072,v17
v_add_u32 v28,0,v22
v_xor_b32 v28,v28,v26
v_lshl_add_u32 v18,v28,5,v27
)"
R"(v_add_u32 v18,6144,v18
v_add_u32 v28,4,v22
v_xor_b32 v28,v28,v26
v_lshl_add_u32 v19,v28,5,v27
v_add_u32 v19,9216,v19
s_lshl_b32 s42,s54,7
s_lshl_b32 s43,s52,5
SA s42,s42,s43
)"
R"(s_lshl_b32 s42,s42,13
s_lshl_b32 s43,%17,12
SA s42,s42,s43
s_mov_b64 s[44:45],%16
SA s44,s44,s42
s_addc_u32 s45,s45,0
SA s44,s44,0x3000000
s_addc_u32 s45,s45,0
)"
R"(s_and_b32 s45,s45,0xffff
SM s46,0x40000
SM s47,0x20000
s_lshl_b32 s42,s52,10
s_lshl_b32 s43,%17,4
SA s42,s42,s43
s_lshl_b32 s42,s42,10
s_lshl_b32 s43,s53,8
)"
R"(SA s42,s42,s43
s_mov_b64 s[48:49],%21
SA s48,s48,s42
s_addc_u32 s49,s49,0
s_and_b32 s49,s49,0xffff
s_sub_u32 s50,0x400000,s42
SM s51,0x20000
s_lshl_b32 s40,%17,12
)"
R"(SA s40,s40,%18
v_lshrrev_b32 v100,3,v20
v_lshlrev_b32 v100,10,v100
v_bfe_u32 v26,v20,1,2
v_xor_b32 v26,v26,v22
v_lshl_add_u32 v100,v26,5,v100
v_and_b32 v26,1,v20
v_lshl_add_u32 v100,v26,4,v100
)"
R"(v_lshl_add_u32 v27,v22,2,v23
v_lshlrev_b32 v27,7,v27
v_lshrrev_b32 v26,1,v23
v_lshl_add_u32 v26,v22,1,v26
v_and_b32 v26,3,v26
v_lshl_add_u32 v27,v26,5,v27
v_and_b32 v26,3,v20
v_lshl_add_u32 v27,v26,3,v27
)"
R"(s_lshl_b32 s69,%17,14
SA s69,s69,%18
SA s69,s69,0
v_xor_b32 v96,0,v27
v_add_u32 v96,s69,v96
v_xor_b32 v97,32,v27
v_add_u32 v97,s69,v97
v_xor_b32 v98,64,v27
)"
R"(v_add_u32 v98,s69,v98
v_xor_b32 v99,96,v27
v_add_u32 v99,s69,v99
s_lshl_b32 s42,%20,7
s_mov_b64 s[56:57],%22
SA s56,s56,s42
s_addc_u32 s57,s57,0
s_and_b32 s57,s57,0xffff
)"
R"(s_sub_u32 s58,0x80000,s42
SM s59,0x20000
s_lshl_b32 s68,%17,17
s_lshl_b32 s42,%19,6
s_lshl_b32 s43,%17,2
SA s42,s42,s43
s_lshl_b32 s42,s42,10
s_mov_b64 s[60:61],%16
)"
R"(SA s60,s60,s42
s_addc_u32 s61,s61,0
SA s60,s60,0x4500000
s_addc_u32 s61,s61,0
SA s62,s60,16384
s_addc_u32 s63,s61,0
SA s64,s62,16384
s_addc_u32 s65,s63,0
)"
R"(SA s66,s64,16384
s_addc_u32 s67,s65,0
v_xor_b32 v26,v22,v21
v_lshlrev_b32 v26,4,v26
v_lshl_add_u32 v101,v21,8,v26
s_lshl_b32 s42,%17,14
SA s42,s42,%18
SA s42,s42,65536
)"
R"(v_add_u32 v101,s42,v101
v_lshrrev_b32 v26,3,v20
s_lshl_b32 s42,%17,3
v_add_u32 v26,s42,v26
v_and_b32 v27,7,v20
v_lshlrev_b32 v28,1,v27
v_and_b32 v29,15,v26
v_xor_b32 v28,v28,v29
)"
R"(v_lshlrev_b32 v28,4,v28
v_lshl_add_u32 v102,v26,8,v28
SA s42,%18,65536
v_add_u32 v102,s42,v102
v_xor_b32 v103,16,v102
v_lshlrev_b32 v104,11,v26
v_lshl_add_u32 v104,v27,4,v104
s_lshl_b32 s42,%19,17
)"
R"(s_lshl_b32 s43,%20,7
SA s42,s42,s43
v_add_u32 v104,s42,v104
v_add_u32 v105,65536,v104
s_mov_b64 s[90:91],%16
SA s90,s90,0x5000000
s_addc_u32 s91,s91,0
SM s89,60000
)"
R"(s_mov_b64 exec,-1
s_nop 1
PL29_%=:
global_load_dword v25,v24,s[90:91] sc1
W vmcnt(0)
v_cmp_ne_u32 vcc,s88,v25
s_sub_u32 s89,s89,1
s_cbranch_scc1 PD30_%=
)"
R"(s_cbranch_vccz PD30_%=
s_sleep 1
s_branch PL29_%=
PD30_%=:
s_mov_b64 exec,-1
s_nop 1
SA m0,s40,0
SM s41,0
)"
R"(B v3,s[44:47],s41 offen offset:0 sc1 lds
B v3,s[44:47],s41 offen offset:1024 sc1 lds
B v3,s[44:47],s41 offen offset:2048 sc1 lds
B v3,s[44:47],s41 offen offset:3072 sc1 lds
SA m0,s40,16384
SM s41,0
B v16,s[48:51],s41 offen offset:0 lds
B v17,s[48:51],s41 offen offset:1024 lds
)"
R"(B v18,s[48:51],s41 offen offset:2048 lds
B v19,s[48:51],s41 offen offset:3072 lds
SA m0,s40,32768
SM s41,16384
B v3,s[44:47],s41 offen offset:0 sc1 lds
B v3,s[44:47],s41 offen offset:1024 sc1 lds
B v3,s[44:47],s41 offen offset:2048 sc1 lds
B v3,s[44:47],s41 offen offset:3072 sc1 lds
)"
R"(SA m0,s40,49152
SM s41,65536
B v16,s[48:51],s41 offen offset:0 lds
B v17,s[48:51],s41 offen offset:1024 lds
B v18,s[48:51],s41 offen offset:2048 lds
B v19,s[48:51],s41 offen offset:3072 lds
W vmcnt(8)
s_barrier
)"
R"(T v[64:65],v8 offset:0
T v[66:67],v8 offset:4096
T v[68:69],v9 offset:0
T v[70:71],v9 offset:4096
T v[72:73],v10 offset:0
T v[74:75],v10 offset:4096
T v[76:77],v11 offset:0
T v[78:79],v11 offset:4096
)"
R"(R v[32:35],v4 offset:0
R v[36:39],v4 offset:1024
R v[40:43],v4 offset:2048
R v[44:47],v4 offset:3072
W lgkmcnt(0)
M a[0:3],v[64:67],v[32:35],0
T v[80:81],v8 offset:8192
SA m0,s40,65536
)"
R"(M a[4:7],v[64:67],v[36:39],0
T v[82:83],v8 offset:12288
SM s41,32768
M a[8:11],v[64:67],v[40:43],0
T v[84:85],v9 offset:8192
B v3,s[44:47],s41 offen offset:0 sc1 lds
M a[12:15],v[64:67],v[44:47],0
T v[86:87],v9 offset:12288
)"
R"(M a[16:19],v[68:71],v[32:35],0
T v[88:89],v10 offset:8192
B v3,s[44:47],s41 offen offset:1024 sc1 lds
M a[20:23],v[68:71],v[36:39],0
T v[90:91],v10 offset:12288
B v3,s[44:47],s41 offen offset:2048 sc1 lds
M a[24:27],v[68:71],v[40:43],0
T v[92:93],v11 offset:8192
)"
R"(B v3,s[44:47],s41 offen offset:3072 sc1 lds
M a[28:31],v[68:71],v[44:47],0
T v[94:95],v11 offset:12288
M a[32:35],v[72:75],v[32:35],0
R v[48:51],v4 offset:8192
SA m0,s40,81920
M a[36:39],v[72:75],v[36:39],0
R v[52:55],v4 offset:9216
)"
R"(SM s41,131072
M a[40:43],v[72:75],v[40:43],0
R v[56:59],v4 offset:10240
B v16,s[48:51],s41 offen offset:0 lds
M a[44:47],v[72:75],v[44:47],0
R v[60:63],v4 offset:11264
M a[48:51],v[76:79],v[32:35],0
B v17,s[48:51],s41 offen offset:1024 lds
)"
R"(M a[52:55],v[76:79],v[36:39],0
B v18,s[48:51],s41 offen offset:2048 lds
M a[56:59],v[76:79],v[40:43],0
B v19,s[48:51],s41 offen offset:3072 lds
M a[60:63],v[76:79],v[44:47],0
W vmcnt(8) lgkmcnt(0)
s_barrier
M a[0:3],v[80:83],v[48:51],a[0:3]
)"
R"(T v[64:65],v8 offset:32768
SA m0,s40,98304
M a[4:7],v[80:83],v[52:55],a[4:7]
T v[66:67],v8 offset:36864
SM s41,49152
M a[8:11],v[80:83],v[56:59],a[8:11]
T v[68:69],v9 offset:32768
B v3,s[44:47],s41 offen offset:0 sc1 lds
)"
R"(M a[12:15],v[80:83],v[60:63],a[12:15]
T v[70:71],v9 offset:36864
B v3,s[44:47],s41 offen offset:1024 sc1 lds
M a[16:19],v[84:87],v[48:51],a[16:19]
T v[72:73],v10 offset:32768
B v3,s[44:47],s41 offen offset:2048 sc1 lds
M a[20:23],v[84:87],v[52:55],a[20:23]
T v[74:75],v10 offset:36864
)"
R"(B v3,s[44:47],s41 offen offset:3072 sc1 lds
M a[24:27],v[84:87],v[56:59],a[24:27]
T v[76:77],v11 offset:32768
SA m0,s40,114688
M a[28:31],v[84:87],v[60:63],a[28:31]
T v[78:79],v11 offset:36864
SM s41,196608
M a[32:35],v[88:91],v[48:51],a[32:35]
)"
R"(R v[32:35],v4 offset:32768
B v16,s[48:51],s41 offen offset:0 lds
M a[36:39],v[88:91],v[52:55],a[36:39]
R v[36:39],v4 offset:33792
B v17,s[48:51],s41 offen offset:1024 lds
M a[40:43],v[88:91],v[56:59],a[40:43]
R v[40:43],v4 offset:34816
B v18,s[48:51],s41 offen offset:2048 lds
)"
R"(M a[44:47],v[88:91],v[60:63],a[44:47]
R v[44:47],v4 offset:35840
B v19,s[48:51],s41 offen offset:3072 lds
M a[48:51],v[92:95],v[48:51],a[48:51]
SA m0,s40,0
SM s41,65536
M a[52:55],v[92:95],v[52:55],a[52:55]
B v3,s[44:47],s41 offen offset:0 sc1 lds
)"
R"(B v3,s[44:47],s41 offen offset:1024 sc1 lds
M a[56:59],v[92:95],v[56:59],a[56:59]
B v3,s[44:47],s41 offen offset:2048 sc1 lds
M a[60:63],v[92:95],v[60:63],a[60:63]
B v3,s[44:47],s41 offen offset:3072 sc1 lds
W lgkmcnt(0)
M a[0:3],v[64:67],v[32:35],a[0:3]
T v[80:81],v8 offset:40960
)"
R"(SA m0,s40,16384
M a[4:7],v[64:67],v[36:39],a[4:7]
T v[82:83],v8 offset:45056
M a[8:11],v[64:67],v[40:43],a[8:11]
T v[84:85],v9 offset:40960
SM s41,262144
M a[12:15],v[64:67],v[44:47],a[12:15]
T v[86:87],v9 offset:45056
)"
R"(M a[16:19],v[68:71],v[32:35],a[16:19]
T v[88:89],v10 offset:40960
M a[20:23],v[68:71],v[36:39],a[20:23]
T v[90:91],v10 offset:45056
B v16,s[48:51],s41 offen offset:0 lds
M a[24:27],v[68:71],v[40:43],a[24:27]
T v[92:93],v11 offset:40960
M a[28:31],v[68:71],v[44:47],a[28:31]
)"
R"(T v[94:95],v11 offset:45056
M a[32:35],v[72:75],v[32:35],a[32:35]
R v[48:51],v4 offset:40960
B v17,s[48:51],s41 offen offset:1024 lds
M a[36:39],v[72:75],v[36:39],a[36:39]
R v[52:55],v4 offset:41984
M a[40:43],v[72:75],v[40:43],a[40:43]
R v[56:59],v4 offset:43008
)"
R"(B v18,s[48:51],s41 offen offset:2048 lds
M a[44:47],v[72:75],v[44:47],a[44:47]
R v[60:63],v4 offset:44032
M a[48:51],v[76:79],v[32:35],a[48:51]
M a[52:55],v[76:79],v[36:39],a[52:55]
B v19,s[48:51],s41 offen offset:3072 lds
M a[56:59],v[76:79],v[40:43],a[56:59]
M a[60:63],v[76:79],v[44:47],a[60:63]
)"
R"(W vmcnt(16) lgkmcnt(0)
s_barrier
M a[0:3],v[80:83],v[48:51],a[0:3]
T v[64:65],v12 offset:0
SA m0,s40,32768
M a[4:7],v[80:83],v[52:55],a[4:7]
T v[66:67],v12 offset:4096
M a[8:11],v[80:83],v[56:59],a[8:11]
)"
R"(T v[68:69],v13 offset:0
SM s41,81920
M a[12:15],v[80:83],v[60:63],a[12:15]
T v[70:71],v13 offset:4096
M a[16:19],v[84:87],v[48:51],a[16:19]
T v[72:73],v14 offset:0
M a[20:23],v[84:87],v[52:55],a[20:23]
T v[74:75],v14 offset:4096
)"
R"(B v3,s[44:47],s41 offen offset:0 sc1 lds
M a[24:27],v[84:87],v[56:59],a[24:27]
T v[76:77],v15 offset:0
M a[28:31],v[84:87],v[60:63],a[28:31]
T v[78:79],v15 offset:4096
M a[32:35],v[88:91],v[48:51],a[32:35]
R v[32:35],v5 offset:0
B v3,s[44:47],s41 offen offset:1024 sc1 lds
)"
R"(M a[36:39],v[88:91],v[52:55],a[36:39]
R v[36:39],v5 offset:1024
M a[40:43],v[88:91],v[56:59],a[40:43]
R v[40:43],v5 offset:2048
B v3,s[44:47],s41 offen offset:2048 sc1 lds
M a[44:47],v[88:91],v[60:63],a[44:47]
R v[44:47],v5 offset:3072
M a[48:51],v[92:95],v[48:51],a[48:51]
)"
R"(M a[52:55],v[92:95],v[52:55],a[52:55]
B v3,s[44:47],s41 offen offset:3072 sc1 lds
M a[56:59],v[92:95],v[56:59],a[56:59]
M a[60:63],v[92:95],v[60:63],a[60:63]
W lgkmcnt(0)
M a[0:3],v[64:67],v[32:35],a[0:3]
T v[80:81],v12 offset:8192
SA m0,s40,49152
)"
R"(M a[4:7],v[64:67],v[36:39],a[4:7]
T v[82:83],v12 offset:12288
M a[8:11],v[64:67],v[40:43],a[8:11]
T v[84:85],v13 offset:8192
SM s41,327680
M a[12:15],v[64:67],v[44:47],a[12:15]
T v[86:87],v13 offset:12288
M a[16:19],v[68:71],v[32:35],a[16:19]
)"
R"(T v[88:89],v14 offset:8192
M a[20:23],v[68:71],v[36:39],a[20:23]
T v[90:91],v14 offset:12288
B v16,s[48:51],s41 offen offset:0 lds
M a[24:27],v[68:71],v[40:43],a[24:27]
T v[92:93],v15 offset:8192
M a[28:31],v[68:71],v[44:47],a[28:31]
T v[94:95],v15 offset:12288
)"
R"(M a[32:35],v[72:75],v[32:35],a[32:35]
R v[48:51],v5 offset:8192
B v17,s[48:51],s41 offen offset:1024 lds
M a[36:39],v[72:75],v[36:39],a[36:39]
R v[52:55],v5 offset:9216
M a[40:43],v[72:75],v[40:43],a[40:43]
R v[56:59],v5 offset:10240
B v18,s[48:51],s41 offen offset:2048 lds
)"
R"(M a[44:47],v[72:75],v[44:47],a[44:47]
R v[60:63],v5 offset:11264
M a[48:51],v[76:79],v[32:35],a[48:51]
M a[52:55],v[76:79],v[36:39],a[52:55]
B v19,s[48:51],s41 offen offset:3072 lds
M a[56:59],v[76:79],v[40:43],a[56:59]
M a[60:63],v[76:79],v[44:47],a[60:63]
W vmcnt(16) lgkmcnt(0)
)"
R"(s_barrier
M a[0:3],v[80:83],v[48:51],a[0:3]
T v[64:65],v12 offset:32768
SA m0,s40,65536
M a[4:7],v[80:83],v[52:55],a[4:7]
T v[66:67],v12 offset:36864
M a[8:11],v[80:83],v[56:59],a[8:11]
T v[68:69],v13 offset:32768
)"
R"(SM s41,98304
M a[12:15],v[80:83],v[60:63],a[12:15]
T v[70:71],v13 offset:36864
M a[16:19],v[84:87],v[48:51],a[16:19]
T v[72:73],v14 offset:32768
M a[20:23],v[84:87],v[52:55],a[20:23]
T v[74:75],v14 offset:36864
B v3,s[44:47],s41 offen offset:0 sc1 lds
)"
R"(M a[24:27],v[84:87],v[56:59],a[24:27]
T v[76:77],v15 offset:32768
M a[28:31],v[84:87],v[60:63],a[28:31]
T v[78:79],v15 offset:36864
M a[32:35],v[88:91],v[48:51],a[32:35]
R v[32:35],v5 offset:32768
B v3,s[44:47],s41 offen offset:1024 sc1 lds
M a[36:39],v[88:91],v[52:55],a[36:39]
)"
R"(R v[36:39],v5 offset:33792
M a[40:43],v[88:91],v[56:59],a[40:43]
R v[40:43],v5 offset:34816
B v3,s[44:47],s41 offen offset:2048 sc1 lds
M a[44:47],v[88:91],v[60:63],a[44:47]
R v[44:47],v5 offset:35840
M a[48:51],v[92:95],v[48:51],a[48:51]
M a[52:55],v[92:95],v[52:55],a[52:55]
)"
R"(B v3,s[44:47],s41 offen offset:3072 sc1 lds
M a[56:59],v[92:95],v[56:59],a[56:59]
M a[60:63],v[92:95],v[60:63],a[60:63]
W lgkmcnt(0)
M a[0:3],v[64:67],v[32:35],a[0:3]
T v[80:81],v12 offset:40960
SA m0,s40,81920
M a[4:7],v[64:67],v[36:39],a[4:7]
)"
R"(T v[82:83],v12 offset:45056
M a[8:11],v[64:67],v[40:43],a[8:11]
T v[84:85],v13 offset:40960
SM s41,393216
M a[12:15],v[64:67],v[44:47],a[12:15]
T v[86:87],v13 offset:45056
M a[16:19],v[68:71],v[32:35],a[16:19]
T v[88:89],v14 offset:40960
)"
R"(M a[20:23],v[68:71],v[36:39],a[20:23]
T v[90:91],v14 offset:45056
B v16,s[48:51],s41 offen offset:0 lds
M a[24:27],v[68:71],v[40:43],a[24:27]
T v[92:93],v15 offset:40960
M a[28:31],v[68:71],v[44:47],a[28:31]
T v[94:95],v15 offset:45056
M a[32:35],v[72:75],v[32:35],a[32:35]
)"
R"(R v[48:51],v5 offset:40960
B v17,s[48:51],s41 offen offset:1024 lds
M a[36:39],v[72:75],v[36:39],a[36:39]
R v[52:55],v5 offset:41984
M a[40:43],v[72:75],v[40:43],a[40:43]
R v[56:59],v5 offset:43008
B v18,s[48:51],s41 offen offset:2048 lds
M a[44:47],v[72:75],v[44:47],a[44:47]
)"
R"(R v[60:63],v5 offset:44032
M a[48:51],v[76:79],v[32:35],a[48:51]
M a[52:55],v[76:79],v[36:39],a[52:55]
B v19,s[48:51],s41 offen offset:3072 lds
M a[56:59],v[76:79],v[40:43],a[56:59]
M a[60:63],v[76:79],v[44:47],a[60:63]
W vmcnt(16) lgkmcnt(0)
s_barrier
)"
R"(M a[0:3],v[80:83],v[48:51],a[0:3]
T v[64:65],v8 offset:0
SA m0,s40,98304
M a[4:7],v[80:83],v[52:55],a[4:7]
T v[66:67],v8 offset:4096
M a[8:11],v[80:83],v[56:59],a[8:11]
T v[68:69],v9 offset:0
SM s41,114688
)"
R"(M a[12:15],v[80:83],v[60:63],a[12:15]
T v[70:71],v9 offset:4096
M a[16:19],v[84:87],v[48:51],a[16:19]
T v[72:73],v10 offset:0
M a[20:23],v[84:87],v[52:55],a[20:23]
T v[74:75],v10 offset:4096
B v3,s[44:47],s41 offen offset:0 sc1 lds
M a[24:27],v[84:87],v[56:59],a[24:27]
)"
R"(T v[76:77],v11 offset:0
M a[28:31],v[84:87],v[60:63],a[28:31]
T v[78:79],v11 offset:4096
M a[32:35],v[88:91],v[48:51],a[32:35]
R v[32:35],v4 offset:0
B v3,s[44:47],s41 offen offset:1024 sc1 lds
M a[36:39],v[88:91],v[52:55],a[36:39]
R v[36:39],v4 offset:1024
)"
R"(M a[40:43],v[88:91],v[56:59],a[40:43]
R v[40:43],v4 offset:2048
B v3,s[44:47],s41 offen offset:2048 sc1 lds
M a[44:47],v[88:91],v[60:63],a[44:47]
R v[44:47],v4 offset:3072
M a[48:51],v[92:95],v[48:51],a[48:51]
M a[52:55],v[92:95],v[52:55],a[52:55]
B v3,s[44:47],s41 offen offset:3072 sc1 lds
)"
R"(M a[56:59],v[92:95],v[56:59],a[56:59]
M a[60:63],v[92:95],v[60:63],a[60:63]
W lgkmcnt(0)
M a[0:3],v[64:67],v[32:35],a[0:3]
T v[80:81],v8 offset:8192
SA m0,s40,114688
M a[4:7],v[64:67],v[36:39],a[4:7]
T v[82:83],v8 offset:12288
)"
R"(M a[8:11],v[64:67],v[40:43],a[8:11]
T v[84:85],v9 offset:8192
SM s41,458752
M a[12:15],v[64:67],v[44:47],a[12:15]
T v[86:87],v9 offset:12288
M a[16:19],v[68:71],v[32:35],a[16:19]
T v[88:89],v10 offset:8192
M a[20:23],v[68:71],v[36:39],a[20:23]
)"
R"(T v[90:91],v10 offset:12288
B v16,s[48:51],s41 offen offset:0 lds
M a[24:27],v[68:71],v[40:43],a[24:27]
T v[92:93],v11 offset:8192
M a[28:31],v[68:71],v[44:47],a[28:31]
T v[94:95],v11 offset:12288
M a[32:35],v[72:75],v[32:35],a[32:35]
R v[48:51],v4 offset:8192
)"
R"(B v17,s[48:51],s41 offen offset:1024 lds
M a[36:39],v[72:75],v[36:39],a[36:39]
R v[52:55],v4 offset:9216
M a[40:43],v[72:75],v[40:43],a[40:43]
R v[56:59],v4 offset:10240
B v18,s[48:51],s41 offen offset:2048 lds
M a[44:47],v[72:75],v[44:47],a[44:47]
R v[60:63],v4 offset:11264
)"
R"(M a[48:51],v[76:79],v[32:35],a[48:51]
M a[52:55],v[76:79],v[36:39],a[52:55]
B v19,s[48:51],s41 offen offset:3072 lds
M a[56:59],v[76:79],v[40:43],a[56:59]
M a[60:63],v[76:79],v[44:47],a[60:63]
W vmcnt(16) lgkmcnt(0)
s_barrier
M a[0:3],v[80:83],v[48:51],a[0:3]
)"
R"(T v[64:65],v8 offset:32768
SA m0,s40,0
M a[4:7],v[80:83],v[52:55],a[4:7]
T v[66:67],v8 offset:36864
M a[8:11],v[80:83],v[56:59],a[8:11]
T v[68:69],v9 offset:32768
SM s41,131072
M a[12:15],v[80:83],v[60:63],a[12:15]
)"
R"(T v[70:71],v9 offset:36864
M a[16:19],v[84:87],v[48:51],a[16:19]
T v[72:73],v10 offset:32768
M a[20:23],v[84:87],v[52:55],a[20:23]
T v[74:75],v10 offset:36864
B v3,s[44:47],s41 offen offset:0 sc1 lds
M a[24:27],v[84:87],v[56:59],a[24:27]
T v[76:77],v11 offset:32768
)"
R"(M a[28:31],v[84:87],v[60:63],a[28:31]
T v[78:79],v11 offset:36864
M a[32:35],v[88:91],v[48:51],a[32:35]
R v[32:35],v4 offset:32768
B v3,s[44:47],s41 offen offset:1024 sc1 lds
M a[36:39],v[88:91],v[52:55],a[36:39]
R v[36:39],v4 offset:33792
M a[40:43],v[88:91],v[56:59],a[40:43]
)"
R"(R v[40:43],v4 offset:34816
B v3,s[44:47],s41 offen offset:2048 sc1 lds
M a[44:47],v[88:91],v[60:63],a[44:47]
R v[44:47],v4 offset:35840
M a[48:51],v[92:95],v[48:51],a[48:51]
M a[52:55],v[92:95],v[52:55],a[52:55]
B v3,s[44:47],s41 offen offset:3072 sc1 lds
M a[56:59],v[92:95],v[56:59],a[56:59]
)"
R"(M a[60:63],v[92:95],v[60:63],a[60:63]
W lgkmcnt(0)
M a[0:3],v[64:67],v[32:35],a[0:3]
T v[80:81],v8 offset:40960
SA m0,s40,16384
M a[4:7],v[64:67],v[36:39],a[4:7]
T v[82:83],v8 offset:45056
M a[8:11],v[64:67],v[40:43],a[8:11]
)"
R"(T v[84:85],v9 offset:40960
SM s41,524288
M a[12:15],v[64:67],v[44:47],a[12:15]
T v[86:87],v9 offset:45056
M a[16:19],v[68:71],v[32:35],a[16:19]
T v[88:89],v10 offset:40960
M a[20:23],v[68:71],v[36:39],a[20:23]
T v[90:91],v10 offset:45056
)"
R"(B v16,s[48:51],s41 offen offset:0 lds
M a[24:27],v[68:71],v[40:43],a[24:27]
T v[92:93],v11 offset:40960
M a[28:31],v[68:71],v[44:47],a[28:31]
T v[94:95],v11 offset:45056
M a[32:35],v[72:75],v[32:35],a[32:35]
R v[48:51],v4 offset:40960
B v17,s[48:51],s41 offen offset:1024 lds
)"
R"(M a[36:39],v[72:75],v[36:39],a[36:39]
R v[52:55],v4 offset:41984
M a[40:43],v[72:75],v[40:43],a[40:43]
R v[56:59],v4 offset:43008
B v18,s[48:51],s41 offen offset:2048 lds
M a[44:47],v[72:75],v[44:47],a[44:47]
R v[60:63],v4 offset:44032
M a[48:51],v[76:79],v[32:35],a[48:51]
)"
R"(M a[52:55],v[76:79],v[36:39],a[52:55]
B v19,s[48:51],s41 offen offset:3072 lds
M a[56:59],v[76:79],v[40:43],a[56:59]
M a[60:63],v[76:79],v[44:47],a[60:63]
W vmcnt(16) lgkmcnt(0)
s_barrier
M a[0:3],v[80:83],v[48:51],a[0:3]
T v[64:65],v12 offset:0
)"
R"(SA m0,s40,32768
M a[4:7],v[80:83],v[52:55],a[4:7]
T v[66:67],v12 offset:4096
M a[8:11],v[80:83],v[56:59],a[8:11]
T v[68:69],v13 offset:0
SM s41,147456
M a[12:15],v[80:83],v[60:63],a[12:15]
T v[70:71],v13 offset:4096
)"
R"(M a[16:19],v[84:87],v[48:51],a[16:19]
T v[72:73],v14 offset:0
M a[20:23],v[84:87],v[52:55],a[20:23]
T v[74:75],v14 offset:4096
B v3,s[44:47],s41 offen offset:0 sc1 lds
M a[24:27],v[84:87],v[56:59],a[24:27]
T v[76:77],v15 offset:0
M a[28:31],v[84:87],v[60:63],a[28:31]
)"
R"(T v[78:79],v15 offset:4096
M a[32:35],v[88:91],v[48:51],a[32:35]
R v[32:35],v5 offset:0
B v3,s[44:47],s41 offen offset:1024 sc1 lds
M a[36:39],v[88:91],v[52:55],a[36:39]
R v[36:39],v5 offset:1024
M a[40:43],v[88:91],v[56:59],a[40:43]
R v[40:43],v5 offset:2048
)"
R"(B v3,s[44:47],s41 offen offset:2048 sc1 lds
M a[44:47],v[88:91],v[60:63],a[44:47]
R v[44:47],v5 offset:3072
M a[48:51],v[92:95],v[48:51],a[48:51]
M a[52:55],v[92:95],v[52:55],a[52:55]
B v3,s[44:47],s41 offen offset:3072 sc1 lds
M a[56:59],v[92:95],v[56:59],a[56:59]
M a[60:63],v[92:95],v[60:63],a[60:63]
)"
R"(W lgkmcnt(0)
M a[0:3],v[64:67],v[32:35],a[0:3]
T v[80:81],v12 offset:8192
SA m0,s40,49152
M a[4:7],v[64:67],v[36:39],a[4:7]
T v[82:83],v12 offset:12288
M a[8:11],v[64:67],v[40:43],a[8:11]
T v[84:85],v13 offset:8192
)"
R"(SM s41,589824
M a[12:15],v[64:67],v[44:47],a[12:15]
T v[86:87],v13 offset:12288
M a[16:19],v[68:71],v[32:35],a[16:19]
T v[88:89],v14 offset:8192
M a[20:23],v[68:71],v[36:39],a[20:23]
T v[90:91],v14 offset:12288
B v16,s[48:51],s41 offen offset:0 lds
)"
R"(M a[24:27],v[68:71],v[40:43],a[24:27]
T v[92:93],v15 offset:8192
M a[28:31],v[68:71],v[44:47],a[28:31]
T v[94:95],v15 offset:12288
M a[32:35],v[72:75],v[32:35],a[32:35]
R v[48:51],v5 offset:8192
B v17,s[48:51],s41 offen offset:1024 lds
M a[36:39],v[72:75],v[36:39],a[36:39]
)"
R"(R v[52:55],v5 offset:9216
M a[40:43],v[72:75],v[40:43],a[40:43]
R v[56:59],v5 offset:10240
B v18,s[48:51],s41 offen offset:2048 lds
M a[44:47],v[72:75],v[44:47],a[44:47]
R v[60:63],v5 offset:11264
M a[48:51],v[76:79],v[32:35],a[48:51]
M a[52:55],v[76:79],v[36:39],a[52:55]
)"
R"(B v19,s[48:51],s41 offen offset:3072 lds
M a[56:59],v[76:79],v[40:43],a[56:59]
M a[60:63],v[76:79],v[44:47],a[60:63]
W vmcnt(16) lgkmcnt(0)
s_barrier
M a[0:3],v[80:83],v[48:51],a[0:3]
T v[64:65],v12 offset:32768
SA m0,s40,65536
)"
R"(M a[4:7],v[80:83],v[52:55],a[4:7]
T v[66:67],v12 offset:36864
M a[8:11],v[80:83],v[56:59],a[8:11]
T v[68:69],v13 offset:32768
SM s41,163840
M a[12:15],v[80:83],v[60:63],a[12:15]
T v[70:71],v13 offset:36864
M a[16:19],v[84:87],v[48:51],a[16:19]
)"
R"(T v[72:73],v14 offset:32768
M a[20:23],v[84:87],v[52:55],a[20:23]
T v[74:75],v14 offset:36864
B v3,s[44:47],s41 offen offset:0 sc1 lds
M a[24:27],v[84:87],v[56:59],a[24:27]
T v[76:77],v15 offset:32768
M a[28:31],v[84:87],v[60:63],a[28:31]
T v[78:79],v15 offset:36864
)"
R"(M a[32:35],v[88:91],v[48:51],a[32:35]
R v[32:35],v5 offset:32768
B v3,s[44:47],s41 offen offset:1024 sc1 lds
M a[36:39],v[88:91],v[52:55],a[36:39]
R v[36:39],v5 offset:33792
M a[40:43],v[88:91],v[56:59],a[40:43]
R v[40:43],v5 offset:34816
B v3,s[44:47],s41 offen offset:2048 sc1 lds
)"
R"(M a[44:47],v[88:91],v[60:63],a[44:47]
R v[44:47],v5 offset:35840
M a[48:51],v[92:95],v[48:51],a[48:51]
M a[52:55],v[92:95],v[52:55],a[52:55]
B v3,s[44:47],s41 offen offset:3072 sc1 lds
M a[56:59],v[92:95],v[56:59],a[56:59]
M a[60:63],v[92:95],v[60:63],a[60:63]
W lgkmcnt(0)
)"
R"(M a[0:3],v[64:67],v[32:35],a[0:3]
T v[80:81],v12 offset:40960
SA m0,s40,81920
M a[4:7],v[64:67],v[36:39],a[4:7]
T v[82:83],v12 offset:45056
M a[8:11],v[64:67],v[40:43],a[8:11]
T v[84:85],v13 offset:40960
SM s41,655360
)"
R"(M a[12:15],v[64:67],v[44:47],a[12:15]
T v[86:87],v13 offset:45056
M a[16:19],v[68:71],v[32:35],a[16:19]
T v[88:89],v14 offset:40960
M a[20:23],v[68:71],v[36:39],a[20:23]
T v[90:91],v14 offset:45056
B v16,s[48:51],s41 offen offset:0 lds
M a[24:27],v[68:71],v[40:43],a[24:27]
)"
R"(T v[92:93],v15 offset:40960
M a[28:31],v[68:71],v[44:47],a[28:31]
T v[94:95],v15 offset:45056
M a[32:35],v[72:75],v[32:35],a[32:35]
R v[48:51],v5 offset:40960
B v17,s[48:51],s41 offen offset:1024 lds
M a[36:39],v[72:75],v[36:39],a[36:39]
R v[52:55],v5 offset:41984
)"
R"(M a[40:43],v[72:75],v[40:43],a[40:43]
R v[56:59],v5 offset:43008
B v18,s[48:51],s41 offen offset:2048 lds
M a[44:47],v[72:75],v[44:47],a[44:47]
R v[60:63],v5 offset:44032
M a[48:51],v[76:79],v[32:35],a[48:51]
M a[52:55],v[76:79],v[36:39],a[52:55]
B v19,s[48:51],s41 offen offset:3072 lds
)"
R"(M a[56:59],v[76:79],v[40:43],a[56:59]
M a[60:63],v[76:79],v[44:47],a[60:63]
W vmcnt(16) lgkmcnt(0)
s_barrier
M a[0:3],v[80:83],v[48:51],a[0:3]
T v[64:65],v8 offset:0
SA m0,s40,98304
M a[4:7],v[80:83],v[52:55],a[4:7]
)"
R"(T v[66:67],v8 offset:4096
M a[8:11],v[80:83],v[56:59],a[8:11]
T v[68:69],v9 offset:0
SM s41,180224
M a[12:15],v[80:83],v[60:63],a[12:15]
T v[70:71],v9 offset:4096
M a[16:19],v[84:87],v[48:51],a[16:19]
T v[72:73],v10 offset:0
)"
R"(M a[20:23],v[84:87],v[52:55],a[20:23]
T v[74:75],v10 offset:4096
B v3,s[44:47],s41 offen offset:0 sc1 lds
M a[24:27],v[84:87],v[56:59],a[24:27]
T v[76:77],v11 offset:0
M a[28:31],v[84:87],v[60:63],a[28:31]
T v[78:79],v11 offset:4096
M a[32:35],v[88:91],v[48:51],a[32:35]
)"
R"(R v[32:35],v4 offset:0
B v3,s[44:47],s41 offen offset:1024 sc1 lds
M a[36:39],v[88:91],v[52:55],a[36:39]
R v[36:39],v4 offset:1024
M a[40:43],v[88:91],v[56:59],a[40:43]
R v[40:43],v4 offset:2048
B v3,s[44:47],s41 offen offset:2048 sc1 lds
M a[44:47],v[88:91],v[60:63],a[44:47]
)"
R"(R v[44:47],v4 offset:3072
M a[48:51],v[92:95],v[48:51],a[48:51]
M a[52:55],v[92:95],v[52:55],a[52:55]
B v3,s[44:47],s41 offen offset:3072 sc1 lds
M a[56:59],v[92:95],v[56:59],a[56:59]
M a[60:63],v[92:95],v[60:63],a[60:63]
W lgkmcnt(0)
M a[0:3],v[64:67],v[32:35],a[0:3]
)"
R"(T v[80:81],v8 offset:8192
SA m0,s40,114688
M a[4:7],v[64:67],v[36:39],a[4:7]
T v[82:83],v8 offset:12288
M a[8:11],v[64:67],v[40:43],a[8:11]
T v[84:85],v9 offset:8192
SM s41,720896
M a[12:15],v[64:67],v[44:47],a[12:15]
)"
R"(T v[86:87],v9 offset:12288
M a[16:19],v[68:71],v[32:35],a[16:19]
T v[88:89],v10 offset:8192
M a[20:23],v[68:71],v[36:39],a[20:23]
T v[90:91],v10 offset:12288
B v16,s[48:51],s41 offen offset:0 lds
M a[24:27],v[68:71],v[40:43],a[24:27]
T v[92:93],v11 offset:8192
)"
R"(M a[28:31],v[68:71],v[44:47],a[28:31]
T v[94:95],v11 offset:12288
M a[32:35],v[72:75],v[32:35],a[32:35]
R v[48:51],v4 offset:8192
B v17,s[48:51],s41 offen offset:1024 lds
M a[36:39],v[72:75],v[36:39],a[36:39]
R v[52:55],v4 offset:9216
M a[40:43],v[72:75],v[40:43],a[40:43]
)"
R"(R v[56:59],v4 offset:10240
B v18,s[48:51],s41 offen offset:2048 lds
M a[44:47],v[72:75],v[44:47],a[44:47]
R v[60:63],v4 offset:11264
M a[48:51],v[76:79],v[32:35],a[48:51]
M a[52:55],v[76:79],v[36:39],a[52:55]
B v19,s[48:51],s41 offen offset:3072 lds
M a[56:59],v[76:79],v[40:43],a[56:59]
)"
R"(M a[60:63],v[76:79],v[44:47],a[60:63]
W vmcnt(16) lgkmcnt(0)
s_barrier
M a[0:3],v[80:83],v[48:51],a[0:3]
T v[64:65],v8 offset:32768
SA m0,s40,0
M a[4:7],v[80:83],v[52:55],a[4:7]
T v[66:67],v8 offset:36864
)"
R"(M a[8:11],v[80:83],v[56:59],a[8:11]
T v[68:69],v9 offset:32768
SM s41,196608
M a[12:15],v[80:83],v[60:63],a[12:15]
T v[70:71],v9 offset:36864
M a[16:19],v[84:87],v[48:51],a[16:19]
T v[72:73],v10 offset:32768
M a[20:23],v[84:87],v[52:55],a[20:23]
)"
R"(T v[74:75],v10 offset:36864
B v3,s[44:47],s41 offen offset:0 sc1 lds
M a[24:27],v[84:87],v[56:59],a[24:27]
T v[76:77],v11 offset:32768
M a[28:31],v[84:87],v[60:63],a[28:31]
T v[78:79],v11 offset:36864
M a[32:35],v[88:91],v[48:51],a[32:35]
R v[32:35],v4 offset:32768
)"
R"(B v3,s[44:47],s41 offen offset:1024 sc1 lds
M a[36:39],v[88:91],v[52:55],a[36:39]
R v[36:39],v4 offset:33792
M a[40:43],v[88:91],v[56:59],a[40:43]
R v[40:43],v4 offset:34816
B v3,s[44:47],s41 offen offset:2048 sc1 lds
M a[44:47],v[88:91],v[60:63],a[44:47]
R v[44:47],v4 offset:35840
)"
R"(M a[48:51],v[92:95],v[48:51],a[48:51]
M a[52:55],v[92:95],v[52:55],a[52:55]
B v3,s[44:47],s41 offen offset:3072 sc1 lds
M a[56:59],v[92:95],v[56:59],a[56:59]
M a[60:63],v[92:95],v[60:63],a[60:63]
W lgkmcnt(0)
M a[0:3],v[64:67],v[32:35],a[0:3]
T v[80:81],v8 offset:40960
)"
R"(SA m0,s40,16384
M a[4:7],v[64:67],v[36:39],a[4:7]
T v[82:83],v8 offset:45056
M a[8:11],v[64:67],v[40:43],a[8:11]
T v[84:85],v9 offset:40960
SM s41,786432
M a[12:15],v[64:67],v[44:47],a[12:15]
T v[86:87],v9 offset:45056
)"
R"(M a[16:19],v[68:71],v[32:35],a[16:19]
T v[88:89],v10 offset:40960
M a[20:23],v[68:71],v[36:39],a[20:23]
T v[90:91],v10 offset:45056
B v16,s[48:51],s41 offen offset:0 lds
M a[24:27],v[68:71],v[40:43],a[24:27]
T v[92:93],v11 offset:40960
M a[28:31],v[68:71],v[44:47],a[28:31]
)"
R"(T v[94:95],v11 offset:45056
M a[32:35],v[72:75],v[32:35],a[32:35]
R v[48:51],v4 offset:40960
B v17,s[48:51],s41 offen offset:1024 lds
M a[36:39],v[72:75],v[36:39],a[36:39]
R v[52:55],v4 offset:41984
M a[40:43],v[72:75],v[40:43],a[40:43]
R v[56:59],v4 offset:43008
)"
R"(B v18,s[48:51],s41 offen offset:2048 lds
M a[44:47],v[72:75],v[44:47],a[44:47]
R v[60:63],v4 offset:44032
M a[48:51],v[76:79],v[32:35],a[48:51]
M a[52:55],v[76:79],v[36:39],a[52:55]
B v19,s[48:51],s41 offen offset:3072 lds
M a[56:59],v[76:79],v[40:43],a[56:59]
M a[60:63],v[76:79],v[44:47],a[60:63]
)"
R"(W vmcnt(16) lgkmcnt(0)
s_barrier
M a[0:3],v[80:83],v[48:51],a[0:3]
T v[64:65],v12 offset:0
SA m0,s40,32768
M a[4:7],v[80:83],v[52:55],a[4:7]
T v[66:67],v12 offset:4096
M a[8:11],v[80:83],v[56:59],a[8:11]
)"
R"(T v[68:69],v13 offset:0
SM s41,212992
M a[12:15],v[80:83],v[60:63],a[12:15]
T v[70:71],v13 offset:4096
M a[16:19],v[84:87],v[48:51],a[16:19]
T v[72:73],v14 offset:0
M a[20:23],v[84:87],v[52:55],a[20:23]
T v[74:75],v14 offset:4096
)"
R"(B v3,s[44:47],s41 offen offset:0 sc1 lds
M a[24:27],v[84:87],v[56:59],a[24:27]
T v[76:77],v15 offset:0
M a[28:31],v[84:87],v[60:63],a[28:31]
T v[78:79],v15 offset:4096
M a[32:35],v[88:91],v[48:51],a[32:35]
R v[32:35],v5 offset:0
B v3,s[44:47],s41 offen offset:1024 sc1 lds
)"
R"(M a[36:39],v[88:91],v[52:55],a[36:39]
R v[36:39],v5 offset:1024
M a[40:43],v[88:91],v[56:59],a[40:43]
R v[40:43],v5 offset:2048
B v3,s[44:47],s41 offen offset:2048 sc1 lds
M a[44:47],v[88:91],v[60:63],a[44:47]
R v[44:47],v5 offset:3072
M a[48:51],v[92:95],v[48:51],a[48:51]
)"
R"(M a[52:55],v[92:95],v[52:55],a[52:55]
B v3,s[44:47],s41 offen offset:3072 sc1 lds
M a[56:59],v[92:95],v[56:59],a[56:59]
M a[60:63],v[92:95],v[60:63],a[60:63]
W lgkmcnt(0)
M a[0:3],v[64:67],v[32:35],a[0:3]
T v[80:81],v12 offset:8192
SA m0,s40,49152
)"
R"(M a[4:7],v[64:67],v[36:39],a[4:7]
T v[82:83],v12 offset:12288
M a[8:11],v[64:67],v[40:43],a[8:11]
T v[84:85],v13 offset:8192
SM s41,851968
M a[12:15],v[64:67],v[44:47],a[12:15]
T v[86:87],v13 offset:12288
M a[16:19],v[68:71],v[32:35],a[16:19]
)"
R"(T v[88:89],v14 offset:8192
M a[20:23],v[68:71],v[36:39],a[20:23]
T v[90:91],v14 offset:12288
B v16,s[48:51],s41 offen offset:0 lds
M a[24:27],v[68:71],v[40:43],a[24:27]
T v[92:93],v15 offset:8192
M a[28:31],v[68:71],v[44:47],a[28:31]
T v[94:95],v15 offset:12288
)"
R"(M a[32:35],v[72:75],v[32:35],a[32:35]
R v[48:51],v5 offset:8192
B v17,s[48:51],s41 offen offset:1024 lds
M a[36:39],v[72:75],v[36:39],a[36:39]
R v[52:55],v5 offset:9216
M a[40:43],v[72:75],v[40:43],a[40:43]
R v[56:59],v5 offset:10240
B v18,s[48:51],s41 offen offset:2048 lds
)"
R"(M a[44:47],v[72:75],v[44:47],a[44:47]
R v[60:63],v5 offset:11264
M a[48:51],v[76:79],v[32:35],a[48:51]
M a[52:55],v[76:79],v[36:39],a[52:55]
B v19,s[48:51],s41 offen offset:3072 lds
M a[56:59],v[76:79],v[40:43],a[56:59]
M a[60:63],v[76:79],v[44:47],a[60:63]
W vmcnt(16) lgkmcnt(0)
)"
R"(s_barrier
M a[0:3],v[80:83],v[48:51],a[0:3]
T v[64:65],v12 offset:32768
SA m0,s40,65536
M a[4:7],v[80:83],v[52:55],a[4:7]
T v[66:67],v12 offset:36864
M a[8:11],v[80:83],v[56:59],a[8:11]
T v[68:69],v13 offset:32768
)"
R"(SM s41,229376
M a[12:15],v[80:83],v[60:63],a[12:15]
T v[70:71],v13 offset:36864
M a[16:19],v[84:87],v[48:51],a[16:19]
T v[72:73],v14 offset:32768
M a[20:23],v[84:87],v[52:55],a[20:23]
T v[74:75],v14 offset:36864
B v3,s[44:47],s41 offen offset:0 sc1 lds
)"
R"(M a[24:27],v[84:87],v[56:59],a[24:27]
T v[76:77],v15 offset:32768
M a[28:31],v[84:87],v[60:63],a[28:31]
T v[78:79],v15 offset:36864
M a[32:35],v[88:91],v[48:51],a[32:35]
R v[32:35],v5 offset:32768
B v3,s[44:47],s41 offen offset:1024 sc1 lds
M a[36:39],v[88:91],v[52:55],a[36:39]
)"
R"(R v[36:39],v5 offset:33792
M a[40:43],v[88:91],v[56:59],a[40:43]
R v[40:43],v5 offset:34816
B v3,s[44:47],s41 offen offset:2048 sc1 lds
M a[44:47],v[88:91],v[60:63],a[44:47]
R v[44:47],v5 offset:35840
M a[48:51],v[92:95],v[48:51],a[48:51]
M a[52:55],v[92:95],v[52:55],a[52:55]
)"
R"(B v3,s[44:47],s41 offen offset:3072 sc1 lds
M a[56:59],v[92:95],v[56:59],a[56:59]
M a[60:63],v[92:95],v[60:63],a[60:63]
W lgkmcnt(0)
M a[0:3],v[64:67],v[32:35],a[0:3]
T v[80:81],v12 offset:40960
SA m0,s40,81920
M a[4:7],v[64:67],v[36:39],a[4:7]
)"
R"(T v[82:83],v12 offset:45056
M a[8:11],v[64:67],v[40:43],a[8:11]
T v[84:85],v13 offset:40960
SM s41,917504
M a[12:15],v[64:67],v[44:47],a[12:15]
T v[86:87],v13 offset:45056
M a[16:19],v[68:71],v[32:35],a[16:19]
T v[88:89],v14 offset:40960
)"
R"(M a[20:23],v[68:71],v[36:39],a[20:23]
T v[90:91],v14 offset:45056
B v16,s[48:51],s41 offen offset:0 lds
M a[24:27],v[68:71],v[40:43],a[24:27]
T v[92:93],v15 offset:40960
M a[28:31],v[68:71],v[44:47],a[28:31]
T v[94:95],v15 offset:45056
M a[32:35],v[72:75],v[32:35],a[32:35]
)"
R"(R v[48:51],v5 offset:40960
B v17,s[48:51],s41 offen offset:1024 lds
M a[36:39],v[72:75],v[36:39],a[36:39]
R v[52:55],v5 offset:41984
M a[40:43],v[72:75],v[40:43],a[40:43]
R v[56:59],v5 offset:43008
B v18,s[48:51],s41 offen offset:2048 lds
M a[44:47],v[72:75],v[44:47],a[44:47]
)"
R"(R v[60:63],v5 offset:44032
M a[48:51],v[76:79],v[32:35],a[48:51]
M a[52:55],v[76:79],v[36:39],a[52:55]
B v19,s[48:51],s41 offen offset:3072 lds
M a[56:59],v[76:79],v[40:43],a[56:59]
M a[60:63],v[76:79],v[44:47],a[60:63]
W vmcnt(16) lgkmcnt(0)
s_barrier
)"
R"(M a[0:3],v[80:83],v[48:51],a[0:3]
T v[64:65],v8 offset:0
SA m0,s40,98304
M a[4:7],v[80:83],v[52:55],a[4:7]
T v[66:67],v8 offset:4096
M a[8:11],v[80:83],v[56:59],a[8:11]
T v[68:69],v9 offset:0
SM s41,245760
)"
R"(M a[12:15],v[80:83],v[60:63],a[12:15]
T v[70:71],v9 offset:4096
M a[16:19],v[84:87],v[48:51],a[16:19]
T v[72:73],v10 offset:0
M a[20:23],v[84:87],v[52:55],a[20:23]
T v[74:75],v10 offset:4096
B v3,s[44:47],s41 offen offset:0 sc1 lds
M a[24:27],v[84:87],v[56:59],a[24:27]
)"
R"(T v[76:77],v11 offset:0
M a[28:31],v[84:87],v[60:63],a[28:31]
T v[78:79],v11 offset:4096
M a[32:35],v[88:91],v[48:51],a[32:35]
R v[32:35],v4 offset:0
B v3,s[44:47],s41 offen offset:1024 sc1 lds
M a[36:39],v[88:91],v[52:55],a[36:39]
R v[36:39],v4 offset:1024
)"
R"(M a[40:43],v[88:91],v[56:59],a[40:43]
R v[40:43],v4 offset:2048
B v3,s[44:47],s41 offen offset:2048 sc1 lds
M a[44:47],v[88:91],v[60:63],a[44:47]
R v[44:47],v4 offset:3072
M a[48:51],v[92:95],v[48:51],a[48:51]
M a[52:55],v[92:95],v[52:55],a[52:55]
B v3,s[44:47],s41 offen offset:3072 sc1 lds
)"
R"(M a[56:59],v[92:95],v[56:59],a[56:59]
M a[60:63],v[92:95],v[60:63],a[60:63]
W lgkmcnt(0)
M a[0:3],v[64:67],v[32:35],a[0:3]
T v[80:81],v8 offset:8192
SA m0,s40,114688
M a[4:7],v[64:67],v[36:39],a[4:7]
T v[82:83],v8 offset:12288
)"
R"(M a[8:11],v[64:67],v[40:43],a[8:11]
T v[84:85],v9 offset:8192
SM s41,983040
M a[12:15],v[64:67],v[44:47],a[12:15]
T v[86:87],v9 offset:12288
M a[16:19],v[68:71],v[32:35],a[16:19]
T v[88:89],v10 offset:8192
M a[20:23],v[68:71],v[36:39],a[20:23]
)"
R"(T v[90:91],v10 offset:12288
B v16,s[48:51],s41 offen offset:0 lds
M a[24:27],v[68:71],v[40:43],a[24:27]
T v[92:93],v11 offset:8192
M a[28:31],v[68:71],v[44:47],a[28:31]
T v[94:95],v11 offset:12288
M a[32:35],v[72:75],v[32:35],a[32:35]
R v[48:51],v4 offset:8192
)"
R"(B v17,s[48:51],s41 offen offset:1024 lds
M a[36:39],v[72:75],v[36:39],a[36:39]
R v[52:55],v4 offset:9216
M a[40:43],v[72:75],v[40:43],a[40:43]
R v[56:59],v4 offset:10240
B v18,s[48:51],s41 offen offset:2048 lds
M a[44:47],v[72:75],v[44:47],a[44:47]
R v[60:63],v4 offset:11264
)"
R"(M a[48:51],v[76:79],v[32:35],a[48:51]
M a[52:55],v[76:79],v[36:39],a[52:55]
B v19,s[48:51],s41 offen offset:3072 lds
M a[56:59],v[76:79],v[40:43],a[56:59]
M a[60:63],v[76:79],v[44:47],a[60:63]
W vmcnt(16) lgkmcnt(0)
s_barrier
M a[0:3],v[80:83],v[48:51],a[0:3]
)"
R"(T v[64:65],v8 offset:32768
M a[4:7],v[80:83],v[52:55],a[4:7]
T v[66:67],v8 offset:36864
M a[8:11],v[80:83],v[56:59],a[8:11]
T v[68:69],v9 offset:32768
M a[12:15],v[80:83],v[60:63],a[12:15]
T v[70:71],v9 offset:36864
M a[16:19],v[84:87],v[48:51],a[16:19]
)"
R"(T v[72:73],v10 offset:32768
M a[20:23],v[84:87],v[52:55],a[20:23]
T v[74:75],v10 offset:36864
M a[24:27],v[84:87],v[56:59],a[24:27]
T v[76:77],v11 offset:32768
M a[28:31],v[84:87],v[60:63],a[28:31]
T v[78:79],v11 offset:36864
M a[32:35],v[88:91],v[48:51],a[32:35]
)"
R"(R v[32:35],v4 offset:32768
M a[36:39],v[88:91],v[52:55],a[36:39]
R v[36:39],v4 offset:33792
M a[40:43],v[88:91],v[56:59],a[40:43]
R v[40:43],v4 offset:34816
M a[44:47],v[88:91],v[60:63],a[44:47]
R v[44:47],v4 offset:35840
M a[48:51],v[92:95],v[48:51],a[48:51]
)"
R"(M a[52:55],v[92:95],v[52:55],a[52:55]
M a[56:59],v[92:95],v[56:59],a[56:59]
M a[60:63],v[92:95],v[60:63],a[60:63]
W lgkmcnt(0)
M a[0:3],v[64:67],v[32:35],a[0:3]
T v[80:81],v8 offset:40960
M a[4:7],v[64:67],v[36:39],a[4:7]
T v[82:83],v8 offset:45056
)"
R"(M a[8:11],v[64:67],v[40:43],a[8:11]
T v[84:85],v9 offset:40960
M a[12:15],v[64:67],v[44:47],a[12:15]
T v[86:87],v9 offset:45056
M a[16:19],v[68:71],v[32:35],a[16:19]
T v[88:89],v10 offset:40960
M a[20:23],v[68:71],v[36:39],a[20:23]
T v[90:91],v10 offset:45056
)"
R"(M a[24:27],v[68:71],v[40:43],a[24:27]
T v[92:93],v11 offset:40960
M a[28:31],v[68:71],v[44:47],a[28:31]
T v[94:95],v11 offset:45056
M a[32:35],v[72:75],v[32:35],a[32:35]
R v[48:51],v4 offset:40960
M a[36:39],v[72:75],v[36:39],a[36:39]
R v[52:55],v4 offset:41984
)"
R"(M a[40:43],v[72:75],v[40:43],a[40:43]
R v[56:59],v4 offset:43008
M a[44:47],v[72:75],v[44:47],a[44:47]
R v[60:63],v4 offset:44032
M a[48:51],v[76:79],v[32:35],a[48:51]
M a[52:55],v[76:79],v[36:39],a[52:55]
M a[56:59],v[76:79],v[40:43],a[56:59]
M a[60:63],v[76:79],v[44:47],a[60:63]
)"
R"(W vmcnt(8) lgkmcnt(0)
s_barrier
M a[0:3],v[80:83],v[48:51],a[0:3]
T v[64:65],v12 offset:0
M a[4:7],v[80:83],v[52:55],a[4:7]
T v[66:67],v12 offset:4096
M a[8:11],v[80:83],v[56:59],a[8:11]
T v[68:69],v13 offset:0
)"
R"(M a[12:15],v[80:83],v[60:63],a[12:15]
T v[70:71],v13 offset:4096
M a[16:19],v[84:87],v[48:51],a[16:19]
T v[72:73],v14 offset:0
M a[20:23],v[84:87],v[52:55],a[20:23]
T v[74:75],v14 offset:4096
M a[24:27],v[84:87],v[56:59],a[24:27]
T v[76:77],v15 offset:0
)"
R"(M a[28:31],v[84:87],v[60:63],a[28:31]
T v[78:79],v15 offset:4096
M a[32:35],v[88:91],v[48:51],a[32:35]
R v[32:35],v5 offset:0
M a[36:39],v[88:91],v[52:55],a[36:39]
R v[36:39],v5 offset:1024
M a[40:43],v[88:91],v[56:59],a[40:43]
R v[40:43],v5 offset:2048
)"
R"(M a[44:47],v[88:91],v[60:63],a[44:47]
R v[44:47],v5 offset:3072
M a[48:51],v[92:95],v[48:51],a[48:51]
M a[52:55],v[92:95],v[52:55],a[52:55]
M a[56:59],v[92:95],v[56:59],a[56:59]
M a[60:63],v[92:95],v[60:63],a[60:63]
W lgkmcnt(0)
M a[0:3],v[64:67],v[32:35],a[0:3]
)"
R"(T v[80:81],v12 offset:8192
M a[4:7],v[64:67],v[36:39],a[4:7]
T v[82:83],v12 offset:12288
M a[8:11],v[64:67],v[40:43],a[8:11]
T v[84:85],v13 offset:8192
M a[12:15],v[64:67],v[44:47],a[12:15]
T v[86:87],v13 offset:12288
M a[16:19],v[68:71],v[32:35],a[16:19]
)"
R"(T v[88:89],v14 offset:8192
M a[20:23],v[68:71],v[36:39],a[20:23]
T v[90:91],v14 offset:12288
M a[24:27],v[68:71],v[40:43],a[24:27]
T v[92:93],v15 offset:8192
M a[28:31],v[68:71],v[44:47],a[28:31]
T v[94:95],v15 offset:12288
M a[32:35],v[72:75],v[32:35],a[32:35]
)"
R"(R v[48:51],v5 offset:8192
M a[36:39],v[72:75],v[36:39],a[36:39]
R v[52:55],v5 offset:9216
M a[40:43],v[72:75],v[40:43],a[40:43]
R v[56:59],v5 offset:10240
M a[44:47],v[72:75],v[44:47],a[44:47]
R v[60:63],v5 offset:11264
M a[48:51],v[76:79],v[32:35],a[48:51]
)"
R"(M a[52:55],v[76:79],v[36:39],a[52:55]
M a[56:59],v[76:79],v[40:43],a[56:59]
M a[60:63],v[76:79],v[44:47],a[60:63]
W vmcnt(0) lgkmcnt(0)
s_barrier
M a[0:3],v[80:83],v[48:51],a[0:3]
T v[64:65],v12 offset:32768
GL v[128:131],v3,s[60:61] offset:0 sc1
)"
R"(M a[4:7],v[80:83],v[52:55],a[4:7]
T v[66:67],v12 offset:36864
GL v[132:135],v3,s[62:63] offset:0 sc1
M a[8:11],v[80:83],v[56:59],a[8:11]
T v[68:69],v13 offset:32768
GL v[136:139],v3,s[64:65] offset:0 sc1
M a[12:15],v[80:83],v[60:63],a[12:15]
T v[70:71],v13 offset:36864
)"
R"(GL v[140:143],v3,s[66:67] offset:0 sc1
M a[16:19],v[84:87],v[48:51],a[16:19]
T v[72:73],v14 offset:32768
SA m0,s69,0
M a[20:23],v[84:87],v[52:55],a[20:23]
T v[74:75],v14 offset:36864
SA s41,s68,0
M a[24:27],v[84:87],v[56:59],a[24:27]
)"
R"(T v[76:77],v15 offset:32768
B v100,s[56:59],s41 offen lds
M a[28:31],v[84:87],v[60:63],a[28:31]
T v[78:79],v15 offset:36864
SA m0,s69,1024
M a[32:35],v[88:91],v[48:51],a[32:35]
R v[32:35],v5 offset:32768
SA s41,s68,8192
)"
R"(M a[36:39],v[88:91],v[52:55],a[36:39]
R v[36:39],v5 offset:33792
B v100,s[56:59],s41 offen lds
M a[40:43],v[88:91],v[56:59],a[40:43]
R v[40:43],v5 offset:34816
SA m0,s69,2048
M a[44:47],v[88:91],v[60:63],a[44:47]
R v[44:47],v5 offset:35840
)"
R"(SA s41,s68,16384
M a[48:51],v[92:95],v[48:51],a[48:51]
B v100,s[56:59],s41 offen lds
SA m0,s69,3072
M a[52:55],v[92:95],v[52:55],a[52:55]
SA s41,s68,24576
B v100,s[56:59],s41 offen lds
M a[56:59],v[92:95],v[56:59],a[56:59]
)"
R"(GL v[144:147],v3,s[60:61] offset:1024 sc1
M a[60:63],v[92:95],v[60:63],a[60:63]
GL v[148:151],v3,s[62:63] offset:1024 sc1
W lgkmcnt(0)
M a[0:3],v[64:67],v[32:35],a[0:3]
T v[80:81],v12 offset:40960
GL v[152:155],v3,s[64:65] offset:1024 sc1
M a[4:7],v[64:67],v[36:39],a[4:7]
)"
R"(T v[82:83],v12 offset:45056
GL v[156:159],v3,s[66:67] offset:1024 sc1
M a[8:11],v[64:67],v[40:43],a[8:11]
T v[84:85],v13 offset:40960
SA m0,s69,4096
M a[12:15],v[64:67],v[44:47],a[12:15]
T v[86:87],v13 offset:45056
SA s41,s68,32768
)"
R"(M a[16:19],v[68:71],v[32:35],a[16:19]
T v[88:89],v14 offset:40960
B v100,s[56:59],s41 offen lds
M a[20:23],v[68:71],v[36:39],a[20:23]
T v[90:91],v14 offset:45056
SA m0,s69,5120
M a[24:27],v[68:71],v[40:43],a[24:27]
T v[92:93],v15 offset:40960
)"
R"(SA s41,s68,40960
B v100,s[56:59],s41 offen lds
M a[28:31],v[68:71],v[44:47],a[28:31]
T v[94:95],v15 offset:45056
SA m0,s69,6144
M a[32:35],v[72:75],v[32:35],a[32:35]
R v[48:51],v5 offset:40960
SA s41,s68,49152
)"
R"(M a[36:39],v[72:75],v[36:39],a[36:39]
R v[52:55],v5 offset:41984
B v100,s[56:59],s41 offen lds
SA m0,s69,7168
M a[40:43],v[72:75],v[40:43],a[40:43]
R v[56:59],v5 offset:43008
SA s41,s68,57344
M a[44:47],v[72:75],v[44:47],a[44:47]
)"
R"(R v[60:63],v5 offset:44032
B v100,s[56:59],s41 offen lds
M a[48:51],v[76:79],v[32:35],a[48:51]
GL v[160:163],v3,s[60:61] offset:2048 sc1
GL v[164:167],v3,s[62:63] offset:2048 sc1
M a[52:55],v[76:79],v[36:39],a[52:55]
GL v[168:171],v3,s[64:65] offset:2048 sc1
GL v[172:175],v3,s[66:67] offset:2048 sc1
)"
R"(M a[56:59],v[76:79],v[40:43],a[56:59]
SA m0,s69,8192
SA s41,s68,65536
M a[60:63],v[76:79],v[44:47],a[60:63]
B v100,s[56:59],s41 offen lds
W lgkmcnt(0)
M a[0:3],v[80:83],v[48:51],a[0:3]
SA m0,s69,9216
)"
R"(SA s41,s68,73728
M a[4:7],v[80:83],v[52:55],a[4:7]
B v100,s[56:59],s41 offen lds
SA m0,s69,10240
M a[8:11],v[80:83],v[56:59],a[8:11]
SA s41,s68,81920
M a[12:15],v[80:83],v[60:63],a[12:15]
B v100,s[56:59],s41 offen lds
)"
R"(SA m0,s69,11264
M a[16:19],v[84:87],v[48:51],a[16:19]
SA s41,s68,90112
M a[20:23],v[84:87],v[52:55],a[20:23]
B v100,s[56:59],s41 offen lds
GL v[176:179],v3,s[60:61] offset:3072 sc1
M a[24:27],v[84:87],v[56:59],a[24:27]
GL v[180:183],v3,s[62:63] offset:3072 sc1
)"
R"(M a[28:31],v[84:87],v[60:63],a[28:31]
GL v[184:187],v3,s[64:65] offset:3072 sc1
GL v[188:191],v3,s[66:67] offset:3072 sc1
M a[32:35],v[88:91],v[48:51],a[32:35]
SA m0,s69,12288
SA s41,s68,98304
M a[36:39],v[88:91],v[52:55],a[36:39]
B v100,s[56:59],s41 offen lds
)"
R"(M a[40:43],v[88:91],v[56:59],a[40:43]
SA m0,s69,13312
SA s41,s68,106496
M a[44:47],v[88:91],v[60:63],a[44:47]
B v100,s[56:59],s41 offen lds
M a[48:51],v[92:95],v[48:51],a[48:51]
SA m0,s69,14336
SA s41,s68,114688
)"
R"(M a[52:55],v[92:95],v[52:55],a[52:55]
B v100,s[56:59],s41 offen lds
M a[56:59],v[92:95],v[56:59],a[56:59]
SA m0,s69,15360
SA s41,s68,122880
M a[60:63],v[92:95],v[60:63],a[60:63]
B v100,s[56:59],s41 offen lds
s_lshl_b32 s42,s55,17
)"
R"(s_lshl_b32 s43,s52,13
SA s42,s42,s43
s_lshr_b32 s43,%17,1
s_lshl_b32 s43,s43,16
SA s42,s42,s43
s_and_b32 s43,%17,1
s_lshl_b32 s43,s43,11
SA s42,s42,s43
)"
R"(s_mov_b64 s[44:45],%16
SA s44,s44,s42
s_addc_u32 s45,s45,0
SA s44,s44,0x5400000
s_addc_u32 s45,s45,0
SA s46,s44,4096
s_addc_u32 s47,s45,0
SA s48,s44,32768
)"
R"(s_addc_u32 s49,s45,0
SA s50,s44,36864
s_addc_u32 s51,s45,0
s_nop 7
AR v8,a0
AR v9,a1
AR v10,a2
AR v11,a3
)"
R"(AR v12,a16
AR v13,a17
AR v14,a18
AR v15,a19
CV v8,v8,v9
CV v9,v10,v11
CV v10,v12,v13
CV v11,v14,v15
)"
R"(GS v3,v[8:11],s[44:45] offset:0 sc1
AR v108,a32
AR v109,a33
AR v110,a34
AR v111,a35
AR v112,a48
AR v113,a49
AR v114,a50
)"
R"(AR v115,a51
CV v108,v108,v109
CV v109,v110,v111
CV v110,v112,v113
CV v111,v114,v115
GS v3,v[108:111],s[44:45] offset:1024 sc1
AR v8,a4
AR v9,a5
)"
R"(AR v10,a6
AR v11,a7
AR v12,a20
AR v13,a21
AR v14,a22
AR v15,a23
CV v8,v8,v9
CV v9,v10,v11
)"
R"(CV v10,v12,v13
CV v11,v14,v15
GS v3,v[8:11],s[46:47] offset:0 sc1
AR v108,a36
AR v109,a37
AR v110,a38
AR v111,a39
AR v112,a52
)"
R"(AR v113,a53
AR v114,a54
AR v115,a55
CV v108,v108,v109
CV v109,v110,v111
CV v110,v112,v113
CV v111,v114,v115
GS v3,v[108:111],s[46:47] offset:1024 sc1
)"
R"(AR v8,a8
AR v9,a9
AR v10,a10
AR v11,a11
AR v12,a24
AR v13,a25
AR v14,a26
AR v15,a27
)"
R"(CV v8,v8,v9
CV v9,v10,v11
CV v10,v12,v13
CV v11,v14,v15
GS v3,v[8:11],s[48:49] offset:0 sc1
AR v108,a40
AR v109,a41
AR v110,a42
)"
R"(AR v111,a43
AR v112,a56
AR v113,a57
AR v114,a58
AR v115,a59
CV v108,v108,v109
CV v109,v110,v111
CV v110,v112,v113
)"
R"(CV v111,v114,v115
GS v3,v[108:111],s[48:49] offset:1024 sc1
AR v8,a12
AR v9,a13
AR v10,a14
AR v11,a15
AR v12,a28
AR v13,a29
)"
R"(AR v14,a30
AR v15,a31
CV v8,v8,v9
CV v9,v10,v11
CV v10,v12,v13
CV v11,v14,v15
GS v3,v[8:11],s[50:51] offset:0 sc1
AR v108,a44
)"
R"(AR v109,a45
AR v110,a46
AR v111,a47
AR v112,a60
AR v113,a61
AR v114,a62
AR v115,a63
CV v108,v108,v109
)"
R"(CV v109,v110,v111
CV v110,v112,v113
CV v111,v114,v115
GS v3,v[108:111],s[50:51] offset:1024 sc1
W vmcnt(8)
T v[32:33],v96 offset:0
T v[34:35],v96 offset:2048
T v[36:37],v97 offset:0
)"
R"(T v[38:39],v97 offset:2048
T v[40:41],v98 offset:0
T v[42:43],v98 offset:2048
T v[44:45],v99 offset:0
T v[46:47],v99 offset:2048
T v[48:49],v96 offset:4096
T v[50:51],v96 offset:6144
T v[52:53],v97 offset:4096
)"
R"(T v[54:55],v97 offset:6144
T v[56:57],v98 offset:4096
T v[58:59],v98 offset:6144
T v[60:61],v99 offset:4096
T v[62:63],v99 offset:6144
T v[64:65],v96 offset:8192
T v[66:67],v96 offset:10240
T v[68:69],v97 offset:8192
)"
R"(T v[70:71],v97 offset:10240
T v[72:73],v98 offset:8192
T v[74:75],v98 offset:10240
T v[76:77],v99 offset:8192
T v[78:79],v99 offset:10240
T v[80:81],v96 offset:12288
T v[82:83],v96 offset:14336
T v[84:85],v97 offset:12288
)"
R"(T v[86:87],v97 offset:14336
T v[88:89],v98 offset:12288
T v[90:91],v98 offset:14336
T v[92:93],v99 offset:12288
T v[94:95],v99 offset:14336
W lgkmcnt(15)
M a[64:67],v[32:35],v[128:131],0
M a[68:71],v[32:35],v[132:135],0
)"
R"(M a[72:75],v[32:35],v[136:139],0
M a[76:79],v[32:35],v[140:143],0
M a[80:83],v[36:39],v[128:131],0
M a[84:87],v[36:39],v[132:135],0
M a[88:91],v[36:39],v[136:139],0
M a[92:95],v[36:39],v[140:143],0
M a[96:99],v[40:43],v[128:131],0
M a[100:103],v[40:43],v[132:135],0
)"
R"(M a[104:107],v[40:43],v[136:139],0
M a[108:111],v[40:43],v[140:143],0
M a[112:115],v[44:47],v[128:131],0
M a[116:119],v[44:47],v[132:135],0
M a[120:123],v[44:47],v[136:139],0
M a[124:127],v[44:47],v[140:143],0
W lgkmcnt(15)
M a[64:67],v[48:51],v[144:147],a[64:67]
)"
R"(M a[68:71],v[48:51],v[148:151],a[68:71]
M a[72:75],v[48:51],v[152:155],a[72:75]
M a[76:79],v[48:51],v[156:159],a[76:79]
M a[80:83],v[52:55],v[144:147],a[80:83]
M a[84:87],v[52:55],v[148:151],a[84:87]
M a[88:91],v[52:55],v[152:155],a[88:91]
M a[92:95],v[52:55],v[156:159],a[92:95]
M a[96:99],v[56:59],v[144:147],a[96:99]
)"
R"(M a[100:103],v[56:59],v[148:151],a[100:103]
M a[104:107],v[56:59],v[152:155],a[104:107]
M a[108:111],v[56:59],v[156:159],a[108:111]
M a[112:115],v[60:63],v[144:147],a[112:115]
M a[116:119],v[60:63],v[148:151],a[116:119]
M a[120:123],v[60:63],v[152:155],a[120:123]
M a[124:127],v[60:63],v[156:159],a[124:127]
W lgkmcnt(8)
)"
R"(M a[64:67],v[64:67],v[160:163],a[64:67]
M a[68:71],v[64:67],v[164:167],a[68:71]
M a[72:75],v[64:67],v[168:171],a[72:75]
M a[76:79],v[64:67],v[172:175],a[76:79]
M a[80:83],v[68:71],v[160:163],a[80:83]
M a[84:87],v[68:71],v[164:167],a[84:87]
M a[88:91],v[68:71],v[168:171],a[88:91]
M a[92:95],v[68:71],v[172:175],a[92:95]
)"
R"(M a[96:99],v[72:75],v[160:163],a[96:99]
M a[100:103],v[72:75],v[164:167],a[100:103]
M a[104:107],v[72:75],v[168:171],a[104:107]
M a[108:111],v[72:75],v[172:175],a[108:111]
M a[112:115],v[76:79],v[160:163],a[112:115]
M a[116:119],v[76:79],v[164:167],a[116:119]
M a[120:123],v[76:79],v[168:171],a[120:123]
M a[124:127],v[76:79],v[172:175],a[124:127]
)"
R"(W lgkmcnt(0)
M a[64:67],v[80:83],v[176:179],a[64:67]
M a[68:71],v[80:83],v[180:183],a[68:71]
M a[72:75],v[80:83],v[184:187],a[72:75]
M a[76:79],v[80:83],v[188:191],a[76:79]
M a[80:83],v[84:87],v[176:179],a[80:83]
M a[84:87],v[84:87],v[180:183],a[84:87]
M a[88:91],v[84:87],v[184:187],a[88:91]
)"
R"(M a[92:95],v[84:87],v[188:191],a[92:95]
M a[96:99],v[88:91],v[176:179],a[96:99]
M a[100:103],v[88:91],v[180:183],a[100:103]
M a[104:107],v[88:91],v[184:187],a[104:107]
M a[108:111],v[88:91],v[188:191],a[108:111]
M a[112:115],v[92:95],v[176:179],a[112:115]
M a[116:119],v[92:95],v[180:183],a[116:119]
M a[120:123],v[92:95],v[184:187],a[120:123]
)"
R"(M a[124:127],v[92:95],v[188:191],a[124:127]
v_xor_b32 v16,0,v101
v_xor_b32 v17,64,v101
v_xor_b32 v18,128,v101
v_xor_b32 v19,192,v101
s_nop 7
W lgkmcnt(0)
s_barrier
)"
R"(ds_write_b128 v16,a[64:67] offset:0
ds_write_b128 v16,a[68:71] offset:4096
ds_write_b128 v16,a[72:75] offset:8192
ds_write_b128 v16,a[76:79] offset:12288
ds_write_b128 v17,a[80:83] offset:0
ds_write_b128 v17,a[84:87] offset:4096
ds_write_b128 v17,a[88:91] offset:8192
ds_write_b128 v17,a[92:95] offset:12288
)"
R"(ds_write_b128 v18,a[96:99] offset:0
ds_write_b128 v18,a[100:103] offset:4096
ds_write_b128 v18,a[104:107] offset:8192
ds_write_b128 v18,a[108:111] offset:12288
ds_write_b128 v19,a[112:115] offset:0
ds_write_b128 v19,a[116:119] offset:4096
ds_write_b128 v19,a[120:123] offset:8192
ds_write_b128 v19,a[124:127] offset:12288
)"
R"(v_and_b32 v24,3,v20
v_lshlrev_b32 v24,6,v24
s_lshl_b32 s42,s55,8
SA s42,s42,53248
v_add_u32 v24,s42,v24
W vmcnt(0) lgkmcnt(0)
s_barrier
s_cmp_lg_u32 %17,0
)"
R"(s_cbranch_scc1 NP31_%=
s_lshl_b32 s43,s55,2
SA s43,s43,s52
s_lshl_b32 s92,s43,6
s_mov_b64 s[90:91],%16
SA s90,s90,s92
s_addc_u32 s91,s91,0
SA s90,s90,0x500d000
)"
R"(s_addc_u32 s91,s91,0
v_mov_b32 v28,s88
v_mov_b32 v29,0
s_mov_b64 exec,1
s_nop 1
global_store_dword v29,v28,s[90:91] sc1
s_mov_b64 exec,-1
s_nop 1
)"
R"(NP31_%=:
R v[32:35],v102 offset:0
R v[36:39],v103 offset:0
R v[40:43],v102 offset:16384
R v[44:47],v103 offset:16384
R v[48:51],v102 offset:32768
R v[52:55],v103 offset:32768
R v[56:59],v102 offset:49152
)"
R"(R v[60:63],v103 offset:49152
R v[64:67],v102 offset:8192
R v[68:71],v103 offset:8192
R v[72:75],v102 offset:24576
R v[76:79],v103 offset:24576
R v[80:83],v102 offset:40960
R v[84:87],v103 offset:40960
R v[88:91],v102 offset:57344
)"
R"(R v[92:95],v103 offset:57344
W lgkmcnt(8)
AD v32,v32,v40
AD v33,v33,v41
AD v34,v34,v42
AD v35,v35,v43
AD v36,v36,v44
AD v37,v37,v45
)"
R"(AD v38,v38,v46
AD v39,v39,v47
AD v32,v32,v48
AD v33,v33,v49
AD v34,v34,v50
AD v35,v35,v51
AD v36,v36,v52
AD v37,v37,v53
)"
R"(AD v38,v38,v54
AD v39,v39,v55
AD v32,v32,v56
AD v33,v33,v57
AD v34,v34,v58
AD v35,v35,v59
AD v36,v36,v60
AD v37,v37,v61
)"
R"(AD v38,v38,v62
AD v39,v39,v63
CV v32,v32,v33
CV v33,v34,v35
CV v34,v36,v37
CV v35,v38,v39
GS v104,v[32:35],%23 offset:1024
W lgkmcnt(0)
)"
R"(AD v64,v64,v72
AD v65,v65,v73
AD v66,v66,v74
AD v67,v67,v75
AD v68,v68,v76
AD v69,v69,v77
AD v70,v70,v78
AD v71,v71,v79
)"
R"(AD v64,v64,v80
AD v65,v65,v81
AD v66,v66,v82
AD v67,v67,v83
AD v68,v68,v84
AD v69,v69,v85
AD v70,v70,v86
AD v71,v71,v87
)"
R"(AD v64,v64,v88
AD v65,v65,v89
AD v66,v66,v90
AD v67,v67,v91
AD v68,v68,v92
AD v69,v69,v93
AD v70,v70,v94
AD v71,v71,v95
)"
R"(CV v64,v64,v65
CV v65,v66,v67
CV v66,v68,v69
CV v67,v70,v71
GS v105,v[64:67],%23 offset:1024
s_lshl_b32 s42,s55,17
s_lshl_b32 s43,s52,15
SA s42,s42,s43
)"
R"(s_lshl_b32 s43,%17,11
SA s42,s42,s43
s_mov_b64 s[44:45],%16
SA s44,s44,s42
s_addc_u32 s45,s45,0
SA s44,s44,0x5400000
s_addc_u32 s45,s45,0
s_lshl_b32 s42,s54,7
)"
R"(s_lshl_b32 s43,s52,5
SA s42,s42,s43
s_lshr_b32 s43,%17,1
s_lshl_b32 s43,s43,4
SA s42,s42,s43
s_lshl_b32 s42,s42,11
s_lshl_b32 s43,s53,8
SA s42,s42,s43
)"
R"(s_and_b32 s43,%17,1
s_lshl_b32 s43,s43,7
SA s42,s42,s43
v_lshlrev_b32 v26,11,v21
v_lshl_add_u32 v26,v22,3,v26
v_add_u32 v26,s42,v26
s_mov_b64 s[90:91],%16
SA s90,s90,0x5000000
)"
R"(s_addc_u32 s91,s91,0
SM s89,60000
s_mov_b64 exec,-1
s_nop 1
PL32_%=:
global_load_dword v25,v24,s[90:91] sc1
W vmcnt(0)
v_cmp_ne_u32 vcc,s88,v25
)"
R"(s_sub_u32 s89,s89,1
s_cbranch_scc1 PD33_%=
s_cbranch_vccz PD33_%=
s_sleep 1
s_branch PL32_%=
PD33_%=:
s_mov_b64 exec,-1
s_nop 1
)"
R"(GL v[96:99],v3,s[44:45] offset:0 sc1
GL v[100:103],v3,s[44:45] offset:1024 sc1
SA s44,s44,8192
s_addc_u32 s45,s45,0
GL v[104:107],v3,s[44:45] offset:0 sc1
GL v[108:111],v3,s[44:45] offset:1024 sc1
SA s44,s44,8192
s_addc_u32 s45,s45,0
)"
R"(GL v[112:115],v3,s[44:45] offset:0 sc1
GL v[116:119],v3,s[44:45] offset:1024 sc1
SA s44,s44,8192
s_addc_u32 s45,s45,0
GL v[120:123],v3,s[44:45] offset:0 sc1
GL v[124:127],v3,s[44:45] offset:1024 sc1
v_mov_b32 v27,0xffff0000
W vmcnt(6)
)"
R"(v_lshlrev_b32 v128,16,v96
v_and_b32 v129,v27,v96
v_lshlrev_b32 v130,16,v97
v_and_b32 v131,v27,v97
v_lshlrev_b32 v132,16,v98
v_and_b32 v133,v27,v98
v_lshlrev_b32 v134,16,v99
v_and_b32 v135,v27,v99
)"
R"(v_lshlrev_b32 v136,16,v100
v_and_b32 v137,v27,v100
v_lshlrev_b32 v138,16,v101
v_and_b32 v139,v27,v101
v_lshlrev_b32 v140,16,v102
v_and_b32 v141,v27,v102
v_lshlrev_b32 v142,16,v103
v_and_b32 v143,v27,v103
)"
R"(W vmcnt(4)
v_lshlrev_b32 v28,16,v104
v_and_b32 v29,v27,v104
AD v128,v128,v28
AD v129,v129,v29
v_lshlrev_b32 v28,16,v105
v_and_b32 v29,v27,v105
AD v130,v130,v28
)"
R"(AD v131,v131,v29
v_lshlrev_b32 v28,16,v106
v_and_b32 v29,v27,v106
AD v132,v132,v28
AD v133,v133,v29
v_lshlrev_b32 v28,16,v107
v_and_b32 v29,v27,v107
AD v134,v134,v28
)"
R"(AD v135,v135,v29
v_lshlrev_b32 v28,16,v108
v_and_b32 v29,v27,v108
AD v136,v136,v28
AD v137,v137,v29
v_lshlrev_b32 v28,16,v109
v_and_b32 v29,v27,v109
AD v138,v138,v28
)"
R"(AD v139,v139,v29
v_lshlrev_b32 v28,16,v110
v_and_b32 v29,v27,v110
AD v140,v140,v28
AD v141,v141,v29
v_lshlrev_b32 v28,16,v111
v_and_b32 v29,v27,v111
AD v142,v142,v28
)"
R"(AD v143,v143,v29
W vmcnt(2)
v_lshlrev_b32 v28,16,v112
v_and_b32 v29,v27,v112
AD v128,v128,v28
AD v129,v129,v29
v_lshlrev_b32 v28,16,v113
v_and_b32 v29,v27,v113
)"
R"(AD v130,v130,v28
AD v131,v131,v29
v_lshlrev_b32 v28,16,v114
v_and_b32 v29,v27,v114
AD v132,v132,v28
AD v133,v133,v29
v_lshlrev_b32 v28,16,v115
v_and_b32 v29,v27,v115
)"
R"(AD v134,v134,v28
AD v135,v135,v29
v_lshlrev_b32 v28,16,v116
v_and_b32 v29,v27,v116
AD v136,v136,v28
AD v137,v137,v29
v_lshlrev_b32 v28,16,v117
v_and_b32 v29,v27,v117
)"
R"(AD v138,v138,v28
AD v139,v139,v29
v_lshlrev_b32 v28,16,v118
v_and_b32 v29,v27,v118
AD v140,v140,v28
AD v141,v141,v29
v_lshlrev_b32 v28,16,v119
v_and_b32 v29,v27,v119
)"
R"(AD v142,v142,v28
AD v143,v143,v29
W vmcnt(0)
v_lshlrev_b32 v28,16,v120
v_and_b32 v29,v27,v120
AD v128,v128,v28
AD v129,v129,v29
v_lshlrev_b32 v28,16,v121
)"
R"(v_and_b32 v29,v27,v121
AD v130,v130,v28
AD v131,v131,v29
v_lshlrev_b32 v28,16,v122
v_and_b32 v29,v27,v122
AD v132,v132,v28
AD v133,v133,v29
v_lshlrev_b32 v28,16,v123
)"
R"(v_and_b32 v29,v27,v123
AD v134,v134,v28
AD v135,v135,v29
v_lshlrev_b32 v28,16,v124
v_and_b32 v29,v27,v124
AD v136,v136,v28
AD v137,v137,v29
v_lshlrev_b32 v28,16,v125
)"
R"(v_and_b32 v29,v27,v125
AD v138,v138,v28
AD v139,v139,v29
v_lshlrev_b32 v28,16,v126
v_and_b32 v29,v27,v126
AD v140,v140,v28
AD v141,v141,v29
v_lshlrev_b32 v28,16,v127
)"
R"(v_and_b32 v29,v27,v127
AD v142,v142,v28
AD v143,v143,v29
CV v128,v128,v129
CV v129,v130,v131
global_store_dwordx2 v26,v[128:129],%23 offset:0
CV v132,v132,v133
CV v133,v134,v135
)"
R"(global_store_dwordx2 v26,v[132:133],%23 offset:32
CV v136,v136,v137
CV v137,v138,v139
global_store_dwordx2 v26,v[136:137],%23 offset:64
CV v140,v140,v141
CV v141,v142,v143
global_store_dwordx2 v26,v[140:141],%23 offset:96
s_or_b32 s42,s95,%17
)"
R"(s_cmp_lg_u32 s42,0
s_cbranch_scc1 NE34_%=
s_mov_b64 s[90:91],%16
SA s90,s90,0x5000000
s_addc_u32 s91,s91,0
v_mov_b32 v8,s88
v_mov_b32 v9,0
s_mov_b64 exec,1
)"
R"(s_nop 1
global_store_dword v9,v8,s[90:91] sc1
s_mov_b64 exec,-1
NE34_%=:
s_branch END_%=
TR9_%=:
s_swappc_b64 s[56:57],s[68:69]
s_branch BK9_%=
)"
R"(TR10_%=:
s_swappc_b64 s[56:57],s[70:71]
s_branch BK10_%=
TR11_%=:
s_swappc_b64 s[56:57],s[72:73]
s_branch BK11_%=
TR12_%=:
s_swappc_b64 s[56:57],s[74:75]
)"
R"(s_branch BK12_%=
TR13_%=:
s_swappc_b64 s[56:57],s[68:69]
s_branch BK13_%=
TR14_%=:
s_swappc_b64 s[56:57],s[70:71]
s_branch BK14_%=
TR15_%=:
)"
R"(s_swappc_b64 s[56:57],s[72:73]
s_branch BK15_%=
TR16_%=:
s_swappc_b64 s[56:57],s[74:75]
s_branch BK16_%=
TR17_%=:
s_swappc_b64 s[56:57],s[68:69]
s_branch BK17_%=
)"
R"(TR18_%=:
s_swappc_b64 s[56:57],s[70:71]
s_branch BK18_%=
TR19_%=:
s_swappc_b64 s[56:57],s[72:73]
s_branch BK19_%=
TR20_%=:
s_swappc_b64 s[56:57],s[68:69]
)"
R"(s_branch BK20_%=
TR21_%=:
s_swappc_b64 s[56:57],s[70:71]
s_branch BK21_%=
TR22_%=:
s_swappc_b64 s[56:57],s[72:73]
s_branch BK22_%=
TR23_%=:
)"
R"(s_swappc_b64 s[56:57],s[74:75]
s_branch BK23_%=
TR24_%=:
s_swappc_b64 s[56:57],s[72:73]
s_branch BK24_%=
TR25_%=:
s_swappc_b64 s[56:57],s[74:75]
s_branch BK25_%=
)"
R"(TR26_%=:
s_swappc_b64 s[56:57],s[72:73]
s_branch BK26_%=
TR27_%=:
s_swappc_b64 s[56:57],s[74:75]
s_branch BK27_%=
IN0_%=:
v_mov_b32 v15,v12
)"
R"(s_nop 1
v_permlane32_swap_b32 v12,v15
v_max_f32 v12,v12,v15
v_mov_b32 v15,v12
s_nop 1
v_permlane16_swap_b32 v12,v15
v_max_f32 v12,v12,v15
AD v12,0x42ac0000,v12
)"
R"(v_mov_b32 v20,v12
v_sub_f32 v24,0,v12
v_sub_f32 v25,0,v12
v_sub_f32 v26,0,v12
v_sub_f32 v27,0,v12
v_sub_f32 v40,v40,v12
v_sub_f32 v41,v41,v12
v_sub_f32 v42,v42,v12
)"
R"(v_sub_f32 v43,v43,v12
v_sub_f32 v44,v44,v12
v_sub_f32 v45,v45,v12
v_sub_f32 v46,v46,v12
v_sub_f32 v47,v47,v12
v_sub_f32 v48,v48,v12
v_sub_f32 v49,v49,v12
v_sub_f32 v50,v50,v12
)"
R"(v_sub_f32 v51,v51,v12
v_sub_f32 v52,v52,v12
v_sub_f32 v53,v53,v12
v_sub_f32 v54,v54,v12
v_sub_f32 v55,v55,v12
SM s48,0xc2180000
SA s68,s68,SL0_%=-IN0_%=
s_addc_u32 s69,s69,0
)"
R"(s_nop 1
s_setpc_b64 s[56:57]
SL0_%=:
s_nop 7
s_nop 3
v_mov_b32 v15,v12
s_nop 1
v_permlane32_swap_b32 v12,v15
)"
R"(v_max_f32 v12,v12,v15
v_mov_b32 v15,v12
s_nop 1
v_permlane16_swap_b32 v12,v15
v_max_f32 v12,v12,v15
AD v12,0x42ac0000,v12
v_max_f32 v12,0,v12
v_exp_f32_e64 v14,-v12
)"
R"(AD v20,v20,v12
v_sub_f32 v24,0,v20
v_sub_f32 v25,0,v20
v_sub_f32 v26,0,v20
v_sub_f32 v27,0,v20
v_sub_f32 v40,v40,v12
v_sub_f32 v41,v41,v12
v_sub_f32 v42,v42,v12
)"
R"(v_sub_f32 v43,v43,v12
v_sub_f32 v44,v44,v12
v_sub_f32 v45,v45,v12
v_sub_f32 v46,v46,v12
v_sub_f32 v47,v47,v12
v_sub_f32 v48,v48,v12
v_sub_f32 v49,v49,v12
v_sub_f32 v50,v50,v12
)"
R"(v_sub_f32 v51,v51,v12
v_sub_f32 v52,v52,v12
v_sub_f32 v53,v53,v12
v_sub_f32 v54,v54,v12
v_sub_f32 v55,v55,v12
MU v208,v208,v14
MU v209,v209,v14
MU v210,v210,v14
)"
R"(MU v211,v211,v14
AR v184,a0
AR v185,a1
AR v186,a2
AR v187,a3
MU v184,v184,v14
MU v185,v185,v14
MU v186,v186,v14
)"
R"(MU v187,v187,v14
AW a0,v184
AW a1,v185
AW a2,v186
AW a3,v187
AR v184,a4
AR v185,a5
AR v186,a6
)"
R"(AR v187,a7
MU v184,v184,v14
MU v185,v185,v14
MU v186,v186,v14
MU v187,v187,v14
AW a4,v184
AW a5,v185
AW a6,v186
)"
R"(AW a7,v187
AR v184,a8
AR v185,a9
AR v186,a10
AR v187,a11
MU v184,v184,v14
MU v185,v185,v14
MU v186,v186,v14
)"
R"(MU v187,v187,v14
AW a8,v184
AW a9,v185
AW a10,v186
AW a11,v187
AR v184,a12
AR v185,a13
AR v186,a14
)"
R"(AR v187,a15
MU v184,v184,v14
MU v185,v185,v14
MU v186,v186,v14
MU v187,v187,v14
AW a12,v184
AW a13,v185
AW a14,v186
)"
R"(AW a15,v187
AR v184,a16
AR v185,a17
AR v186,a18
AR v187,a19
MU v184,v184,v14
MU v185,v185,v14
MU v186,v186,v14
)"
R"(MU v187,v187,v14
AW a16,v184
AW a17,v185
AW a18,v186
AW a19,v187
AR v184,a20
AR v185,a21
AR v186,a22
)"
R"(AR v187,a23
MU v184,v184,v14
MU v185,v185,v14
MU v186,v186,v14
MU v187,v187,v14
AW a20,v184
AW a21,v185
AW a22,v186
)"
R"(AW a23,v187
AR v184,a24
AR v185,a25
AR v186,a26
AR v187,a27
MU v184,v184,v14
MU v185,v185,v14
MU v186,v186,v14
)"
R"(MU v187,v187,v14
AW a24,v184
AW a25,v185
AW a26,v186
AW a27,v187
AR v184,a28
AR v185,a29
AR v186,a30
)"
R"(AR v187,a31
MU v184,v184,v14
MU v185,v185,v14
MU v186,v186,v14
MU v187,v187,v14
AW a28,v184
AW a29,v185
AW a30,v186
)"
R"(AW a31,v187
s_nop 1
s_setpc_b64 s[56:57]
IN1_%=:
v_mov_b32 v15,v12
s_nop 1
v_permlane32_swap_b32 v12,v15
v_max_f32 v12,v12,v15
)"
R"(v_mov_b32 v15,v12
s_nop 1
v_permlane16_swap_b32 v12,v15
v_max_f32 v12,v12,v15
AD v12,0x42ac0000,v12
v_mov_b32 v21,v12
v_sub_f32 v28,0,v12
v_sub_f32 v29,0,v12
)"
R"(v_sub_f32 v30,0,v12
v_sub_f32 v31,0,v12
v_sub_f32 v56,v56,v12
v_sub_f32 v57,v57,v12
v_sub_f32 v58,v58,v12
v_sub_f32 v59,v59,v12
v_sub_f32 v60,v60,v12
v_sub_f32 v61,v61,v12
)"
R"(v_sub_f32 v62,v62,v12
v_sub_f32 v63,v63,v12
v_sub_f32 v64,v64,v12
v_sub_f32 v65,v65,v12
v_sub_f32 v66,v66,v12
v_sub_f32 v67,v67,v12
v_sub_f32 v68,v68,v12
v_sub_f32 v69,v69,v12
)"
R"(v_sub_f32 v70,v70,v12
v_sub_f32 v71,v71,v12
SM s49,0xc2180000
SA s70,s70,SL1_%=-IN1_%=
s_addc_u32 s71,s71,0
s_nop 1
s_setpc_b64 s[56:57]
SL1_%=:
)"
R"(s_nop 7
s_nop 3
v_mov_b32 v15,v12
s_nop 1
v_permlane32_swap_b32 v12,v15
v_max_f32 v12,v12,v15
v_mov_b32 v15,v12
s_nop 1
)"
R"(v_permlane16_swap_b32 v12,v15
v_max_f32 v12,v12,v15
AD v12,0x42ac0000,v12
v_max_f32 v12,0,v12
v_exp_f32_e64 v14,-v12
AD v21,v21,v12
v_sub_f32 v28,0,v21
v_sub_f32 v29,0,v21
)"
R"(v_sub_f32 v30,0,v21
v_sub_f32 v31,0,v21
v_sub_f32 v56,v56,v12
v_sub_f32 v57,v57,v12
v_sub_f32 v58,v58,v12
v_sub_f32 v59,v59,v12
v_sub_f32 v60,v60,v12
v_sub_f32 v61,v61,v12
)"
R"(v_sub_f32 v62,v62,v12
v_sub_f32 v63,v63,v12
v_sub_f32 v64,v64,v12
v_sub_f32 v65,v65,v12
v_sub_f32 v66,v66,v12
v_sub_f32 v67,v67,v12
v_sub_f32 v68,v68,v12
v_sub_f32 v69,v69,v12
)"
R"(v_sub_f32 v70,v70,v12
v_sub_f32 v71,v71,v12
MU v212,v212,v14
MU v213,v213,v14
MU v214,v214,v14
MU v215,v215,v14
AR v184,a32
AR v185,a33
)"
R"(AR v186,a34
AR v187,a35
MU v184,v184,v14
MU v185,v185,v14
MU v186,v186,v14
MU v187,v187,v14
AW a32,v184
AW a33,v185
)"
R"(AW a34,v186
AW a35,v187
AR v184,a36
AR v185,a37
AR v186,a38
AR v187,a39
MU v184,v184,v14
MU v185,v185,v14
)"
R"(MU v186,v186,v14
MU v187,v187,v14
AW a36,v184
AW a37,v185
AW a38,v186
AW a39,v187
AR v184,a40
AR v185,a41
)"
R"(AR v186,a42
AR v187,a43
MU v184,v184,v14
MU v185,v185,v14
MU v186,v186,v14
MU v187,v187,v14
AW a40,v184
AW a41,v185
)"
R"(AW a42,v186
AW a43,v187
AR v184,a44
AR v185,a45
AR v186,a46
AR v187,a47
MU v184,v184,v14
MU v185,v185,v14
)"
R"(MU v186,v186,v14
MU v187,v187,v14
AW a44,v184
AW a45,v185
AW a46,v186
AW a47,v187
AR v184,a48
AR v185,a49
)"
R"(AR v186,a50
AR v187,a51
MU v184,v184,v14
MU v185,v185,v14
MU v186,v186,v14
MU v187,v187,v14
AW a48,v184
AW a49,v185
)"
R"(AW a50,v186
AW a51,v187
AR v184,a52
AR v185,a53
AR v186,a54
AR v187,a55
MU v184,v184,v14
MU v185,v185,v14
)"
R"(MU v186,v186,v14
MU v187,v187,v14
AW a52,v184
AW a53,v185
AW a54,v186
AW a55,v187
AR v184,a56
AR v185,a57
)"
R"(AR v186,a58
AR v187,a59
MU v184,v184,v14
MU v185,v185,v14
MU v186,v186,v14
MU v187,v187,v14
AW a56,v184
AW a57,v185
)"
R"(AW a58,v186
AW a59,v187
AR v184,a60
AR v185,a61
AR v186,a62
AR v187,a63
MU v184,v184,v14
MU v185,v185,v14
)"
R"(MU v186,v186,v14
MU v187,v187,v14
AW a60,v184
AW a61,v185
AW a62,v186
AW a63,v187
s_nop 1
s_setpc_b64 s[56:57]
)"
R"(IN2_%=:
v_mov_b32 v15,v12
s_nop 1
v_permlane32_swap_b32 v12,v15
v_max_f32 v12,v12,v15
v_mov_b32 v15,v12
s_nop 1
v_permlane16_swap_b32 v12,v15
)"
R"(v_max_f32 v12,v12,v15
AD v12,0x42ac0000,v12
v_mov_b32 v22,v12
v_sub_f32 v32,0,v12
v_sub_f32 v33,0,v12
v_sub_f32 v34,0,v12
v_sub_f32 v35,0,v12
v_sub_f32 v40,v40,v12
)"
R"(v_sub_f32 v41,v41,v12
v_sub_f32 v42,v42,v12
v_sub_f32 v43,v43,v12
v_sub_f32 v44,v44,v12
v_sub_f32 v45,v45,v12
v_sub_f32 v46,v46,v12
v_sub_f32 v47,v47,v12
v_sub_f32 v48,v48,v12
)"
R"(v_sub_f32 v49,v49,v12
v_sub_f32 v50,v50,v12
v_sub_f32 v51,v51,v12
v_sub_f32 v52,v52,v12
v_sub_f32 v53,v53,v12
v_sub_f32 v54,v54,v12
v_sub_f32 v55,v55,v12
SM s50,0xc2180000
)"
R"(SA s72,s72,SL2_%=-IN2_%=
s_addc_u32 s73,s73,0
s_nop 1
s_setpc_b64 s[56:57]
SL2_%=:
s_nop 7
s_nop 3
v_mov_b32 v15,v12
)"
R"(s_nop 1
v_permlane32_swap_b32 v12,v15
v_max_f32 v12,v12,v15
v_mov_b32 v15,v12
s_nop 1
v_permlane16_swap_b32 v12,v15
v_max_f32 v12,v12,v15
AD v12,0x42ac0000,v12
)"
R"(v_max_f32 v12,0,v12
v_exp_f32_e64 v14,-v12
AD v22,v22,v12
v_sub_f32 v32,0,v22
v_sub_f32 v33,0,v22
v_sub_f32 v34,0,v22
v_sub_f32 v35,0,v22
v_sub_f32 v40,v40,v12
)"
R"(v_sub_f32 v41,v41,v12
v_sub_f32 v42,v42,v12
v_sub_f32 v43,v43,v12
v_sub_f32 v44,v44,v12
v_sub_f32 v45,v45,v12
v_sub_f32 v46,v46,v12
v_sub_f32 v47,v47,v12
v_sub_f32 v48,v48,v12
)"
R"(v_sub_f32 v49,v49,v12
v_sub_f32 v50,v50,v12
v_sub_f32 v51,v51,v12
v_sub_f32 v52,v52,v12
v_sub_f32 v53,v53,v12
v_sub_f32 v54,v54,v12
v_sub_f32 v55,v55,v12
MU v216,v216,v14
)"
R"(MU v217,v217,v14
MU v218,v218,v14
MU v219,v219,v14
AR v184,a64
AR v185,a65
AR v186,a66
AR v187,a67
MU v184,v184,v14
)"
R"(MU v185,v185,v14
MU v186,v186,v14
MU v187,v187,v14
AW a64,v184
AW a65,v185
AW a66,v186
AW a67,v187
AR v184,a68
)"
R"(AR v185,a69
AR v186,a70
AR v187,a71
MU v184,v184,v14
MU v185,v185,v14
MU v186,v186,v14
MU v187,v187,v14
AW a68,v184
)"
R"(AW a69,v185
AW a70,v186
AW a71,v187
AR v184,a72
AR v185,a73
AR v186,a74
AR v187,a75
MU v184,v184,v14
)"
R"(MU v185,v185,v14
MU v186,v186,v14
MU v187,v187,v14
AW a72,v184
AW a73,v185
AW a74,v186
AW a75,v187
AR v184,a76
)"
R"(AR v185,a77
AR v186,a78
AR v187,a79
MU v184,v184,v14
MU v185,v185,v14
MU v186,v186,v14
MU v187,v187,v14
AW a76,v184
)"
R"(AW a77,v185
AW a78,v186
AW a79,v187
AR v184,a80
AR v185,a81
AR v186,a82
AR v187,a83
MU v184,v184,v14
)"
R"(MU v185,v185,v14
MU v186,v186,v14
MU v187,v187,v14
AW a80,v184
AW a81,v185
AW a82,v186
AW a83,v187
AR v184,a84
)"
R"(AR v185,a85
AR v186,a86
AR v187,a87
MU v184,v184,v14
MU v185,v185,v14
MU v186,v186,v14
MU v187,v187,v14
AW a84,v184
)"
R"(AW a85,v185
AW a86,v186
AW a87,v187
AR v184,a88
AR v185,a89
AR v186,a90
AR v187,a91
MU v184,v184,v14
)"
R"(MU v185,v185,v14
MU v186,v186,v14
MU v187,v187,v14
AW a88,v184
AW a89,v185
AW a90,v186
AW a91,v187
AR v184,a92
)"
R"(AR v185,a93
AR v186,a94
AR v187,a95
MU v184,v184,v14
MU v185,v185,v14
MU v186,v186,v14
MU v187,v187,v14
AW a92,v184
)"
R"(AW a93,v185
AW a94,v186
AW a95,v187
s_nop 1
s_setpc_b64 s[56:57]
IN3_%=:
v_mov_b32 v15,v12
s_nop 1
)"
R"(v_permlane32_swap_b32 v12,v15
v_max_f32 v12,v12,v15
v_mov_b32 v15,v12
s_nop 1
v_permlane16_swap_b32 v12,v15
v_max_f32 v12,v12,v15
AD v12,0x42ac0000,v12
v_mov_b32 v23,v12
)"
R"(v_sub_f32 v36,0,v12
v_sub_f32 v37,0,v12
v_sub_f32 v38,0,v12
v_sub_f32 v39,0,v12
v_sub_f32 v56,v56,v12
v_sub_f32 v57,v57,v12
v_sub_f32 v58,v58,v12
v_sub_f32 v59,v59,v12
)"
R"(v_sub_f32 v60,v60,v12
v_sub_f32 v61,v61,v12
v_sub_f32 v62,v62,v12
v_sub_f32 v63,v63,v12
v_sub_f32 v64,v64,v12
v_sub_f32 v65,v65,v12
v_sub_f32 v66,v66,v12
v_sub_f32 v67,v67,v12
)"
R"(v_sub_f32 v68,v68,v12
v_sub_f32 v69,v69,v12
v_sub_f32 v70,v70,v12
v_sub_f32 v71,v71,v12
SM s51,0xc2180000
SA s74,s74,SL3_%=-IN3_%=
s_addc_u32 s75,s75,0
s_nop 1
)"
R"(s_setpc_b64 s[56:57]
SL3_%=:
s_nop 7
s_nop 3
v_mov_b32 v15,v12
s_nop 1
v_permlane32_swap_b32 v12,v15
v_max_f32 v12,v12,v15
)"
R"(v_mov_b32 v15,v12
s_nop 1
v_permlane16_swap_b32 v12,v15
v_max_f32 v12,v12,v15
AD v12,0x42ac0000,v12
v_max_f32 v12,0,v12
v_exp_f32_e64 v14,-v12
AD v23,v23,v12
)"
R"(v_sub_f32 v36,0,v23
v_sub_f32 v37,0,v23
v_sub_f32 v38,0,v23
v_sub_f32 v39,0,v23
v_sub_f32 v56,v56,v12
v_sub_f32 v57,v57,v12
v_sub_f32 v58,v58,v12
v_sub_f32 v59,v59,v12
)"
R"(v_sub_f32 v60,v60,v12
v_sub_f32 v61,v61,v12
v_sub_f32 v62,v62,v12
v_sub_f32 v63,v63,v12
v_sub_f32 v64,v64,v12
v_sub_f32 v65,v65,v12
v_sub_f32 v66,v66,v12
v_sub_f32 v67,v67,v12
)"
R"(v_sub_f32 v68,v68,v12
v_sub_f32 v69,v69,v12
v_sub_f32 v70,v70,v12
v_sub_f32 v71,v71,v12
MU v220,v220,v14
MU v221,v221,v14
MU v222,v222,v14
MU v223,v223,v14
)"
R"(AR v184,a96
AR v185,a97
AR v186,a98
AR v187,a99
MU v184,v184,v14
MU v185,v185,v14
MU v186,v186,v14
MU v187,v187,v14
)"
R"(AW a96,v184
AW a97,v185
AW a98,v186
AW a99,v187
AR v184,a100
AR v185,a101
AR v186,a102
AR v187,a103
)"
R"(MU v184,v184,v14
MU v185,v185,v14
MU v186,v186,v14
MU v187,v187,v14
AW a100,v184
AW a101,v185
AW a102,v186
AW a103,v187
)"
R"(AR v184,a104
AR v185,a105
AR v186,a106
AR v187,a107
MU v184,v184,v14
MU v185,v185,v14
MU v186,v186,v14
MU v187,v187,v14
)"
R"(AW a104,v184
AW a105,v185
AW a106,v186
AW a107,v187
AR v184,a108
AR v185,a109
AR v186,a110
AR v187,a111
)"
R"(MU v184,v184,v14
MU v185,v185,v14
MU v186,v186,v14
MU v187,v187,v14
AW a108,v184
AW a109,v185
AW a110,v186
AW a111,v187
)"
R"(AR v184,a112
AR v185,a113
AR v186,a114
AR v187,a115
MU v184,v184,v14
MU v185,v185,v14
MU v186,v186,v14
MU v187,v187,v14
)"
R"(AW a112,v184
AW a113,v185
AW a114,v186
AW a115,v187
AR v184,a116
AR v185,a117
AR v186,a118
AR v187,a119
)"
R"(MU v184,v184,v14
MU v185,v185,v14
MU v186,v186,v14
MU v187,v187,v14
AW a116,v184
AW a117,v185
AW a118,v186
AW a119,v187
)"
R"(AR v184,a120
AR v185,a121
AR v186,a122
AR v187,a123
MU v184,v184,v14
MU v185,v185,v14
MU v186,v186,v14
MU v187,v187,v14
)"
R"(AW a120,v184
AW a121,v185
AW a122,v186
AW a123,v187
AR v184,a124
AR v185,a125
AR v186,a126
AR v187,a127
)"
R"(MU v184,v184,v14
MU v185,v185,v14
MU v186,v186,v14
MU v187,v187,v14
AW a124,v184
AW a125,v185
AW a126,v186
AW a127,v187
)"
R"(s_nop 1
s_setpc_b64 s[56:57]
END_%=:
.purgem M
.purgem T
.purgem R
.purgem B
.purgem AR
)"
R"(.purgem CV
.purgem W
.purgem EX
.purgem AD
.purgem MX
.purgem SA
.purgem SM
.purgem GS
)"
R"(.purgem GL
.purgem AW
.purgem MU
)"
      :
      : "v"(voffA), "v"(voffW), "v"(voffB), "v"(abase), "v"(wbase), "v"(wbaseB), "v"(lane16), "s"(P.wphi), "s"(j),
        "s"(P.c), "s"(P.bx), "s"(P.x), "s"(P.wk), "s"(P.wv), "s"(P.wq), "s"(P.wb),
        "s"(P.ws), "s"(w), "s"(ldsb), "s"(h), "s"(j), "s"(P.woa), "s"(P.wob), "s"(P.out), "s"(stp), "s"(tp[0]), "s"(tp[1])
      :
"v0", "v1", "v2", "v3", "v4", "v5", "v6", "v7", "v8", "v9", "v10", "v11", "v12", "v13", "v14", "v15", "v16", "v17", "v18", "v19", "v20", "v21", "v22", "v23", "v24", "v25", "v26", "v27", "v28", "v29", "v30", "v31", "v32", "v33", "v34", "v35", "v36", "v37", "v38", "v39", "v40", "v41", "v42", "v43", "v44", "v45", "v46", "v47", "v48", "v49", "v50", "v51", "v52", "v53", "v54", "v55", "v56", "v57", "v58", "v59", "v60", "v61", "v62", "v63", "v64", "v65", "v66", "v67", "v68", "v69", "v70", "v71", "v72", "v73", "v74", "v75", "v76", "v77", "v78", "v79", "v80", "v81", "v82", "v83", "v84", "v85", "v86", "v87", "v88", "v89", "v90", "v91", "v92", "v93", "v94", "v95", "v96", "v97", "v98", "v99", "v100", "v101", "v102", "v103", "v104", "v105", "v106", "v107", "v108", "v109", "v110", "v111", "v112", "v113", "v114", "v115", "v116", "v117", "v118", "v119", "v120", "v121", "v122", "v123", "v124", "v125", "v126", "v127", "v128", "v129", "v130", "v131", "v132", "v133", "v134", "v135", "v136", "v137", "v138", "v139", "v140", "v141", "v142", "v143", "v144", "v145", "v146", "v147", "v148", "v149", "v150", "v151", "v152", "v153", "v154", "v155", "v156", "v157", "v158", "v159", "v160", "v161", "v162", "v163", "v164", "v165", "v166", "v167", "v168", "v169", "v170", "v171", "v172", "v173", "v174", "v175", "v176", "v177", "v178", "v179", "v180", "v181", "v182", "v183", "v184", "v185", "v186", "v187", "v188", "v189", "v190", "v191", "v192", "v193", "v194", "v195", "v196", "v197", "v198", "v199", "v200", "v201", "v202", "v203", "v204", "v205", "v206", "v207", "v208", "v209", "v210", "v211", "v212", "v213", "v214", "v215", "v216", "v217", "v218", "v219", "v220", "v221", "v222", "v223", "v224", "v225", "v226", "v227", "v228", "v229", "v230", "v231", "v232", "v233", "v234", "v235", "v236", "v237", "v238", "v239", "a0", "a1", "a2", "a3", "a4", "a5", "a6", "a7", "a8", "a9", "a10", "a11", "a12", "a13", "a14", "a15", "a16", "a17", "a18", "a19", "a20", "a21", "a22", "a23", "a24", "a25", "a26", "a27", "a28", "a29", "a30", "a31", "a32", "a33", "a34", "a35", "a36", "a37", "a38", "a39", "a40", "a41", "a42", "a43", "a44", "a45", "a46", "a47", "a48", "a49", "a50", "a51", "a52", "a53", "a54", "a55", "a56", "a57", "a58", "a59", "a60", "a61", "a62", "a63", "a64", "a65", "a66", "a67", "a68", "a69", "a70", "a71", "a72", "a73", "a74", "a75", "a76", "a77", "a78", "a79", "a80", "a81", "a82", "a83", "a84", "a85", "a86", "a87", "a88", "a89", "a90", "a91", "a92", "a93", "a94", "a95", "a96", "a97", "a98", "a99", "a100", "a101", "a102", "a103", "a104", "a105", "a106", "a107", "a108", "a109", "a110", "a111", "a112", "a113", "a114", "a115", "a116", "a117", "a118", "a119", "a120", "a121", "a122", "a123", "a124", "a125", "a126", "a127", "a128", "a129", "a130", "a131", "a132", "a133", "a134", "a135", "a136", "a137", "a138", "a139", "a140", "a141", "a142", "a143", "a144", "a145", "a146", "a147", "a148", "a149", "a150", "a151", "a152", "a153", "a154", "a155", "a156", "a157", "a158", "a159", "a160", "a161", "a162", "a163", "a164", "a165", "a166", "a167", "a168", "a169", "a170", "a171", "a172", "a173", "a174", "a175", "a176", "a177", "a178", "a179", "a180", "a181", "a182", "a183", "a184", "a185", "a186", "a187", "a188", "a189", "a190", "a191", "a192", "a193", "a194", "a195", "a196", "a197", "a198", "a199", "a200", "a201", "a202", "a203", "a204", "a205", "a206", "a207", "a208", "a209", "a210", "a211", "a212", "a213", "a214", "a215", "a216", "a217", "a218", "a219", "a220", "a221", "a222", "a223", "a224", "a225", "a226", "a227", "a228", "a229", "a230", "a231", "a232", "a233", "a234", "a235", "a236", "a237", "a238", "a239", "a240", "a241", "a242", "a243", "a244", "a245", "a246", "a247", "a248", "a249", "a250", "a251", "a252", "a253", "a254", "a255", "s40", "s41", "s42", "s43", "s44", "s45", "s46", "s47", "s48", "s49", "s50", "s51", "s52", "s53", "s54", "s55", "s56", "s57", "s58", "s59", "s60", "s61", "s62", "s63", "s64", "s65", "s66", "s67", "s68", "s69", "s70", "s71", "s72", "s73", "s74", "s75", "s76", "s77", "s78", "s79", "s80", "s81", "s82", "s83", "s84", "s85", "s86", "s87", "s88", "s89", "s90", "s91", "s92", "s93", "s94", "s95", "s96", "s97", "vcc", "scc", "memory"
  );
}

extern "C" void kernel_launch(void* const* d_in, const int* in_sizes, int n_in, void* d_out, int out_size, void* d_ws, size_t ws_size, hipStream_t stream) {
  (void)in_sizes; (void)n_in; (void)out_size; (void)ws_size;
  (void)hipFuncSetAttribute((const void*)fused_kernel, hipFuncAttributeMaxDynamicSharedMemorySize, F_LDS);
  FP p;
  p.x = (const char*)d_in[0]; p.c = (const char*)d_in[1]; p.wq = (const char*)d_in[2]; p.wk = (const char*)d_in[3]; p.wv = (const char*)d_in[4];
  p.woa = (const char*)d_in[5]; p.bx = (const char*)d_in[6]; p.wb = (const char*)d_in[7]; p.wphi = (const char*)d_in[8]; p.wob = (const char*)d_in[9];
  p.ws = (char*)d_ws; p.out = (char*)d_out;
  fused_kernel<<<256, 256, F_LDS, stream>>>(p);
}
```
